```python
import torch

def _softmax(x, axis=-1):
    return torch.softmax(x, dim=axis)
def _gelu(x):
    return torch.nn.functional.gelu(x, approximate="tanh")
def _silu(x):
    return torch.nn.functional.silu(x)
def _sigmoid(x):
    return torch.sigmoid(x)
def _layernorm(x, axis=-1, eps=1e-5):
    m = x.mean(dim=axis, keepdim=True)
    v = x.var(dim=axis, keepdim=True, unbiased=False)
    return (x - m) / torch.sqrt(v + eps)
def _scatter_add(tgt, idx, src, axis=0):
    return tgt.clone().index_add_(axis, idx, src)
def _segment_sum(src, seg, n):
    out = torch.zeros((n,) + src.shape[1:], dtype=src.dtype)
    return out.index_add_(0, seg, src)
def _pad(x, pw):
    flat = tuple(v for pair in reversed(pw) for v in pair)
    return torch.nn.functional.pad(x, flat)

kernel_name = 'recurrent_scan__spectral_fnet__glu_gated_geglu__prompt'

def setup_inputs():

    _w = (lambda a: (lambda t: t.to(torch.bfloat16) if t.is_floating_point() else t)(torch.as_tensor(a)))
    def _A_setup():
        B, S, D = 1, 2048, 640
        ns = torch.arange(S)[:, None] * torch.arange(S)[None, :]
        nd = torch.arange(D)[:, None] * torch.arange(D)[None, :]
        Ws_re = torch.cos(-2.0 * torch.pi * ns / S).to(float)
        Ws_im = torch.sin(-2.0 * torch.pi * ns / S).to(float)
        Wd_re = torch.cos(-2.0 * torch.pi * nd / D).to(float)
        Wd_im = torch.sin(-2.0 * torch.pi * nd / D).to(float)
        return {
            "x": _w((lambda *s: torch.randn(*s, dtype=torch.bfloat16) * 0.05)(B, S, D)),
            "dft_seq_re": _w(Ws_re),
            "dft_seq_im": _w(Ws_im),
            "dft_hid_re": _w(Wd_re),
            "dft_hid_im": _w(Wd_im),
        }
    _A_in = _A_setup()

    def _B_setup():
        B, S, Dm, Df = 1, 2048, 640, 1536
        return {
            "x": _w((lambda *s: torch.randn(*s, dtype=torch.bfloat16) * 0.05)(B, S, Dm)),
            "w_gate": _w((lambda *s: torch.randn(*s, dtype=torch.bfloat16) * 0.05)(Dm, Df) * (Dm ** -0.5)),
            "w_up": _w((lambda *s: torch.randn(*s, dtype=torch.bfloat16) * 0.05)(Dm, Df) * (Dm ** -0.5)),
            "w_down": _w((lambda *s: torch.randn(*s, dtype=torch.bfloat16) * 0.05)(Df, Dm) * (Df ** -0.5)),
        }
    _B_in = _B_setup()

    return {
        "A_x": _A_in["x"],
        "A_dft_seq_re": _A_in["dft_seq_re"],
        "A_dft_seq_im": _A_in["dft_seq_im"],
        "A_dft_hid_re": _A_in["dft_hid_re"],
        "A_dft_hid_im": _A_in["dft_hid_im"],
        "B_x": _B_in["x"],
        "B_w_gate": _B_in["w_gate"],
        "B_w_up": _B_in["w_up"],
        "B_w_down": _B_in["w_down"],
    }

def reference(A_x, A_dft_seq_re, A_dft_seq_im, A_dft_hid_re, A_dft_hid_im, B_x, B_w_gate, B_w_up, B_w_down):
    def _A_ref(x, dft_seq_re, dft_seq_im, dft_hid_re, dft_hid_im):
        a = torch.einsum("bsd,ed->bse", x, dft_hid_re)
        b = torch.einsum("bsd,ed->bse", x, dft_hid_im)
        y = torch.einsum("ts,bse->bte", dft_seq_re, a) - torch.einsum("ts,bse->bte", dft_seq_im, b)
        return y

    def _B_ref(x, w_gate, w_up, w_down):
        g = torch.einsum("bsd,df->bsf", x, w_gate)
        u = torch.einsum("bsd,df->bsf", x, w_up)
        h = _gelu(g) * u
        return torch.einsum("bsf,fd->bsd", h, w_down)

    x = A_x
    xn = _layernorm(x, axis=-1)
    s = _A_ref(xn, A_dft_seq_re, A_dft_seq_im, A_dft_hid_re, A_dft_hid_im)
    g = _silu(_B_ref(xn, B_w_gate, B_w_up, B_w_down))
    return x + s * g
```

```cpp
#include <hip/hip_runtime.h>
#include <hip/hip_bf16.h>
typedef unsigned short u16;
__device__ __forceinline__ float bf2f(u16 v){ return __uint_as_float(((unsigned)v)<<16); }
__device__ __forceinline__ u16 f2bf(float f){ __hip_bfloat16 h = __float2bfloat16(f); return *(u16*)&h; }
constexpr int S=2048, D=640, F=1536;

__global__ void ln_k(const u16* x, u16* xn){
  int row = blockIdx.x; int t = threadIdx.x;
  float v[10]; float s=0;
  for(int i=0;i<10;i++){ v[i]=bf2f(x[row*D+t+64*i]); s+=v[i]; }
  for(int o=32;o;o>>=1) s+=__shfl_xor(s,o);
  float m=s/D; float q=0;
  for(int i=0;i<10;i++){ float d=v[i]-m; q+=d*d; }
  for(int o=32;o;o>>=1) q+=__shfl_xor(q,o);
  float r=rsqrtf(q/D+1e-5f);
  for(int i=0;i<10;i++) xn[row*D+t+64*i]=f2bf((v[i]-m)*r);
}
__global__ void gemm_k(const u16* A,int lda,const u16* B,int sbk,int sbn,float* C,int M,int N,int K){
  __shared__ float As[16][17], Bs[16][17];
  int tx=threadIdx.x&15, ty=threadIdx.x>>4;
  int m=blockIdx.y*16+ty, n=blockIdx.x*16+tx;
  float acc=0;
  for(int k0=0;k0<K;k0+=16){
    As[ty][tx]=bf2f(A[(long)m*lda+k0+tx]);
    Bs[ty][tx]=bf2f(B[(long)(k0+ty)*sbk+(long)n*sbn]);
    __syncthreads();
    for(int k=0;k<16;k++) acc+=As[ty][k]*Bs[k][tx];
    __syncthreads();
  }
  C[(long)m*N+n]=acc;
}
__global__ void cvt_k(const float* a,u16* o,int n){int i=blockIdx.x*256+threadIdx.x; if(i<n)o[i]=f2bf(a[i]);}
__global__ void geglu_k(const float* g,const float* u,u16* h,int n){int i=blockIdx.x*256+threadIdx.x; if(i<n){float x=g[i]; float t=tanhf(0.7978845608028654f*(x+0.044715f*x*x*x)); h[i]=f2bf(0.5f*x*(1.f+t)*u[i]);}}
__global__ void fin_k(const u16* x,const float* yr,const float* yi,const float* d,u16* o,int n){int i=blockIdx.x*256+threadIdx.x; if(i<n){float dd=d[i]; float sg=dd/(1.f+__expf(-dd)); o[i]=f2bf(bf2f(x[i])+(yr[i]-yi[i])*sg);}}

extern "C" void kernel_launch(void* const* d_in, const int* in_sizes, int n_in,
                              void* d_out, int out_size, void* d_ws, size_t ws_size,
                              hipStream_t stream) {
  const u16* x=(const u16*)d_in[0]; const u16* wsr=(const u16*)d_in[1]; const u16* wsi=(const u16*)d_in[2];
  const u16* wdr=(const u16*)d_in[3]; const u16* wdi=(const u16*)d_in[4];
  const u16* wg=(const u16*)d_in[6]; const u16* wu=(const u16*)d_in[7]; const u16* wdn=(const u16*)d_in[8];
  char* ws=(char*)d_ws;
  u16* xn=(u16*)ws; ws+=S*D*2;
  float* f0=(float*)ws; ws+=S*F*4; float* f1=(float*)ws; ws+=S*F*4; float* f2=(float*)ws; ws+=S*F*4;
  u16* a=(u16*)ws; ws+=S*D*2; u16* b=(u16*)ws; ws+=S*D*2; u16* h=(u16*)ws; ws+=S*F*2;
  ln_k<<<S,64,0,stream>>>(x,xn);
  gemm_k<<<dim3(D/16,S/16),256,0,stream>>>(xn,D,wdr,1,D,f0,S,D,D);
  cvt_k<<<S*D/256,256,0,stream>>>(f0,a,S*D);
  gemm_k<<<dim3(D/16,S/16),256,0,stream>>>(xn,D,wdi,1,D,f0,S,D,D);
  cvt_k<<<S*D/256,256,0,stream>>>(f0,b,S*D);
  gemm_k<<<dim3(F/16,S/16),256,0,stream>>>(xn,D,wg,F,1,f0,S,F,D);
  gemm_k<<<dim3(F/16,S/16),256,0,stream>>>(xn,D,wu,F,1,f1,S,F,D);
  geglu_k<<<S*F/256,256,0,stream>>>(f0,f1,h,S*F);
  gemm_k<<<dim3(D/16,S/16),256,0,stream>>>(wsr,S,a,D,1,f0,S,D,S);
  gemm_k<<<dim3(D/16,S/16),256,0,stream>>>(wsi,S,b,D,1,f1,S,D,S);
  gemm_k<<<dim3(D/16,S/16),256,0,stream>>>(h,F,wdn,D,1,f2,S,D,F);
  fin_k<<<S*D/256,256,0,stream>>>(x,f0,f1,f2,(u16*)d_out,S*D);
}
```

```cpp
#include <hip/hip_runtime.h>
#include <hip/hip_bf16.h>

typedef unsigned short u16;
typedef unsigned long long u64;
typedef short bf16x8 __attribute__((ext_vector_type(8)));
typedef short bf16x4 __attribute__((ext_vector_type(4)));
typedef float f32x4 __attribute__((ext_vector_type(4)));
typedef float f32x2 __attribute__((ext_vector_type(2)));
typedef unsigned u32x4 __attribute__((ext_vector_type(4)));
typedef unsigned u32x2 __attribute__((ext_vector_type(2)));
typedef int i32x4 __attribute__((ext_vector_type(4)));
typedef __bf16 bf2v __attribute__((ext_vector_type(2)));
#define LAS __attribute__((address_space(3)))
#define GAS __attribute__((address_space(1)))

namespace {
constexpr int S = 2048, D = 640, F = 1536;
constexpr int NTHR = 512;
constexpr int ARING = 0;
constexpr int BRING = 65536;
constexpr int TAB = 147456;
constexpr int T_ST = TAB;
constexpr int T_CS = TAB + 1024;
constexpr int T_CG = TAB + 2304;
constexpr int T_PAD = TAB + 3072;
constexpr int T_EK = TAB + 12288;
constexpr int T_BIG = TAB + 4096;
constexpr int X_TILE = ARING + 16384;
constexpr int AUX_CS = BRING + 40960;
constexpr int LDS_BYTES = 163840;
constexpr unsigned W_EPOCH = 0;
constexpr unsigned W_FA = 4096, W_FB = W_FA + 2048, W_FH = W_FB + 2048, W_FP = W_FH + 2048;
constexpr unsigned W_CSP = 16384;
constexpr unsigned W_ST = 81920;
constexpr unsigned W_CSD = W_ST + 32768;
constexpr unsigned W_ABT = 262144;
constexpr unsigned W_H = W_ABT + 2u * 4 * 64 * 10 * 1024;
constexpr unsigned W_WDT = W_H + 32u * 96 * 2 * 1024;
constexpr unsigned W_P = W_WDT + 8u * 48 * 5 * 1024;
constexpr unsigned W_END = W_P + 256u * 4 * 10 * 1024;
constexpr unsigned W_STAMP = W_END;
constexpr int AUX_SC1 = 16;

struct Params {
  const u16 *x, *wsr, *wsi, *wdr, *wdi, *wg, *wu, *wdn;
  u16* out;
  unsigned char* ws;
};

typedef const unsigned char* cptr;
__device__ __forceinline__ void dma2(cptr base, unsigned v0, unsigned v1, unsigned lds) {
  asm volatile("s_mov_b32 m0, %3\n\ts_nop 0\n\tglobal_load_lds_dwordx4 %0, %2\n\tglobal_load_lds_dwordx4 %1, %2 offset:1024"
               ::"v"(v0), "v"(v1), "s"(base), "s"(lds) : "memory");
}
__device__ __forceinline__ void dma3(cptr base, unsigned v0, unsigned v1, unsigned v2, unsigned lds) {
  asm volatile("s_mov_b32 m0, %4\n\ts_nop 0\n\tglobal_load_lds_dwordx4 %0, %3\n\tglobal_load_lds_dwordx4 %1, %3 offset:1024\n\t"
               "global_load_lds_dwordx4 %2, %3 offset:2048" ::"v"(v0), "v"(v1), "v"(v2), "s"(base), "s"(lds) : "memory");
}
__device__ __forceinline__ void dma4(cptr base, unsigned v0, unsigned v1, unsigned v2, unsigned v3, unsigned lds) {
  asm volatile("s_mov_b32 m0, %5\n\ts_nop 0\n\tglobal_load_lds_dwordx4 %0, %4\n\tglobal_load_lds_dwordx4 %1, %4 offset:1024\n\t"
               "global_load_lds_dwordx4 %2, %4 offset:2048\n\tglobal_load_lds_dwordx4 %3, %4 offset:3072"
               ::"v"(v0), "v"(v1), "v"(v2), "v"(v3), "s"(base), "s"(lds) : "memory");
}
__device__ __forceinline__ void dma4c(cptr base, unsigned v, unsigned lds) {
  asm volatile("s_mov_b32 m0, %2\n\ts_nop 0\n\tglobal_load_lds_dwordx4 %0, %1 sc1\n\tglobal_load_lds_dwordx4 %0, %1 offset:1024 sc1\n\t"
               "global_load_lds_dwordx4 %0, %1 offset:2048 sc1\n\tglobal_load_lds_dwordx4 %0, %1 offset:3072 sc1"
               ::"v"(v), "s"(base), "s"(lds) : "memory");
}
__device__ __forceinline__ void dma2c(cptr base, unsigned v, unsigned lds) {
  asm volatile("s_mov_b32 m0, %2\n\ts_nop 0\n\tglobal_load_lds_dwordx4 %0, %1 sc1\n\tglobal_load_lds_dwordx4 %0, %1 offset:1024 sc1"
               ::"v"(v), "s"(base), "s"(lds) : "memory");
}
__device__ __forceinline__ void touch(cptr base, unsigned voff, unsigned pad) {
  asm volatile("s_mov_b32 m0, %2\n\ts_nop 0\n\tglobal_load_lds_dword %0, %1" ::"v"(voff), "s"(base), "s"(pad) : "memory");
}
template <int N> __device__ __forceinline__ void wait_vm() { asm volatile("s_waitcnt vmcnt(%0)" ::"i"(N) : "memory"); }
#define SCHED0() __builtin_amdgcn_sched_barrier(0)
__device__ __forceinline__ void bar() {
  SCHED0();
  __builtin_amdgcn_s_waitcnt(0xC07F);
  asm volatile("s_barrier" ::: "memory");
  SCHED0();
}
__device__ __forceinline__ f32x4 mfma(bf16x8 a, bf16x8 b, f32x4 c) { return __builtin_amdgcn_mfma_f32_16x16x32_bf16(a, b, c, 0, 0, 0); }
__device__ __forceinline__ bf16x8 ldsr(LAS char* p) { return *(LAS bf16x8*)p; }
__device__ __forceinline__ bf16x4 ldstr(LAS char* p) { return __builtin_amdgcn_ds_read_tr16_b64_v4i16((LAS bf16x4*)p); }
__device__ __forceinline__ bf16x8 cat(bf16x4 a, bf16x4 b) { return __builtin_shufflevector(a, b, 0, 1, 2, 3, 4, 5, 6, 7); }
__device__ __forceinline__ unsigned pack2(float a, float b) {
  f32x2 v = {a, b};
  bf2v r = __builtin_convertvector(v, bf2v);
  return __builtin_bit_cast(unsigned, r);
}
__device__ __forceinline__ float asf(unsigned v) { return __builtin_bit_cast(float, v); }
__device__ __forceinline__ float bflo(unsigned v) { return __builtin_bit_cast(float, v << 16); }
__device__ __forceinline__ float bfhi(unsigned v) { return __builtin_bit_cast(float, v & 0xffff0000u); }
template <int CTRL> __device__ __forceinline__ float dpp_add(float v) {
  return v + __builtin_bit_cast(float, __builtin_amdgcn_update_dpp(0, __builtin_bit_cast(int, v), CTRL, 0xf, 0xf, true));
}
__device__ __forceinline__ float row16_sum(float v) {
  v = dpp_add<0x128>(v); v = dpp_add<0x124>(v); v = dpp_add<0x122>(v); v = dpp_add<0x121>(v);
  return v;
}
__device__ __forceinline__ u64 ld_flag(const unsigned char* ws, unsigned off) {
  return __hip_atomic_load((GAS u64*)(ws + off), __ATOMIC_RELAXED, __HIP_MEMORY_SCOPE_AGENT);
}
__device__ __forceinline__ u64 next_tag(u64 prev) { return prev * 6364136223846793005ull + 1442695040888963407ull; }

#define STAMP(i) do { } while (0)

struct Ids {
  int rb, cb, tt, et, kq;
};

template <int WN>
__device__ __forceinline__ void compute(const Params& p, LAS char* L, const Ids& id, int wm, int lane) {
  constexpr int NG = WN == 0 ? 3 : 2;
  constexpr int G0 = WN == 0 ? 0 : 3;
  const int r = lane & 15, q = lane >> 4;
  const __amdgpu_buffer_rsrc_t rws = __builtin_amdgcn_make_buffer_rsrc((void*)p.ws, 0, (int)W_END, 0x00020000);

  {
    const int b = blockIdx.x;
    const u64 tag = next_tag(ld_flag(p.ws, W_EPOCH));
    const int k5 = WN * 4 + q;
    const int row = wm == 0 ? b * 8 + k5 : (b & 127) * 5 + (k5 < 5 ? k5 : 4);
    const u16* src = wm == 0 ? p.x : (b < 128 ? p.wdr : p.wdi);
    u32x4 v[5];
#pragma unroll
    for (int i = 0; i < 5; ++i) v[i] = *(const GAS u32x4*)((const GAS char*)src + row * 1280 + i * 256 + r * 16);
    float s = 0.f;
#pragma unroll
    for (int i = 0; i < 5; ++i)
#pragma unroll
      for (int e = 0; e < 4; ++e) s += bflo(v[i][e]) + bfhi(v[i][e]);
    s = row16_sum(s);
    float o0 = s, o1 = s;
    if (wm == 0) {
      const float mean = s * (1.0f / D);
      float q0 = 0.f, q1 = 0.f;
#pragma unroll
      for (int i = 0; i < 5; ++i)
#pragma unroll
        for (int e = 0; e < 4; ++e) {
          const float d0 = bflo(v[i][e]) - mean, d1 = bfhi(v[i][e]) - mean;
          q0 += d0 * d0; q1 += d1 * d1;
        }
      o0 = mean;
      o1 = __builtin_amdgcn_rsqf(row16_sum(q0 + q1) * (1.0f / D) + 1e-5f);
    }
    if (r == 0 && (wm == 0 || k5 < 5)) {
      const u32x4 o = {__builtin_bit_cast(unsigned, o0), (unsigned)tag, __builtin_bit_cast(unsigned, o1), (unsigned)(tag >> 32)};
      __builtin_amdgcn_raw_buffer_store_b128(o, rws, wm == 0 ? W_ST + row * 16 : W_CSD + (b * 5 + k5) * 16, 0, AUX_SC1);
    }
  }

  const int offK0 = r * 128 + ((q ^ (r >> 1)) & 7) * 16, offK1 = offK0 ^ 64;
  LAS char* const aBase0 = L + ARING + wm * 8192 + offK0;
  LAS char* const aBase1 = L + ARING + wm * 8192 + offK1;
  const f32x4 z4 = {0.f, 0.f, 0.f, 0.f};
  bf16x8 xa0[4], xa1[4];
#define PIN(ptr) asm volatile("" : "+v"(ptr))

  f32x4 accP[NG][4];
  bf16x8 pb0[NG], pb1[NG];
  LAS char* const p1B0 = L + BRING + G0 * 2048 + offK0;
  LAS char* const p1B1 = L + BRING + G0 * 2048 + offK1;
  bar();
#pragma unroll
  for (int t = 0; t < 4; ++t) xa0[t] = ldsr(aBase0 + t * 2048);
#pragma unroll
  for (int g = 0; g < NG; ++g) pb0[g] = ldsr(p1B0 + 2 * 10240 + g * 2048);
#pragma unroll
  for (int t = 0; t < 4; ++t) xa1[t] = ldsr(aBase1 + t * 2048);
#pragma unroll
  for (int g = 0; g < NG; ++g) pb1[g] = ldsr(p1B1 + 2 * 10240 + g * 2048);
#pragma unroll
  for (int k = 0; k < 4 * NG; ++k) accP[k >> 2][k & 3] = mfma(xa0[k & 3], pb0[k >> 2], z4);
#pragma nounroll
  for (int i = 1; i < 10; ++i) {
    LAS char* a0 = aBase0 + (i & 3) * 16384; LAS char* a1 = aBase1 + (i & 3) * 16384;
    LAS char* b0 = p1B0 + ((i + 2) & 3) * 10240; LAS char* b1 = p1B1 + ((i + 2) & 3) * 10240;
    PIN(a0); PIN(a1); PIN(b0); PIN(b1);
    bar();
#pragma unroll
    for (int k = 0; k < 4 * NG; ++k) {
      accP[k >> 2][k & 3] = mfma(xa1[k & 3], pb1[k >> 2], accP[k >> 2][k & 3]);
      if (k == 0) pb0[0] = ldsr(b0);
      if (k < 4) xa0[k] = ldsr(a0 + k * 2048);
      else if (k < 3 + NG) pb0[k - 3] = ldsr(b0 + (k - 3) * 2048);
      else if (k == 3 + NG) pb1[0] = ldsr(b1);
      else if (k == 8) pb1[1] = ldsr(b1 + 2048);
      SCHED0();
    }
#pragma unroll
    for (int k = 0; k < 4 * NG; ++k) {
      accP[k >> 2][k & 3] = mfma(xa0[k & 3], pb0[k >> 2], accP[k >> 2][k & 3]);
      if (k == 0) pb1[NG - 1] = ldsr(b1 + (NG - 1) * 2048);
      if (k < 4) xa1[k] = ldsr(a1 + k * 2048);
      SCHED0();
    }
  }

  f32x4 accH[6][4];
  bf16x4 w0[12], w1[12];
  const int trBase = (8 * q + (r >> 2)) * 192 + 8 * (r & 3);
  LAS char* const tE = L + BRING + trBase + 32 * (q & 1);
  LAS char* const tO = L + BRING + trBase - 32 * (q & 1);
  auto p2_rd = [&](LAS char* e, LAS char* o, int ks, int h) -> bf16x4 {
    const int gam = 3 * WN + (h >> 2);
    return ldstr(((gam & 1) ? o : e) + ks * 6144 + gam * 32 + ((h >> 1) & 1) * 12288 + (h & 1) * 768);
  };
  u32x4 abo[2][NG];
  const unsigned abBase = W_ABT + (unsigned)((((id.cb >> 3) * 4 + ((id.cb & 7) >> 1)) * 64 + id.rb * 4 + wm * 2) * 10 + (id.cb & 1) * 5 + G0) * 1024u + lane * 16;
  auto ab_store = [&](int n) {
    if (n < 2 * NG) __builtin_amdgcn_raw_buffer_store_b128(abo[n / NG][n % NG], rws, abBase + ((n / NG) * 10 + n % NG) * 1024, 0, AUX_SC1);
  };
  auto p2_stage = [&](int j, int sn) {
    const int sl = (j + 2) % 3;
    const int so = sl == 2 ? 57344 : sl * 24576;
    LAS char* a0 = aBase0 + ((j + 2) & 3) * 16384; LAS char* a1 = aBase1 + ((j + 2) & 3) * 16384;
    LAS char* e = tE + so; LAS char* o = tO + so;
    PIN(a0); PIN(a1); PIN(e); PIN(o);
    bar();
#pragma unroll
    for (int k = 0; k < 24; ++k) {
      accH[k >> 2][k & 3] = mfma(cat(w1[(k >> 2) * 2], w1[(k >> 2) * 2 + 1]), xa1[k & 3], accH[k >> 2][k & 3]);
      if (k < 4) xa0[k] = ldsr(a0 + k * 2048);
      else if (k < 16) w0[k - 4] = p2_rd(e, o, 0, k - 4);
      else w1[k - 16] = p2_rd(e, o, 1, k - 16);
      SCHED0();
    }
#pragma unroll
    for (int k = 0; k < 24; ++k) {
      accH[k >> 2][k & 3] = mfma(cat(w0[(k >> 2) * 2], w0[(k >> 2) * 2 + 1]), xa0[k & 3], accH[k >> 2][k & 3]);
      if (k < 4) w1[8 + k] = p2_rd(e, o, 1, 8 + k);
      else if (k < 8) xa1[k - 4] = ldsr(a1 + (k - 4) * 2048);
      if (sn >= 0 && k == 10) ab_store(sn);
      if (sn >= 0 && k == 22) ab_store(sn + 1);
      SCHED0();
    }
  };
  bar();
  {
    LAS char* e = tE + 57344; LAS char* o = tO + 57344;
    float cs[NG];
    f32x4 st[4];
#pragma unroll
    for (int k = 0; k < 4 * NG; ++k) {
      accP[k >> 2][k & 3] = mfma(xa1[k & 3], pb1[k >> 2], accP[k >> 2][k & 3]);
      if (k < 2) { xa0[2 * k] = ldsr(aBase0 + 2 * 16384 + k * 4096); xa0[2 * k + 1] = ldsr(aBase0 + 2 * 16384 + k * 4096 + 2048); }
      else if (k < 8) { w0[2 * k - 4] = p2_rd(e, o, 0, 2 * k - 4); w0[2 * k - 3] = p2_rd(e, o, 0, 2 * k - 3); }
      SCHED0();
    }
#pragma unroll
    for (int g = 0; g < NG; ++g) cs[g] = *(LAS float*)(L + T_CS + ((G0 + g) * 16 + r) * 4);
#pragma unroll
    for (int k = 0; k < 24; ++k) {
      const int u = k / 12, g = (k % 12) >> 2, c = k & 3;
      if (k % 12 == 0) {
#pragma unroll
        for (int n = 0; n < 4; ++n) st[n] = *(LAS f32x4*)(L + T_ST + (wm * 64 + u * 32 + (n >> 1) * 16 + 4 * q) * 8 + (n & 1) * 16);
      }
      accH[k >> 2][k & 3] = mfma(cat(w0[(k >> 2) * 2], w0[(k >> 2) * 2 + 1]), xa0[k & 3], z4);
      if (k < 4) xa1[k] = ldsr(aBase1 + 2 * 16384 + k * 2048);
      else if (k < 16) w1[k - 4] = p2_rd(e, o, 1, k - 4);
      if (g < NG) {
        const f32x4 a = accP[g < NG ? g : 0][2 * u + (c >> 1)];
        const float cc = cs[g < NG ? g : 0];
        abo[u][g < NG ? g : 0][c] = pack2(a[(c & 1) * 2] * st[c][1] + st[c][0] * cc, a[(c & 1) * 2 + 1] * st[c][3] + st[c][2] * cc);
      }
      if (k == 11) ab_store(0);
      if (k == 23) ab_store(1);
      SCHED0();
    }
  }
  p2_stage(1, 2);
  p2_stage(2, 4);
#pragma nounroll
  for (int j = 3; j < 10; ++j) {
    if (j == 4) wait_vm<0>();
    p2_stage(j, -1);
  }

  f32x4 accY[5][4];
  bf16x8 yb0[5], yb1[5];
  LAS char* const yBase = L + BRING + WN * 5120 + lane * 16;
  const unsigned hbase = W_H + (unsigned)(((id.rb * 2 + wm) * 96 + id.cb * 6 + 3 * WN) * 2) * 1024u + lane * 16;
  float gv[8], uv[8], wv[8];
  f32x4 cgv, cuv, ek[2];
  auto h_tab = [&](int c) {
    cgv = *(LAS f32x4*)(L + T_CG + ((3 * WN + (c >> 1)) * 16 + 4 * q) * 4);
    cuv = *(LAS f32x4*)(L + T_CG + 384 + ((3 * WN + (c >> 1)) * 16 + 4 * q) * 4);
    ek[0] = *(LAS f32x4*)(L + T_EK + (wm * 64 + (c & 1) * 32 + r) * 16);
    ek[1] = *(LAS f32x4*)(L + T_EK + (wm * 64 + (c & 1) * 32 + 16 + r) * 16);
  };
  auto h_op = [&](int c, int n) {
    const int g = c >> 1, u = c & 1, ph = n >> 3, x = n & 7, t = x >> 2, e = x & 3;
    if (ph == 0) { gv[x] = accH[2 * g][2 * u + t][e] + ek[t][0] * cgv[e]; PIN(gv[x]); uv[x] = accH[2 * g + 1][2 * u + t][e] + ek[t][0] * cuv[e]; PIN(uv[x]); }
    if (ph == 1) { wv[x] = gv[x] * (ek[t][1] + ek[t][2] * (gv[x] * gv[x])); PIN(wv[x]); }
    if (ph == 2) { wv[x] = __builtin_amdgcn_exp2f(wv[x]); PIN(wv[x]); uv[x] *= gv[x]; PIN(uv[x]); }
    if (ph == 3) { wv[x] = __builtin_amdgcn_rcpf(wv[x] * ek[t][3] + ek[t][3]); PIN(wv[x]); }
    if (ph == 4) {
      uv[x] *= wv[x]; PIN(uv[x]);
      if (x == 0 && c < 5) h_tab(c + 1);
      if (x == 7) {
        u32x4 o = {pack2(uv[0], uv[1]), pack2(uv[2], uv[3]), pack2(uv[4], uv[5]), pack2(uv[6], uv[7])};
        __builtin_amdgcn_raw_buffer_store_b128(o, rws, hbase + (g * 2 + u) * 1024, 0, AUX_SC1);
      }
    }
  };
  auto y_stage = [&](int i, int c) {
    LAS char* a0 = aBase0 + (i & 3) * 16384; LAS char* a1 = aBase1 + (i & 3) * 16384;
    LAS char* b = yBase + (i & 3) * 20480;
    PIN(a0); PIN(a1); PIN(b);
    bar();
#pragma unroll
    for (int k = 0; k < 20; ++k) {
      accY[k >> 2][k & 3] = mfma(yb1[k >> 2], xa1[k & 3], accY[k >> 2][k & 3]);
      if (k < 4) xa0[k] = ldsr(a0 + k * 2048);
      else if (k < 9) yb0[k - 4] = ldsr(b + (k - 4) * 1024);
      else if (k < 11) yb1[k - 9] = ldsr(b + 10240 + (k - 9) * 1024);
      else if (k == 12) yb1[2] = ldsr(b + 10240 + 2048);
      else if (k == 16) yb1[3] = ldsr(b + 10240 + 3072);
      if (c >= 0) h_op(c, k);
      SCHED0();
    }
#pragma unroll
    for (int k = 0; k < 20; ++k) {
      accY[k >> 2][k & 3] = mfma(yb0[k >> 2], xa0[k & 3], accY[k >> 2][k & 3]);
      if (k == 0) yb1[4] = ldsr(b + 10240 + 4096);
      else if (k < 5) xa1[k - 1] = ldsr(a1 + (k - 1) * 2048);
      if (c >= 0) h_op(c, 20 + k);
      SCHED0();
    }
  };
  bar();
  {
#pragma unroll
    for (int k = 0; k < 24; ++k) {
      accH[k >> 2][k & 3] = mfma(cat(w1[(k >> 2) * 2], w1[(k >> 2) * 2 + 1]), xa1[k & 3], accH[k >> 2][k & 3]);
      if (k < 4) xa0[k] = ldsr(aBase0 + k * 2048);
      else if (k < 9) yb0[k - 4] = ldsr(yBase + (k - 4) * 1024);
      if (k == 4) h_tab(0);
      if (k >= 10) {
#pragma unroll
        for (int n = 3 * (k - 10); n < 3 * (k - 9) && n < 40; ++n) h_op(0, n);
      }
      SCHED0();
    }
#pragma unroll
    for (int k = 0; k < 20; ++k) {
      accY[k >> 2][k & 3] = mfma(yb0[k >> 2], xa0[k & 3], z4);
      if (k < 4) xa1[k] = ldsr(aBase1 + k * 2048);
      else if (k >= 12 && k < 17) yb1[k - 12] = ldsr(yBase + 10240 + (k - 12) * 1024);
#pragma unroll
      for (int n = 40 + 4 * k; n < 44 + 4 * k; ++n) h_op(n / 40, n % 40);
      SCHED0();
    }
  }
  y_stage(1, 3); y_stage(2, 4); y_stage(3, 5);
#pragma nounroll
  for (int i = 4; i < 16; ++i) {
    if (i == 6) wait_vm<0>();
    y_stage(i, -1);
  }

  const int qid = wm * 2 + WN;
  const int tile = id.tt * 4 + id.et;
  f32x4 accD[NG][2];
#pragma unroll
  for (int g = 0; g < NG; ++g) { accD[g][0] = z4; accD[g][1] = z4; }
  LAS char* const hB = L + ARING + wm * 1024 + lane * 16;
  LAS char* const dB = L + BRING + G0 * 1024 + lane * 16;
  bf16x8 ha0[4], db0[2][NG], ha1[4], db1[2][NG];
  auto d_rd = [&](int i, int hf, bf16x8(&ha)[4], bf16x8(&wb)[2][NG]) {
    LAS char* a = hB + (i & 3) * 16384 + hf * 8192;
    LAS char* b = dB + (i & 3) * 20480 + hf * 10240;
#pragma unroll
    for (int k = 0; k < 2; ++k) {
      ha[2 * k] = ldsr(a + k * 4096); ha[2 * k + 1] = ldsr(a + k * 4096 + 2048);
#pragma unroll
      for (int g = 0; g < NG; ++g) wb[k][g] = ldsr(b + k * 5120 + g * 1024);
    }
  };
  auto d_mm = [&](bf16x8(&ha)[4], bf16x8(&wb)[2][NG]) {
#pragma unroll
    for (int k = 0; k < 2; ++k) {
      const bf16x8 f0 = __builtin_shufflevector(ha[2 * k], ha[2 * k + 1], 0, 1, 2, 3, 8, 9, 10, 11);
      const bf16x8 f1 = __builtin_shufflevector(ha[2 * k], ha[2 * k + 1], 4, 5, 6, 7, 12, 13, 14, 15);
#pragma unroll
      for (int g = 0; g < NG; ++g) { accD[g][0] = mfma(wb[k][g], f0, accD[g][0]); accD[g][1] = mfma(wb[k][g], f1, accD[g][1]); }
    }
  };
  const unsigned pbase = W_P + (unsigned)(((tile * 4 + qid) * 4 + id.kq) * 10) * 1024u + lane * 16;
  auto p_store = [&](int n) {
    const f32x4 a = accY[n % 5][(n / 5) * 2], b = accY[n % 5][(n / 5) * 2 + 1];
    const u32x4 o = {pack2(a[0], a[1]), pack2(a[2], a[3]), pack2(b[0], b[1]), pack2(b[2], b[3])};
    if (qid != id.kq) __builtin_amdgcn_raw_buffer_store_b128(o, rws, pbase + n * 1024, 0, AUX_SC1);
    else *(LAS u32x4*)(L + T_BIG + n * 1024 + lane * 16) = o;
  };
  auto d_stage = [&](int i, int n0, int n1, int n2) {
    bar();
    d_rd(i, 0, ha0, db0); SCHED0();
    d_mm(ha1, db1); SCHED0();
    d_rd(i, 1, ha1, db1); SCHED0();
    if (n0 >= 0) p_store(n0);
    d_mm(ha0, db0);
    if (n1 >= 0) p_store(n1);
    if (n2 >= 0) p_store(n2);
  };
  bar();
  d_rd(0, 0, ha0, db0);
  SCHED0();
#pragma unroll
  for (int k = 0; k < 20; ++k) accY[k >> 2][k & 3] = mfma(yb1[k >> 2], xa1[k & 3], accY[k >> 2][k & 3]);
  SCHED0();
  d_rd(0, 1, ha1, db1);
  SCHED0();
  p_store(0);
  d_mm(ha0, db0);
  p_store(1);
  d_stage(1, 2, 3, 4); d_stage(2, 5, 6, 7); d_stage(3, 8, 9, -1);
#pragma nounroll
  for (int i = 4; i < 12; ++i) {
    if (i == 5) wait_vm<0>();
    d_stage(i, -1, -1, -1);
  }
  d_mm(ha1, db1);
#pragma unroll
  for (int g = 0; g < NG; ++g)
#pragma unroll
    for (int t = 0; t < 2; ++t)
#pragma unroll
      for (int e = 0; e < 4; ++e) {
        const float d = accD[g][t][e];
        accD[g][t][e] = d * __builtin_amdgcn_rcpf(1.f + __builtin_amdgcn_exp2f(-1.4426950409f * d));
      }
  bar();
#pragma unroll
  for (int g = 0; g < NG; ++g) {
    const int po = (wm * 5 + G0 + g) * 1024 + lane * 16;
    const u32x4 y0 = *(LAS u32x4*)(L + T_BIG + po), y1 = *(LAS u32x4*)(L + BRING + po), y2 = *(LAS u32x4*)(L + BRING + 10240 + po), y3 = *(LAS u32x4*)(L + ARING + po);
#pragma unroll
    for (int t = 0; t < 2; ++t) {
      const int tl = wm * 32 + 8 * (r >> 2) + 4 * t + (r & 3);
      LAS u32x2* xp = (LAS u32x2*)(L + X_TILE + tl * 160 + (G0 + g) * 32 + q * 8);
      const u32x2 xv = *xp;
      u32x2 ov;
#pragma unroll
      for (int h = 0; h < 2; ++h) {
        const int n = 2 * t + h;
        const float lo = (bflo(y0[n]) + bflo(y1[n])) + (bflo(y2[n]) + bflo(y3[n]));
        const float hi = (bfhi(y0[n]) + bfhi(y1[n])) + (bfhi(y2[n]) + bfhi(y3[n]));
        ov[h] = pack2(bflo(xv[h]) + lo * accD[g][t][2 * h], bfhi(xv[h]) + hi * accD[g][t][2 * h + 1]);
      }
      *xp = ov;
    }
  }
}

template <int LD>
__device__ __forceinline__ void loader(const Params& p, unsigned lb, const Ids& id, int lane) {
  const unsigned vl = lane * 16;
  const unsigned c0 = ((lane & 7) ^ (lane >> 4)) * 16, c1 = c0 ^ 64;
  const int tile = id.tt * 4 + id.et;
  const cptr bp = p.ws + W_P + (unsigned)((tile * 4 + id.kq) * 4 * 10) * 1024u + ((id.kq + 1 + LD) & 3) * 10240;
  const int rl = 8 * (lane >> 5) + ((lane >> 3) & 3);
  const unsigned vx0 = rl * 1280 + c0, vx1 = (rl + 16) * 1280 + c1 - 1024, vx2 = (rl + 4) * 1280 + c0 - 2048, vx3 = (rl + 20) * 1280 + c1 - 3072;
  const unsigned vw0 = rl * 4096 + c0, vw1 = (rl + 16) * 4096 + c1 - 1024, vw2 = (rl + 4) * 4096 + c0 - 2048, vw3 = (rl + 20) * 4096 + c1 - 3072;
  const cptr bx = (cptr)p.x + id.rb * 128 * 1280;
  const cptr bw = (cptr)(id.kq < 2 ? p.wsr : p.wsi) + (id.tt * 128 * 4096 + (id.kq & 1) * 2048);
  const cptr bh = p.ws + W_H + (unsigned)(id.tt * 2 + (id.kq >> 1)) * 196608u;
  constexpr int GA0 = LD == 0 ? 0 : 3, GA1 = LD == 0 ? 3 : LD == 1 ? 4 : 3;
  auto issueA = [&](int j) {
    const unsigned l = lb + ARING + (j & 3) * 16384;
    if (j < 20) {
      const cptr so = bx + (j < 10 ? j : j - 10) * 128;
#pragma unroll
      for (int gq = GA0; gq < GA1; ++gq) dma4(so + gq * 32 * 1280, vx0, vx1, vx2, vx3, l + gq * 4096);
    } else if (j < 36) {
      const cptr so = bw + (j - 20) * 128;
#pragma unroll
      for (int gq = GA0; gq < GA1; ++gq) dma4(so + gq * 32 * 4096, vw0, vw1, vw2, vw3, l + gq * 4096);
    } else {
      const cptr so = bh + (j - 36) * 16384;
#pragma unroll
      for (int gq = GA0; gq < GA1; ++gq) dma4c(so + gq * 4096, vl, l + gq * 4096);
    }
  };
  auto issueP = [&]() {
    const unsigned l = lb + (LD == 0 ? ARING : BRING + (LD - 1) * 10240);
    dma4c(bp, vl, l); dma4c(bp + 4096, vl, l + 4096); dma2c(bp + 8192, vl, l + 8192);
  };
  if (LD == 0) {
    issueA(0); issueA(1);
    wait_vm<12>(); bar(); issueA(2); issueA(3);
#pragma nounroll
    for (int i = 1; i < 45; ++i) { wait_vm<24>(); bar(); issueA(i + 3); }
  } else {
    const unsigned vd0 = (lane >> 3) * 1280 + c0, vd1 = (lane >> 3) * 1280 + c1 + 9216, vd2 = vd0 + 2 * 9216, vd3 = vd1 + 2 * 9216;
    unsigned vg[3];
#pragma unroll
    for (int i = 0; i < 3; ++i) {
      const int n = i * 64 + lane, k = n / 12, pos = n - k * 12;
      vg[i] = k * 3072 + (pos ^ (((k >> 3) & 1) << 1)) * 16 - i * 1024;
    }
    const cptr bd = (cptr)(id.cb < 8 ? p.wdr : p.wdi) + (id.cb & 7) * 80 * 1280;
    const cptr bg = (cptr)(LD == 1 ? p.wg : p.wu) + id.cb * 192;
    const cptr bab = p.ws + W_ABT + (unsigned)(((id.kq >> 1) * 4 + id.et) * 64 + (id.kq & 1) * 32) * 10240u;
    const cptr bwd = p.ws + W_WDT + (unsigned)(id.et * 2 + (id.kq & 1)) * 245760u;
    auto issue1 = [&](int i) {
      const unsigned l = lb + BRING + ((i + 2) & 3) * 10240;
      if (LD == 1) dma4(bd + i * 128, vd0, vd1, vd2, vd3, l);
      else { dma4(bd + (i * 128 + 32 * 1280), vd0, vd1, vd2, vd3, l + 4096); dma2(bd + (i * 128 + 64 * 1280), vd0, vd1, l + 8192); }
    };
    auto issue2 = [&](int j) {
      const int sl = (j + 2) % 3;
      const unsigned l = lb + BRING + (sl == 2 ? 57344 : sl * 24576) + (LD - 1) * 12288;
#pragma unroll
      for (int pp = 0; pp < 4; ++pp) dma3(bg + (j * 64 + pp * 16) * 3072, vg[0], vg[1], vg[2], l + pp * 3072);
    };
    auto issue3 = [&](int i) {
      const unsigned l = lb + BRING + (i & 3) * 20480;
      const cptr so = i < 36 ? bab + (i - 20) * 20480 : bwd + (i - 36) * 20480;
#pragma unroll
      for (int gq = (LD == 1 ? 0 : 2); gq < (LD == 1 ? 2 : 5); ++gq) dma4c(so + gq * 4096, vl, l + gq * 4096);
    };
    constexpr bool L1 = LD == 1;
    issue1(0); issueA(0); issue1(1); issueA(1);
    wait_vm<L1 ? 8 : 6>(); bar(); issue1(2); issueA(2); issue1(3); issueA(3);
#pragma nounroll
    for (int i = 1; i < 7; ++i) { wait_vm<L1 ? 16 : 12>(); bar(); issue1(i + 3); issueA(i + 3); }
    wait_vm<L1 ? 16 : 12>(); bar(); issue2(0); issueA(10);
    wait_vm<L1 ? 24 : 18>(); bar(); issueA(11);
#pragma nounroll
    for (int i = 9; i < 18; ++i) { wait_vm<L1 ? 20 : 12>(); bar(); issue2(i - 8); issueA(i + 3); }
    wait_vm<L1 ? 20 : 12>(); bar(); issue3(20); issueA(21);
    wait_vm<L1 ? 16 : 12>(); bar(); issue3(21); issueA(22);
    wait_vm<L1 ? 16 : 12>(); bar(); issue3(22); issue3(23); issueA(23);
#pragma nounroll
    for (int i = 21; i < 45; ++i) { wait_vm<24>(); bar(); issue3(i + 3); issueA(i + 3); }
  }
  wait_vm<24>(); bar(); issueP();
  wait_vm<22>(); bar();
  wait_vm<10>(); bar();
  wait_vm<0>(); bar();
}

__device__ __forceinline__ void st_flag(unsigned char* ws, unsigned off, u64 v, int lane) {
  if (lane == 0) __hip_atomic_store((GAS u64*)(ws + off), v, __ATOMIC_RELAXED, __HIP_MEMORY_SCOPE_AGENT);
}
template <int N>
__device__ __forceinline__ void wait_flags(const unsigned char* ws, unsigned off, u64 tag, bool ok) {
  for (int spin = 0; spin < (1 << 16) && !__all(ok); ++spin) {
    ok = true;
#pragma unroll
    for (int k = 0; k < N; ++k) ok &= ld_flag(ws, off + k * 512) == tag;
  }
  __builtin_amdgcn_fence(__ATOMIC_ACQUIRE, "wavefront");
}

__device__ __forceinline__ void helper(const Params& p, LAS char* L, unsigned lb, const Ids& id, int lane) {
  const int b = blockIdx.x;
  const __amdgpu_buffer_rsrc_t rws = __builtin_amdgcn_make_buffer_rsrc((void*)p.ws, 0, (int)W_END, 0x00020000);
  STAMP(60);
  const u64 prev = ld_flag(p.ws, W_EPOCH);
  const int bb = b < 240 ? b : b - 240;
  const int pc = bb % 48, pr = bb / 48;
  const int wk = bb / 5, wc = bb % 5;
  const cptr sm = (cptr)(pc < 24 ? p.wg : p.wu) + (pr * 128 * 3072 + (pc % 24) * 128);
  const cptr sn = (cptr)p.wdn + (wk * 32 * 1280 + wc * 256);
  const unsigned va = (lane >> 3) * 3072 + (lane & 7) * 16, vn = (lane >> 4) * 1280 + (lane & 15) * 16;
  bar(); STAMP(0);
#pragma nounroll
  for (int i = 0; i < 4; ++i) {
    dma4(sm + i * 32 * 3072, va, va + 23552, va + 2 * 23552, va + 3 * 23552, lb + AUX_CS + i * 4096);
    dma2(sn + i * 8 * 1280, vn, vn + 4096, lb + T_BIG + i * 2048);
    bar(); STAMP(1 + i);
  }
  const u64 tag = next_tag(prev);
  const unsigned tlo = (unsigned)tag, thi = (unsigned)(tag >> 32);
  const int go = lane < 40 ? lane : 0;
  auto ld_gr = [&](u32x4(&g)[4]) {
    g[0] = __builtin_amdgcn_raw_buffer_load_b128(rws, W_ST + (id.rb * 128 + lane) * 16, 0, AUX_SC1);
    g[1] = __builtin_amdgcn_raw_buffer_load_b128(rws, W_ST + (id.rb * 128 + 64 + lane) * 16, 0, AUX_SC1);
    g[2] = __builtin_amdgcn_raw_buffer_load_b128(rws, W_CSD + (id.cb * 80 + go) * 16, 0, AUX_SC1);
    g[3] = __builtin_amdgcn_raw_buffer_load_b128(rws, W_CSD + (id.cb * 80 + 40 + go) * 16, 0, AUX_SC1);
  };
  bar(); STAMP(5);
  bar(); STAMP(6);
  wait_vm<0>();
  const int g4 = lane >> 4, q4 = (lane & 15) >> 2, p4 = lane & 3;
  u32x4 gr[4];
  ld_gr(gr);
  {
    const bf16x8 ones = {0x3f80, 0x3f80, 0x3f80, 0x3f80, 0x3f80, 0x3f80, 0x3f80, 0x3f80};
    f32x4 acc[4];
    LAS char* a = L + AUX_CS + (8 * g4 + q4) * 128 + 8 * p4;
#pragma unroll
    for (int c = 0; c < 4; ++c) {
      acc[c] = f32x4{0.f, 0.f, 0.f, 0.f};
#pragma unroll
      for (int ks = 0; ks < 4; ++ks) acc[c] = mfma(ones, cat(ldstr(a + ks * 4096 + c * 32), ldstr(a + ks * 4096 + 512 + c * 32)), acc[c]);
      if (c == 0) { bar(); STAMP(7); }
      if (c == 2) { bar(); STAMP(8); }
    }
    const float v = g4 == 0 ? acc[0][0] : g4 == 1 ? acc[1][0] : g4 == 2 ? acc[2][0] : acc[3][0];
    __builtin_amdgcn_raw_buffer_store_b32(__builtin_bit_cast(unsigned, v), rws, lane * 4 + W_CSP + (pr * 3072 + pc * 64) * 4, 0, AUX_SC1);
  }
  bar(); STAMP(9);
  for (int spin = 0; spin < (1 << 16); ++spin) {
    bool ok = true;
#pragma unroll
    for (int k = 0; k < 4; ++k) ok &= gr[k][1] == tlo && gr[k][3] == thi;
    if (__all(ok)) break;
    ld_gr(gr);
  }
  const int rho = (lane & 32) + ((lane >> 2) & 1) * 16 + ((lane >> 3) & 3) * 4 + (lane & 3);
  {
    const float sg = id.cb >= 8 ? -1.f : 1.f;
#pragma unroll
    for (int h = 0; h < 2; ++h) {
      const float rs = sg * asf(gr[h][2]);
      *(LAS f32x2*)(L + T_ST + (h * 64 + rho) * 8) = f32x2{-asf(gr[h][0]) * rs, rs};
    }
    *(LAS unsigned*)(L + T_CS + go * 4) = gr[2][0];
    *(LAS unsigned*)(L + T_CS + (40 + go) * 4) = gr[3][0];
  }
  bar(); STAMP(10);
#pragma unroll
  for (int h = 0; h < 2; ++h) {
    const float rs = asf(gr[h][2]);
    *(LAS f32x4*)(L + T_EK + (h * 64 + rho) * 16) =
        f32x4{-asf(gr[h][0]), -2.302208198f * rs, -0.1029432397f * rs * rs * rs, __builtin_amdgcn_rcpf(rs * rs)};
  }
  {
    LAS char* a = L + T_BIG + (4 * g4 + q4) * 256 + 8 * p4;
#pragma unroll
    for (int c = 0; c < 8; ++c) {
      const bf16x8 f = cat(ldstr(a + c * 32), ldstr(a + 4096 + c * 32));
      const int eg = wc * 8 + c, c80 = eg / 5, g5 = eg - c80 * 5;
      __builtin_amdgcn_raw_buffer_store_b128(__builtin_bit_cast(u32x4, f), rws, lane * 16 + W_WDT + ((c80 * 48 + wk) * 5 + g5) * 1024, 0, AUX_SC1);
      if (c == 3) { bar(); STAMP(11); }
    }
  }
  bar(); STAMP(12);
  bar(); STAMP(13);
  wait_vm<0>();
  st_flag(p.ws, W_FA + b * 8, tag, lane);
  bar(); STAMP(14);
  st_flag(p.ws, W_FB + (id.rb * 16 + id.cb) * 8, tag, lane);
  bar(); STAMP(15);
  u64 fa[4];
#pragma unroll
  for (int k = 0; k < 4; ++k) fa[k] = ld_flag(p.ws, W_FA + lane * 8 + k * 512);
  bar(); STAMP(16);
  const unsigned fbo = W_FB + ((8 * (id.kq & 1) + (lane & 7)) * 16 + 8 * (id.kq >> 1) + 2 * id.et + ((lane >> 3) & 1)) * 8;
  const u64 fb = ld_flag(p.ws, fbo);
  bar(); STAMP(17);
  wait_flags<4>(p.ws, W_FA + lane * 8, tag, fa[0] == tag && fa[1] == tag && fa[2] == tag && fa[3] == tag);
  if (b == 0) st_flag(p.ws, W_EPOCH, tag, lane);
  float cv[3][5];
#pragma unroll
  for (int i = 0; i < 3; ++i) {
    const int n = i * 64 + lane, m = n >= 96 ? 1 : 0;
    const int off = (m * 1536 + id.cb * 96 + n - m * 96) * 4;
#pragma unroll
    for (int k = 0; k < 5; ++k) cv[i][k] = __builtin_bit_cast(float, __builtin_amdgcn_raw_buffer_load_b32(rws, off, W_CSP + k * 12288, AUX_SC1));
  }
  wait_flags<1>(p.ws, fbo, tag, fb == tag);
  bar(); STAMP(18);
  bar(); STAMP(19);
#pragma unroll
  for (int i = 0; i < 3; ++i) *(LAS float*)(L + T_CG + (i * 64 + lane) * 4) = ((cv[i][0] + cv[i][1]) + (cv[i][2] + cv[i][3])) + cv[i][4];
#pragma nounroll
  for (int i = 20; i < 27; ++i) { bar(); STAMP(i); }
  st_flag(p.ws, W_FH + (id.rb * 16 + id.cb) * 8, tag, lane);
  bar(); STAMP(27);
  bar(); STAMP(28);
  bar(); STAMP(29);
  bar(); STAMP(30);
  const unsigned fho = W_FH + (id.tt * 16 + (lane & 15)) * 8;
  const u64 fh = ld_flag(p.ws, fho);
  bar(); STAMP(31);
  bar(); STAMP(32);
  wait_flags<1>(p.ws, fho, tag, fh == tag);
#pragma nounroll
  for (int i = 33; i < 42; ++i) { bar(); STAMP(i); }
  const int tile = id.tt * 4 + id.et;
  st_flag(p.ws, W_FP + (tile * 4 + id.kq) * 8, tag, lane);
  bar(); STAMP(42);
  bar(); STAMP(43);
  const unsigned fpo = W_FP + (tile * 4 + (lane & 3)) * 8;
  const u64 fp = ld_flag(p.ws, fpo);
  bar(); STAMP(44);
  wait_flags<1>(p.ws, fpo, tag, fp == tag);
  bar(); STAMP(45);
  bar(); STAMP(46);
  {
    const cptr so = (cptr)p.x + ((id.tt * 128 + (id.kq >> 1) * 64) * 1280 + (id.et * 160 + (id.kq & 1) * 80) * 2);
#pragma unroll
    for (int i = 0; i < 5; ++i) {
      const int n0 = i * 128 + lane, r0 = n0 / 10, n1 = n0 + 64, r1 = n1 / 10;
      dma2(so, r0 * 1280 + (n0 - r0 * 10) * 16, r1 * 1280 + (n1 - r1 * 10) * 16 - 1024, lb + X_TILE + i * 2048);
    }
  }
  bar(); STAMP(47);
  wait_vm<0>();
  bar(); STAMP(48);
}

__global__ __launch_bounds__(NTHR, 2) void fused_kernel(Params p) {
  extern __shared__ __attribute__((aligned(16))) char smem[];
  LAS char* L = (LAS char*)smem;
  const unsigned lb = (unsigned)(size_t)L;
  const int tid = threadIdx.x, lane = tid & 63, w = __builtin_amdgcn_readfirstlane(tid >> 6);
  const int b = blockIdx.x, g = b & 7, rr = b >> 3;
  Ids id;
  id.rb = (g & 1) * 8 + (rr & 7);
  id.cb = (g >> 1) * 4 + (rr >> 3);
  id.tt = id.rb;
  id.et = rr >> 3;
  id.kq = g >> 1;
  if (w < 4) {
    if (w & 1) compute<1>(p, L, id, w >> 1, lane);
    else compute<0>(p, L, id, w >> 1, lane);
  } else if (w == 4) loader<0>(p, lb, id, lane);
  else if (w == 5) loader<1>(p, lb, id, lane);
  else if (w == 6) loader<2>(p, lb, id, lane);
  else helper(p, L, lb, id, lane);
  bar();
  GAS char* const ob = (GAS char*)p.out + (size_t)(id.tt * 128 + (id.kq >> 1) * 64) * 1280 + (id.et * 160 + (id.kq & 1) * 80) * 2;
  {
    const int row = tid / 10, c = tid - row * 10;
    *(GAS u32x4*)(ob + row * 1280 + c * 16) = *(LAS u32x4*)(L + X_TILE + tid * 16);
  }
  if (tid < 128) {
    const int n = tid + 512, row = n / 10, c = n - row * 10;
    *(GAS u32x4*)(ob + row * 1280 + c * 16) = *(LAS u32x4*)(L + X_TILE + n * 16);
  }
}
}

extern "C" void kernel_launch(void* const* d_in, const int* in_sizes, int n_in, void* d_out, int out_size, void* d_ws, size_t ws_size,
                              hipStream_t stream) {
  Params p;
  p.x = (const u16*)d_in[0];
  p.wsr = (const u16*)d_in[1];
  p.wsi = (const u16*)d_in[2];
  p.wdr = (const u16*)d_in[3];
  p.wdi = (const u16*)d_in[4];
  p.wg = (const u16*)d_in[6];
  p.wu = (const u16*)d_in[7];
  p.wdn = (const u16*)d_in[8];
  p.out = (u16*)d_out;
  p.ws = (unsigned char*)d_ws;
  (void)hipFuncSetAttribute((const void*)fused_kernel, hipFuncAttributeMaxDynamicSharedMemorySize, LDS_BYTES);
  fused_kernel<<<256, NTHR, LDS_BYTES, stream>>>(p);
}
```

```cpp
#include <hip/hip_runtime.h>
#include <hip/hip_bf16.h>

typedef unsigned short u16;
typedef unsigned long long u64;
typedef short bf16x8 __attribute__((ext_vector_type(8)));
typedef short bf16x4 __attribute__((ext_vector_type(4)));
typedef float f32x4 __attribute__((ext_vector_type(4)));
typedef float f32x2 __attribute__((ext_vector_type(2)));
typedef unsigned u32x4 __attribute__((ext_vector_type(4)));
typedef unsigned u32x2 __attribute__((ext_vector_type(2)));
typedef int i32x4 __attribute__((ext_vector_type(4)));
typedef __bf16 bf2v __attribute__((ext_vector_type(2)));
#define LAS __attribute__((address_space(3)))
#define GAS __attribute__((address_space(1)))

namespace {
constexpr int S = 2048, D = 640, F = 1536;
constexpr int NTHR = 512;
constexpr int ARING = 0;
constexpr int BRING = 65536;
constexpr int TAB = 147456;
constexpr int T_ST = TAB;
constexpr int T_CS = TAB + 1024;
constexpr int T_CP = TAB + 1344;
constexpr int T_CG = TAB + 2368;
constexpr int T_PAD = TAB + 3328;
constexpr int T_EK = TAB + 12288;
constexpr int T_BIG = TAB + 4096;
constexpr int X_TILE = ARING + 3 * 16384;
constexpr int LDS_BYTES = 163840;
constexpr unsigned W_EPOCH = 0;
constexpr unsigned W_FA = 4096, W_FB = W_FA + 2048, W_FH = W_FB + 2048, W_FP = W_FH + 2048, W_FG = W_FP + 2048;
constexpr unsigned W_CSP = 139264;
constexpr unsigned W_ST = 81920;
constexpr unsigned W_CSD = W_ST + 32768;
constexpr unsigned W_ABT = 262144;
constexpr unsigned W_H = W_ABT + 2u * 4 * 64 * 10 * 1024;
constexpr unsigned W_WDT = W_H + 32u * 96 * 2 * 1024;
constexpr unsigned W_GU = W_WDT + 8u * 48 * 5 * 1024;
constexpr unsigned W_P = W_GU + 16u * 20 * 12 * 1024;
constexpr unsigned W_END = W_P + 256u * 4 * 10 * 1024;
constexpr unsigned W_STAMP = W_END;
constexpr int AUX_SC1 = 16;
#define DRAIN_G 3
#define PRO 2
#define DRAIN_P 41
#define CODE_BYTES 32512
#define WT0 9
#define WTS 2
#define MAP 1
#define ROT (3 * (b >> 6) + ((b >> 3) & 7))
#define NT_W 1
#define NT_G 1
#define SELNT(c, f) ((c) ? f##nt : f)
#define GB 8

struct Params {
  const u16 *x, *wsr, *wsi, *wdr, *wdi, *wg, *wu, *wdn;
  u16* out;
  unsigned char* ws;
};

typedef const unsigned char* cptr;
#define DMA_BODY(POL) \
__device__ __forceinline__ void dma2##POL(cptr base, unsigned v0, unsigned v1, unsigned lds) { \
  asm volatile("s_mov_b32 m0, %3\n\ts_nop 0\n\tglobal_load_lds_dwordx4 %0, %2 " #POL "\n\tglobal_load_lds_dwordx4 %1, %2 offset:1024 " #POL \
               ::"v"(v0), "v"(v1), "s"(base), "s"(lds) : "memory"); \
} \
__device__ __forceinline__ void dma3##POL(cptr base, unsigned v0, unsigned v1, unsigned v2, unsigned lds) { \
  asm volatile("s_mov_b32 m0, %4\n\ts_nop 0\n\tglobal_load_lds_dwordx4 %0, %3 " #POL "\n\tglobal_load_lds_dwordx4 %1, %3 offset:1024 " #POL "\n\t" \
               "global_load_lds_dwordx4 %2, %3 offset:2048 " #POL ::"v"(v0), "v"(v1), "v"(v2), "s"(base), "s"(lds) : "memory"); \
} \
__device__ __forceinline__ void dma4##POL(cptr base, unsigned v0, unsigned v1, unsigned v2, unsigned v3, unsigned lds) { \
  asm volatile("s_mov_b32 m0, %5\n\ts_nop 0\n\tglobal_load_lds_dwordx4 %0, %4 " #POL "\n\tglobal_load_lds_dwordx4 %1, %4 offset:1024 " #POL "\n\t" \
               "global_load_lds_dwordx4 %2, %4 offset:2048 " #POL "\n\tglobal_load_lds_dwordx4 %3, %4 offset:3072 " #POL \
               ::"v"(v0), "v"(v1), "v"(v2), "v"(v3), "s"(base), "s"(lds) : "memory"); \
}
DMA_BODY()
DMA_BODY(nt)
DMA_BODY(sc1)
#define dma4s dma4sc1
__device__ __forceinline__ void dma1(cptr base, unsigned v, unsigned lds) {
  asm volatile("s_mov_b32 m0, %2\n\ts_nop 0\n\tglobal_load_lds_dwordx4 %0, %1" ::"v"(v), "s"(base), "s"(lds) : "memory");
}
__device__ __forceinline__ void dma4c(cptr base, unsigned v, unsigned lds) {
  asm volatile("s_mov_b32 m0, %2\n\ts_nop 0\n\tglobal_load_lds_dwordx4 %0, %1 sc1\n\tglobal_load_lds_dwordx4 %0, %1 offset:1024 sc1\n\t"
               "global_load_lds_dwordx4 %0, %1 offset:2048 sc1\n\tglobal_load_lds_dwordx4 %0, %1 offset:3072 sc1"
               ::"v"(v), "s"(base), "s"(lds) : "memory");
}
__device__ __forceinline__ void dma2c(cptr base, unsigned v, unsigned lds) {
  asm volatile("s_mov_b32 m0, %2\n\ts_nop 0\n\tglobal_load_lds_dwordx4 %0, %1 sc1\n\tglobal_load_lds_dwordx4 %0, %1 offset:1024 sc1"
               ::"v"(v), "s"(base), "s"(lds) : "memory");
}
__device__ __forceinline__ void touch(cptr base, unsigned voff, unsigned pad) {
  asm volatile("s_mov_b32 m0, %2\n\ts_nop 0\n\tglobal_load_lds_dword %0, %1" ::"v"(voff), "s"(base), "s"(pad) : "memory");
}
template <int N> __device__ __forceinline__ void wait_vm() { asm volatile("s_waitcnt vmcnt(%0)" ::"i"(N) : "memory"); }
#define SCHED0() __builtin_amdgcn_sched_barrier(0)
__device__ __forceinline__ void bar() {
  SCHED0();
  __builtin_amdgcn_s_waitcnt(0xC07F);
  asm volatile("s_barrier" ::: "memory");
  SCHED0();
}
__device__ __forceinline__ f32x4 mfma(bf16x8 a, bf16x8 b, f32x4 c) { return __builtin_amdgcn_mfma_f32_16x16x32_bf16(a, b, c, 0, 0, 0); }
__device__ __forceinline__ bf16x8 ldsr(LAS char* p) { return *(LAS bf16x8*)p; }
__device__ __forceinline__ bf16x4 ldstr(LAS char* p) { return __builtin_amdgcn_ds_read_tr16_b64_v4i16((LAS bf16x4*)p); }
__device__ __forceinline__ bf16x8 cat(bf16x4 a, bf16x4 b) { return __builtin_shufflevector(a, b, 0, 1, 2, 3, 4, 5, 6, 7); }
__device__ __forceinline__ unsigned pack2(float a, float b) {
  f32x2 v = {a, b};
  bf2v r = __builtin_convertvector(v, bf2v);
  return __builtin_bit_cast(unsigned, r);
}
__device__ __forceinline__ float asf(unsigned v) { return __builtin_bit_cast(float, v); }
__device__ __forceinline__ float bflo(unsigned v) { return __builtin_bit_cast(float, v << 16); }
__device__ __forceinline__ float bfhi(unsigned v) { return __builtin_bit_cast(float, v & 0xffff0000u); }
template <int CTRL> __device__ __forceinline__ float dpp_add(float v) {
  return v + __builtin_bit_cast(float, __builtin_amdgcn_update_dpp(0, __builtin_bit_cast(int, v), CTRL, 0xf, 0xf, true));
}
__device__ __forceinline__ float row8_sum(float v) {
  v = dpp_add<0xB1>(v); v = dpp_add<0x4E>(v); v = dpp_add<0x141>(v);
  return v;
}
__device__ __forceinline__ float dot_ones(unsigned v, float acc) {
  return __builtin_amdgcn_fdot2_f32_bf16(__builtin_bit_cast(bf2v, v), __builtin_bit_cast(bf2v, 0x3f803f80u), acc, false);
}
__device__ __forceinline__ u64 ld_flag(const unsigned char* ws, unsigned off) {
  return __hip_atomic_load((GAS u64*)(ws + off), __ATOMIC_RELAXED, __HIP_MEMORY_SCOPE_AGENT);
}
__device__ __forceinline__ u64 next_tag(u64 prev) { return prev * 6364136223846793005ull + 1442695040888963407ull; }

#define STAMP(i) do { } while (0)

struct Ids {
  int rb, cb, tt, et, kq;
};

template <int I> struct IC { static constexpr int v = I; };
template <int I, int N, class Fn> __device__ __forceinline__ void sfor(Fn&& f) {
  if constexpr (I < N) { f(IC<I>{}); sfor<I + 1, N>(f); }
}
__device__ __forceinline__ int fresh_lane() {
  int l;
  asm volatile("v_mbcnt_lo_u32_b32 %0, -1, 0\n\tv_mbcnt_hi_u32_b32 %0, -1, %0" : "=v"(l));
  return l;
}
__device__ __forceinline__ float sel(float a, float b, u64 m) {
  float o;
  asm("v_cndmask_b32_e64 %0, %1, %2, %3" : "=v"(o) : "v"(a), "v"(b), "s"(m));
  return o;
}
__device__ __forceinline__ float pick4(f32x4 v) {
  float a = v[0], b = v[1], c = v[2], d = v[3];
  asm volatile("s_nop 7" : "+v"(a), "+v"(b), "+v"(c), "+v"(d));
  return sel(sel(a, b, 0xAAAAAAAAAAAAAAAAull), sel(c, d, 0xAAAAAAAAAAAAAAAAull), 0xCCCCCCCCCCCCCCCCull);
}
#define PIN(x) asm volatile("" : "+v"(x))
template <int N>
__device__ __forceinline__ void finish(f32x4 (&dk)[N], const f32x4 (&d1)[N], f32x4 (&ys)[N], LAS char* xp) {
  f32x4 ex[N];
  u32x2 xv[N];
#pragma unroll
  for (int g = 0; g < N; ++g) xv[g] = *(volatile LAS u32x2*)(xp + g * 32);
  SCHED0();
#pragma unroll
  for (int g = 0; g < N; ++g) { dk[g] += d1[g]; ex[g] = dk[g] * -1.4426950409f; }
  SCHED0();
#pragma unroll
  for (int g = 0; g < N; ++g) {
#pragma unroll
    for (int e = 0; e < 4; ++e) ex[g][e] = __builtin_amdgcn_exp2f(ex[g][e]);
    dk[g] *= ys[g];
  }
  SCHED0();
#pragma unroll
  for (int g = 0; g < N; ++g) ex[g] += 1.f;
  SCHED0();
#pragma unroll
  for (int g = 0; g < N; ++g) {
#pragma unroll
    for (int e = 0; e < 4; ++e) ex[g][e] = __builtin_amdgcn_rcpf(ex[g][e]);
    ys[g] = f32x4{bflo(xv[g][0]), bfhi(xv[g][0]), bflo(xv[g][1]), bfhi(xv[g][1])};
  }
  SCHED0();
#pragma unroll
  for (int g = 0; g < N; ++g) {
    const f32x4 o = dk[g] * ex[g] + ys[g];
    *(volatile LAS u32x2*)(xp + g * 32) = u32x2{pack2(o[0], o[1]), pack2(o[2], o[3])};
  }
}

__device__ __forceinline__ void compute(const Params& p, LAS char* L, const Ids& id, int w) {
  int lane = fresh_lane(), r = lane & 15, q = lane >> 4;
  const __amdgpu_buffer_rsrc_t rws = __builtin_amdgcn_make_buffer_rsrc((void*)p.ws, 0, (int)W_END, 0x00020000);
  const f32x4 z4 = {0.f, 0.f, 0.f, 0.f};

  const bf16x8 ones = {0x3f80, 0x3f80, 0x3f80, 0x3f80, 0x3f80, 0x3f80, 0x3f80, 0x3f80};
  if (blockIdx.x < 240) {
    const int pc = blockIdx.x % 48, pr = blockIdx.x / 48;
    const unsigned va = (lane >> 3) * 3072 + (lane & 7) * 16;
    SELNT(NT_G, dma4)((cptr)(pc < 24 ? p.wg : p.wu) + ((pr * 128 + w * 32) * 3072 + (pc % 24) * 128), va, va + 23552, va + 2 * 23552, va + 3 * 23552,
         (unsigned)(size_t)L + BRING + (w ? w * 20480 + 10240 : 0));
    LAS char* a = L + BRING + (w ? w * 20480 + 10240 : 0) + (8 * q + (r >> 2)) * 128 + 8 * (r & 3);
    const unsigned go = lane * 16 + W_GU + (((pc % 24) * 4 * 20 + pr * 4 + w) * 2 + pc / 24) * 1024;
    wait_vm<0>();
    bf16x8 f[4];
#pragma unroll
    for (int c = 0; c < 4; ++c) f[c] = cat(ldstr(a + c * 32), ldstr(a + 512 + c * 32));
    f32x4 acc[4];
#pragma unroll
    for (int c = 0; c < 4; ++c) acc[c] = mfma(ones, f[c], z4);
    SCHED0();
#pragma unroll
    for (int c = 0; c < 4; ++c) __builtin_amdgcn_raw_buffer_store_b128(__builtin_bit_cast(u32x4, f[c]), rws, go + c * 40960, 0, AUX_SC1);
    SCHED0();
    float v01 = (q & 1) ? acc[1][0] : acc[0][0], v23 = (q & 1) ? acc[3][0] : acc[2][0];
    PIN(v01); PIN(v23);
    *(LAS float*)(L + T_CP + (w * 64 + lane) * 4) = (q & 2) ? v23 : v01;
  }

  bf16x8 xr[20][2];
  f32x4 accP[5][2], st[4];
  float cs[5];
  {
    f32x4 accS[2], accQ[2];
    bf16x8 pb[2][5];
    const int oK0 = r * 128 + ((q ^ (r >> 1)) & 7) * 16, oK1 = oK0 ^ 64;
    LAS char* xA[2] = {L + ARING + w * 4096 + oK0, L + ARING + w * 4096 + oK1};
    LAS char* pB[2] = {L + BRING + 10240 + oK0, L + BRING + 10240 + oK1};
    PIN(xA[0]); PIN(xA[1]); PIN(pB[0]); PIN(pB[1]);
    auto rdx = [&](int i, int ks, int t) { xr[2 * i + ks][t] = ldsr(xA[ks] + (i & 3) * 16384 + t * 2048); };
    auto rdw = [&](int i, int ks, int g) { pb[ks][g] = ldsr(pB[ks] + ((i & 3) ? (i & 3) * 20480 - 10240 : 0) + g * 2048); };
    auto stat = [&](int s, int k) {
      if (k < 2) accS[k] = mfma(xr[s][k], ones, s == 0 ? z4 : accS[k]);
      else accQ[k - 2] = mfma(xr[s][k - 2], xr[s][k - 2], s == 0 ? z4 : accQ[k - 2]);
    };
#pragma unroll
    for (int i = 0; i < 10; ++i) {
      if (i == DRAIN_G) wait_vm<0>();
      bar();
#pragma unroll
      for (int k = 0; k < 10; ++k) {
        if (i > 0) accP[k >> 1][k & 1] = mfma(xr[2 * i - 1][k & 1], pb[1][k >> 1], accP[k >> 1][k & 1]);
        if (k & 1) rdw(i, 0, k >> 1);
        else if (k < 8) rdx(i, k >> 2, (k >> 1) & 1);
        SCHED0();
      }
#pragma unroll
      for (int k = 0; k < 18; ++k) {
        if (k < 2) stat(2 * i, k);
        else if (k < 12) {
          accP[(k - 2) >> 1][k & 1] = mfma(xr[2 * i][k & 1], pb[0][(k - 2) >> 1], i == 0 ? z4 : accP[(k - 2) >> 1][k & 1]);
          if (k & 1) rdw(i, 1, (k - 2) >> 1);
        } else if (k < 14) stat(2 * i, k - 10);
        else stat(2 * i + 1, k - 14);
        SCHED0();
      }
    }
    lane = fresh_lane(); r = lane & 15; q = lane >> 4;
    const bool diag = (r >> 2) == q;
    LAS char* const tw = diag ? L + T_ST + (w * 32 + r) * 4 : L + T_PAD;
    LAS char* const te = diag ? L + T_EK + (w * 32 + r) * 16 : L + T_PAD;
    const float sg = id.cb >= 8 ? -1.f : 1.f;
    float sm[2], sq[2], rs[2];
#pragma unroll
    for (int k = 0; k < 10; ++k) {
      accP[k >> 1][k & 1] = mfma(xr[19][k & 1], pb[1][k >> 1], accP[k >> 1][k & 1]);
      if (k < 2) {
#pragma unroll
        for (int g = 3 * k; g < 3 * k + 3 && g < 5; ++g) cs[g] = *(LAS float*)(L + T_CS + (g * 16 + r) * 4);
      }
      if (k == 1 || k == 2) {
        const int t = k - 1;
        sm[t] = pick4(accS[t]) * (-1.0f / D); sq[t] = pick4(accQ[t]) * (1.0f / D);
        PIN(sm[t]); PIN(sq[t]);
      }
      if (k == 3) {
#pragma unroll
        for (int t = 0; t < 2; ++t) { sq[t] = fmaxf(sq[t] - sm[t] * sm[t], 0.f) + 1e-5f; PIN(sq[t]); }
#pragma unroll
        for (int t = 0; t < 2; ++t) { rs[t] = __builtin_amdgcn_rsqf(sq[t]); PIN(rs[t]); }
      }
      if (k == 4) {
#pragma unroll
        for (int t = 0; t < 2; ++t) {
          const float v = rs[t] * sg;
          *(LAS float*)(tw + t * 64) = v; *(LAS float*)(tw + 512 + t * 64) = v * sm[t];
        }
      }
      if (k == 5) {
#pragma unroll
        for (int n = 0; n < 4; ++n) st[n] = *(LAS f32x4*)(L + T_ST + (n >> 1) * 512 + (w * 32 + (n & 1) * 16 + 4 * q) * 4);
      }
      if (k == 6 || k == 7) {
        const int t = k - 6;
        *(LAS f32x4*)(te + t * 256) = f32x4{sm[t], -2.302208198f * rs[t], -0.1029432397f * rs[t] * rs[t] * rs[t], sq[t]};
      }
      SCHED0();
    }
  }
  LAS char* const fB0 = L + BRING + lane * 16;
  LAS char* const fB3 = L + BRING + 61440 + lane * 16;
  auto fragB = [&](int stage, int n) -> bf16x8 { return (stage & 3) == 3 ? ldsr(fB3 + n * 1024) : ldsr(fB0 + (stage & 3) * 20480 + n * 1024); };
  LAS char* const xB0 = L + ARING + w * 4096 + r * 128 + ((q ^ (r >> 1)) & 7) * 16;
  LAS char* const xB1 = L + ARING + w * 4096 + ((r * 128 + ((q ^ (r >> 1)) & 7) * 16) ^ 64);
  const unsigned abBase = W_ABT + (unsigned)((((id.cb >> 3) * 4 + ((id.cb & 7) >> 1)) * 64 + id.rb * 4 + w) * 10 + (id.cb & 1) * 5) * 1024u + lane * 16;

  u32x4 abo;
  f32x4 accH[2][4];
  bf16x8 wb[4][2];
  const unsigned hbase = W_H + (unsigned)(((id.rb * 2 + (w >> 1)) * 96 + id.cb * 6) * 2 + (w & 1)) * 1024u + lane * 16;
  float gv[8], uv[8], wv[8];
  f32x4 cgv, cuv, ek[2];
  auto h_tab = [&](int c) {
    cgv = *(LAS f32x4*)(L + T_CG + (c * 16 + 4 * q) * 4);
    cuv = *(LAS f32x4*)(L + T_CG + 384 + (c * 16 + 4 * q) * 4);
  };
  auto h_op = [&](int c, int n) {
    const int ph = n >> 3, x = n & 7, t = x >> 2, e = x & 3;
    if (ph == 0) { gv[x] = accH[c & 1][t][e] + ek[t][0] * cgv[e]; PIN(gv[x]); uv[x] = accH[c & 1][2 + t][e] + ek[t][0] * cuv[e]; PIN(uv[x]); }
    if (ph == 1) { wv[x] = gv[x] * (ek[t][1] + ek[t][2] * (gv[x] * gv[x])); PIN(wv[x]); }
    if (ph == 2) { wv[x] = __builtin_amdgcn_exp2f(wv[x]); PIN(wv[x]); uv[x] *= gv[x]; PIN(uv[x]); }
    if (ph == 3) { wv[x] = __builtin_amdgcn_rcpf(wv[x] * ek[t][3] + ek[t][3]); PIN(wv[x]); }
    if (ph == 4) {
      uv[x] *= wv[x]; PIN(uv[x]);
      if (x == 7) {
        u32x4 o = {pack2(uv[0], uv[1]), pack2(uv[2], uv[3]), pack2(uv[4], uv[5]), pack2(uv[6], uv[7])};
        __builtin_amdgcn_raw_buffer_store_b128(o, rws, hbase + c * 2048, 0, AUX_SC1);
      }
    }
  };
  const int wm = w >> 1, wn = w & 1;
  LAS char *aBase0, *aBase1, *yBase;
  bf16x8 xa0[4], xa1[4], yb0[5], yb1[5];
  auto rdg = [&](int g, int m) { wb[g & 3][m] = fragB(10 + g / 10, (g % 10) * 2 + m); };
  bar();
#pragma unroll
  for (int g = 0; g < 3; ++g) { rdg(g, 0); rdg(g, 1); }
  sfor<0, 120>([&](auto gi) __attribute__((always_inline)) {
    constexpr int g = decltype(gi)::v, c = g / 20, s = g % 20;
    if (g == 110) {
      const int l2 = fresh_lane(), r2 = l2 & 15, oK0 = r2 * 128 + (((l2 >> 4) ^ (r2 >> 1)) & 7) * 16;
      aBase0 = L + ARING + wm * 8192 + oK0; aBase1 = L + ARING + wm * 8192 + (oK0 ^ 64); yBase = L + BRING + wn * 5120 + l2 * 16;
    }
    if (g % 10 == 8) {
      if (g == 38) wait_vm<0>();
      bar();
    }
#pragma unroll
    for (int k = 0; k < 4; ++k) {
      accH[c & 1][k] = mfma(wb[g & 3][k >> 1], xr[s][k & 1], s == 0 ? z4 : accH[c & 1][k]);
      bool rd = true;
      if (g >= 118) {
        const int n = (g - 118) * 4 + k;
        if (n < 4) xa0[n] = ldsr(aBase0 + 2 * 16384 + n * 2048); else yb0[n - 4] = ldsr(yBase + 2 * 20480 + (n - 4) * 1024);
      } else if (g % 10 == 8) rdg(g + 2 + (k >> 1), k & 1);
      else if (g % 10 != 7 && k < 2) rdg(g + 3, k);
      else rd = false;
      if (!rd) {
        const int n = s % 10 == 7 ? 2 * s + k : 2 * s + k - 2;
        if (c == 0) {
          if (n < 25) {
            const int gg = n / 5, ph = n % 5;
            if (ph < 4) {
              const int t = ph >> 1, e = (ph & 1) * 2;
              unsigned pk = pack2(accP[gg][t][e] * st[t][e] + st[2 + t][e] * cs[gg], accP[gg][t][e + 1] * st[t][e + 1] + st[2 + t][e + 1] * cs[gg]);
              PIN(pk);
              abo[ph] = pk;
            } else __builtin_amdgcn_raw_buffer_store_b128(abo, rws, abBase + gg * 1024, 0, AUX_SC1);
          }
          if (n >= 10 && n < 26 && !(n & 1)) {
            const int m = (n - 10) >> 1;
            xr[14 + (m >> 1)][m & 1] = ldsr(((m & 2) ? xB1 : xB0) + ((7 + (m >> 2)) & 3) * 16384 + (m & 1) * 2048);
          }
          if (n == 34) ek[0] = *(LAS f32x4*)(L + T_EK + (w * 32 + r) * 16);
          if (n == 35) ek[1] = *(LAS f32x4*)(L + T_EK + (w * 32 + 16 + r) * 16);
        } else if (c < 5) h_op(c - 1, n);
        else if (n < 36) { h_op(4, n); if (n >= 32) h_op(4, n + 4); }
        if (n == (c == 0 ? 38 : 36)) h_tab(c);
      }
      SCHED0();
    }
  });

  f32x4 accY[5][4];
  auto y_stage = [&](int i, int h0) {
    LAS char* a0 = aBase0 + ((i + 2) & 3) * 16384; LAS char* a1 = aBase1 + ((i + 2) & 3) * 16384;
    LAS char* b = yBase + ((i + 2) & 3) * 20480;
    PIN(a0); PIN(a1); PIN(b);
    bar();
    int n = h0;
#pragma unroll
    for (int k = 0; k < 20; ++k) {
      accY[k >> 2][k & 3] = mfma(yb1[k >> 2], xa1[k & 3], accY[k >> 2][k & 3]);
      if (k < 4) xa0[k] = ldsr(a0 + k * 2048);
      else if (k < 9) yb0[k - 4] = ldsr(b + (k - 4) * 1024);
      else if (k < 11) yb1[k - 9] = ldsr(b + 10240 + (k - 9) * 1024);
      else if (k == 12) yb1[2] = ldsr(b + 10240 + 2048);
      else if (k == 16) yb1[3] = ldsr(b + 10240 + 3072);
      else if (n < 40) h_op(5, n++);
      SCHED0();
    }
#pragma unroll
    for (int k = 0; k < 20; ++k) {
      accY[k >> 2][k & 3] = mfma(yb0[k >> 2], xa0[k & 3], accY[k >> 2][k & 3]);
      if (k == 0) yb1[4] = ldsr(b + 10240 + 4096);
      else if (k < 5) xa1[k - 1] = ldsr(a1 + (k - 1) * 2048);
      else if (n < 40) h_op(5, n++);
      SCHED0();
    }
  };
#pragma unroll
  for (int k = 0; k < 20; ++k) {
    if (k == 0) yb0[4] = ldsr(yBase + 2 * 20480 + 4096);
    accY[k >> 2][k & 3] = mfma(yb0[k >> 2], xa0[k & 3], z4);
    if (k < 4) xa1[k] = ldsr(aBase1 + 2 * 16384 + k * 2048);
    else if (k < 9) yb1[k - 4] = ldsr(yBase + 2 * 20480 + 10240 + (k - 4) * 1024);
    else h_op(5, k - 9);
    SCHED0();
  }
  y_stage(1, 11); y_stage(2, 33);
#pragma nounroll
  for (int i = 3; i < 16; ++i) {
    if (i == 5) wait_vm<0>();
    y_stage(i, 40);
  }

  lane = fresh_lane(); r = lane & 15; q = lane >> 4;
  const int tile = id.tt * 4 + id.et;
  f32x4 accD[5][2];
#pragma unroll
  for (int g = 0; g < 5; ++g) { accD[g][0] = z4; accD[g][1] = z4; }
  LAS char* const hB = L + ARING + wm * 1024 + wn * 8192 + lane * 16 + wn * 8;
  LAS char* const dB = fB0 + wn * 10240;
  bf16x8 hh[2][2], dw[2][5];
  auto d_rd = [&](LAS char* a, LAS char* a2, LAS char* b, int ks, int m) {
    if (m < 2) hh[ks][m] = cat(*(volatile LAS bf16x4*)((m ? a2 : a) + ks * 4096), *(volatile LAS bf16x4*)((m ? a2 : a) + ks * 4096 + 2048));
    else dw[ks][m - 2] = ldsr(b + ks * 5120 + (m - 2) * 1024);
  };
  auto d_mm = [&](int ks, int k) { accD[k >> 1][k & 1] = mfma(dw[ks][k >> 1], hh[ks][k & 1], accD[k >> 1][k & 1]); };
  const unsigned pbase = W_P + (unsigned)(((tile * 4 + w) * 4 + id.kq) * 10) * 1024u + lane * 16;
  auto p_store = [&](int u) {
    u32x4 o[5];
#pragma unroll
    for (int g = 0; g < 5; ++g) {
      const f32x4 a = accY[g][2 * u], b = accY[g][2 * u + 1];
      o[g] = u32x4{pack2(a[0], a[1]), pack2(a[2], a[3]), pack2(b[0], b[1]), pack2(b[2], b[3])};
    }
    if (w != id.kq) {
#pragma unroll
      for (int g = 0; g < 5; ++g) __builtin_amdgcn_raw_buffer_store_b128(o[g], rws, pbase + (u * 5 + g) * 1024, 0, AUX_SC1);
    } else {
#pragma unroll
      for (int g = 0; g < 5; ++g) *(LAS u32x4*)(L + T_BIG + (u * 5 + g) * 1024 + lane * 16) = o[g];
    }
  };
  auto d_second = [&](LAS char* a, LAS char* a2, LAS char* b) {
#pragma unroll
    for (int k = 0; k < 10; ++k) {
      d_mm(0, k);
      if (k < 2) d_rd(a, a2, b, 1, k); else if (k < 4) d_rd(a, a2, b, 1, k + 3);
      SCHED0();
    }
  };
  auto d_stage = [&](int i) {
    LAS char* a = hB + ((i + 2) & 3) * 16384; LAS char* a2 = a + (8 - 16 * wn); LAS char* b = dB + ((i + 2) & 3) * 20480;
    PIN(a); PIN(a2); PIN(b);
    bar();
#pragma unroll
    for (int k = 0; k < 10; ++k) {
      d_mm(1, k);
      if (k < 7) d_rd(a, a2, b, 0, k); else d_rd(a, a2, b, 1, k - 5);
      SCHED0();
    }
    d_second(a, a2, b);
  };
  {
    LAS char* a = hB + 2 * 16384; LAS char* a2 = a + (8 - 16 * wn); LAS char* b = dB + 2 * 20480;
    PIN(a); PIN(a2); PIN(b);
    bar();
#pragma unroll
    for (int k = 0; k < 20; ++k) {
      accY[k >> 2][k & 3] = mfma(yb1[k >> 2], xa1[k & 3], accY[k >> 2][k & 3]);
      if (k < 7) d_rd(a, a2, b, 0, k); else if (k < 10) d_rd(a, a2, b, 1, k - 5);
      SCHED0();
    }
    d_second(a, a2, b);
  }
  p_store(0);
  p_store(1);
#pragma nounroll
  for (int i = 1; i < 12; ++i) {
    if (i == DRAIN_P - 38) wait_vm<0>();
    d_stage(i);
  }
#pragma unroll
  for (int k = 0; k < 10; ++k) {
    d_mm(1, k < 5 ? 2 * k + 1 : 2 * (9 - k));
    if (k >= 5) *(LAS f32x4*)(fB0 + ((w ^ 1) * 5 + k - 5) * 1024) = accD[k - 5][1];
    SCHED0();
  }
  LAS char* dp = fB0 + w * 5120;
  *(LAS f32x4*)(L + ARING + w * 2048 + 1024 + lane * 16) = accD[4][0];
  *(LAS f32x4*)(L + ARING + w * 2048 + lane * 16) = accD[3][0];
  LAS char* xp = L + X_TILE + (wm * 32 + 8 * (r >> 2) + 4 * wn + (r & 3)) * 160 + q * 8;
  PIN(xp); PIN(dp);
  bar();
  f32x4 dk[3], ys[3];
#pragma unroll
  for (int g = 0; g < 3; ++g) dk[g] = *(LAS f32x4*)(dp + g * 1024);
#pragma unroll
  for (int g = 0; g < 3; ++g) ys[g] = *(LAS f32x4*)(dp + 61440 + g * 1024);
  const f32x4 own[3] = {accD[0][0], accD[1][0], accD[2][0]};
  finish<3>(dk, own, ys, xp);
}

__device__ __forceinline__ void aux_tail(const Params& p, LAS char* L, int t, int lane) {
  const int po = (t >> 1) * 5120 + lane * 16 + (t & 1) * 8;
  u32x2 y[4][5];
#pragma unroll
  for (int g = 0; g < 5; ++g) {
    y[0][g] = *(volatile LAS u32x2*)(L + T_BIG + po + g * 1024);
    y[1][g] = *(volatile LAS u32x2*)(L + BRING + 2 * 20480 + po + g * 1024);
    y[2][g] = *(volatile LAS u32x2*)(L + BRING + 2 * 20480 + 10240 + po + g * 1024);
    y[3][g] = *(volatile LAS u32x2*)(L + ARING + 2 * 16384 + po + g * 1024);
  }
  SCHED0();
  LAS char* dp = L + ARING + t * 2048 + lane * 16;
  f32x4 ys[2];
#pragma unroll
  for (int g = 0; g < 5; ++g) {
    f32x4 v[4];
#pragma unroll
    for (int k = 0; k < 4; ++k) v[k] = f32x4{bflo(y[k][g][0]), bfhi(y[k][g][0]), bflo(y[k][g][1]), bfhi(y[k][g][1])};
    const f32x4 sum = (v[0] + v[1]) + (v[2] + v[3]);
    if (g < 3) *(LAS f32x4*)(L + BRING + 61440 + (t * 5 + g) * 1024 + lane * 16) = sum;
    else ys[g - 3] = sum;
  }
  const int r = lane & 15;
  LAS char* xp = L + X_TILE + ((t >> 1) * 32 + 8 * (r >> 2) + 4 * (t & 1) + (r & 3)) * 160 + (lane >> 4) * 8 + 96;
  LAS char* dq = L + BRING + t * 5120 + 3072 + lane * 16;
  PIN(xp); PIN(dp); PIN(dq); PIN(ys[0]); PIN(ys[1]);
  wait_vm<0>();
  bar();
  if (t == 3) { STAMP(50); }
  f32x4 dk[2], own[2];
#pragma unroll
  for (int g = 0; g < 2; ++g) { dk[g] = *(LAS f32x4*)(dq + g * 1024); own[g] = *(LAS f32x4*)(dp + g * 1024); }
  finish<2>(dk, own, ys, xp);
}

__device__ __forceinline__ void st_flag(unsigned char* ws, unsigned off, u64 v, int lane) {
  if (lane == 0) __hip_atomic_store((GAS u64*)(ws + off), v, __ATOMIC_RELAXED, __HIP_MEMORY_SCOPE_AGENT);
}
__device__ __forceinline__ void wait_flag(const unsigned char* ws, unsigned off, u64 tag, u64 seen) {
  if (!__all(seen == tag)) {
#pragma nounroll
    for (int spin = 0; spin < (1 << 16); ++spin)
      if (__all(ld_flag(ws, off) == tag)) break;
  }
  __builtin_amdgcn_fence(__ATOMIC_ACQUIRE, "wavefront");
}
template <int N, class Fn>
__device__ __forceinline__ void wait_tags(u32x4 (&g)[N], unsigned tlo, unsigned thi, Fn&& reload) {
  auto good = [&]() {
    unsigned bad = 0;
#pragma unroll
    for (int k = 0; k < N; ++k) bad |= (g[k][1] ^ tlo) | (g[k][3] ^ thi);
    return __all(bad == 0);
  };
  if (!good()) {
#pragma nounroll
    for (int spin = 0; spin < (1 << 16); ++spin) {
      reload();
      if (good()) break;
    }
  }
}
__device__ __forceinline__ void loader(const Params& p, LAS char* L, unsigned lb, const Ids& id, int ld, int lane) {
  const int b = blockIdx.x;
  const unsigned vl = lane * 16;
  const unsigned c0 = ((lane & 7) ^ (lane >> 4)) * 16, c1 = c0 ^ 64;
  const int rl = 8 * (lane >> 5) + ((lane >> 3) & 3);
  const int rot = ROT;
  {
    const unsigned vd0 = (lane >> 3) * 1280 + c0, vd1 = (lane >> 3) * 1280 + c1 + 9216;
    const unsigned v0 = ld < 2 ? rl * 1280 + c0 : vd0, v1 = ld < 2 ? (rl + 16) * 1280 + c1 - 1024 : vd1,
                   v2 = ld < 2 ? (rl + 4) * 1280 + c0 - 2048 : vd0 + 2 * 9216, v3 = ld < 2 ? (rl + 20) * 1280 + c1 - 3072 : vd1 + 2 * 9216;
    const cptr bd = (cptr)(id.cb < 8 ? p.wdr : p.wdi) + (id.cb & 7) * 80 * 1280;
    const cptr b1 = ld < 2 ? (cptr)p.x + (id.rb * 128 + ld * 64) * 1280 : bd;
    int kc = rot % 10;
#pragma nounroll
    for (int j = 0; j < 10; ++j) {
      if (j == 2 || j == 3) { if (ld == 2) wait_vm<10>(); else wait_vm<8>(); bar(); }
      if (j > 4) { if (ld == 2) wait_vm<20>(); else wait_vm<16>(); bar(); }
      const unsigned lw = lb + BRING + ((j & 3) ? (j & 3) * 20480 : 10240);
      const unsigned l = ld < 2 ? lb + ARING + (j & 3) * 16384 + ld * 8192 : lw;
      dma4(b1 + kc * 128, v0, v1, v2, v3, l);
      dma4(b1 + (kc * 128 + 32 * 1280), v0, v1, v2, v3, l + 4096);
      if (ld == 2) dma2(b1 + (kc * 128 + 64 * 1280), v0, v1, l + 8192);
      kc = kc == 9 ? 0 : kc + 1;
    }
    wait_vm<16>(); bar();
  }
  const unsigned vw0 = rl * 4096 + c0, vw1 = (rl + 16) * 4096 + c1 - 1024, vw2 = (rl + 4) * 4096 + c0 - 2048, vw3 = (rl + 20) * 4096 + c1 - 3072;
  const unsigned u0 = ld ? vl : vw0, u1 = ld ? vl : vw1, u2 = ld ? vl : vw2, u3 = ld ? vl : vw3;
  const cptr bw = (cptr)(id.kq < 2 ? p.wsr : p.wsi) + (id.tt * 128 * 4096 + (id.kq & 1) * 2048);
  const int g0 = ld ? 2 * ld - 2 : 4;
  const unsigned l0 = lb + BRING + g0 * 4096, l1 = ld ? l0 + 4096 : lb + ARING, ls1 = ld ? 20480 : 16384, l2 = lb + ARING + (ld + 1) * 4096;
  auto yk = [&](int y) { return (y & ~3) | ((y + id.et) & 3); };
  {
    const cptr bg = p.ws + W_GU + (unsigned)id.cb * 245760u;
    const int ka[2] = {((g0 + rot) % 10) * 4096, ((g0 + rot + 5) % 10) * 4096}, kb[2] = {((g0 + 1 + rot) % 10) * 4096, ((g0 + rot + 6) % 10) * 4096};
#pragma nounroll
    for (int j = 10; j < 22; ++j) {
      if (j != 11) {
        if (j <= 12 || ld == 0) wait_vm<8>(); else wait_vm<16>();
        bar();
      }
      const cptr so = bg + ((j - 10) >> 1) * 40960;
      dma4c(so + ((j & 1) ? ka[1] : ka[0]), vl, l0 + (j & 3) * 20480);
      if (ld) dma4c(so + ((j & 1) ? kb[1] : kb[0]), vl, l1 + (j & 3) * 20480);
      const int n = (j - 16) * 3 + ld;
      if (n >= 0 && n < 16) dma4(bw + (yk(n >> 2) * 128 + (n & 3) * 32 * 4096), vw0, vw1, vw2, vw3, lb + ARING + (((n >> 2) + 2) & 3) * 16384 + (n & 3) * 4096);
    }
  }
  const int c80 = id.et * 2 + (id.kq & 1);
  const cptr bh = p.ws + W_H + (unsigned)(id.tt * 2 + (id.kq >> 1)) * 196608u;
  const cptr bab = p.ws + W_ABT + ((unsigned)(((id.kq >> 1) * 4 + id.et) * 64 + (id.kq & 1) * 32) * 10240u + g0 * 4096);
  const cptr bwd = p.ws + W_WDT + (unsigned)c80 * 245760u;
  const cptr y1 = ld ? bab + 4096 : bw, y2 = bw + (ld + 1) * 32 * 4096;
  const int ym1 = ld ? 20480 : 128;
  const cptr d0 = bwd + g0 * 4096, d1 = ld ? d0 + 4096 : bh, d2 = bh + (ld + 1) * 4096;
#pragma nounroll
  for (int i = 19; i < 35; ++i) {
    if (i >= 26) wait_vm<24>(); else if (ld == 0) wait_vm<8>(); else wait_vm<16>();
    bar();
    const int k = yk(i - 19), sl = (i + 3) & 3;
    dma4c(bab + k * 20480, vl, l0 + sl * 20480);
    if (i >= 23 || ld) dma4s(y1 + k * ym1, u0, u1, u2, u3, l1 + sl * ls1);
    if (i >= 23) dma4(y2 + k * 128, vw0, vw1, vw2, vw3, l2 + sl * 16384);
  }
#pragma nounroll
  for (int i = 35; i < 47; ++i) {
    wait_vm<24>();
    bar();
    const int sl = (i + 3) & 3;
    dma4c(d0 + (i - 35) * 20480, vl, l0 + sl * 20480);
    dma4c(d1 + (i - 35) * ls1, vl, l1 + sl * ls1);
    dma4c(d2 + (i - 35) * 16384, vl, l2 + sl * 16384);
  }
  wait_vm<24>(); bar();
  const cptr bp = p.ws + W_P + ((unsigned)(((id.tt * 4 + id.et) * 4 + id.kq) * 4 * 10) * 1024u + ((id.kq + 1 + ld) & 3) * 10240);
  const unsigned l = lb + (ld == 0 ? ARING + 2 * 16384 : BRING + 2 * 20480 + (ld - 1) * 10240);
  dma4c(bp, vl, l); dma4c(bp + 4096, vl, l + 4096); dma2c(bp + 8192, vl, l + 8192);
}

__device__ __forceinline__ void helper(const Params& p, LAS char* L, unsigned lb, const Ids& id, int lane, u64 pc) {
  const int b = blockIdx.x;
  const __amdgpu_buffer_rsrc_t rws = __builtin_amdgcn_make_buffer_rsrc((void*)p.ws, 0, (int)W_END, 0x00020000);
  STAMP(60);
  __builtin_amdgcn_s_setprio(3);
  const u64 prev = ld_flag(p.ws, W_EPOCH);
  const int r8 = lane >> 3;
  u32x4 dv[10];
  const GAS char* ds = (const GAS char*)(b < 128 ? p.wdr : p.wdi) + (((b & 127) * 5 + (r8 < 5 ? r8 : 4)) * 1280 + (lane & 7) * 16);
#pragma unroll
  for (int i = 0; i < 10; ++i) dv[i] = *(const GAS u32x4*)(ds + i * 128);
  auto code_touch = [&]() {
    const unsigned off = (((b >> 3) % ((CODE_BYTES + 8191) / 8192)) * 64 + lane) * 128;
    touch((cptr)(pc & ~255ull), off < CODE_BYTES ? off : 0, lb + T_PAD);
  };
  code_touch();
  const u64 tag = next_tag(prev);
  const unsigned tlo = (unsigned)tag, thi = (unsigned)(tag >> 32);
  {
    float t4[4] = {0.f, 0.f, 0.f, 0.f};
#pragma unroll
    for (int i = 0; i < 10; ++i)
#pragma unroll
      for (int e = 0; e < 4; ++e) t4[e] = dot_ones(dv[i][e], t4[e]);
    const unsigned sm = __builtin_bit_cast(unsigned, row8_sum((t4[0] + t4[1]) + (t4[2] + t4[3])));
    const u32x4 o = {sm, tlo, sm, thi};
    if ((lane & 7) == 0 && r8 < 5) __builtin_amdgcn_raw_buffer_store_b128(o, rws, W_CSD + (b * 5 + r8) * 16, 0, AUX_SC1);
  }
  const int pcl = (id.cb * 3) >> 1, tile = id.tt * 4 + id.et, c80 = id.et * 2 + (id.kq & 1);
  const int wk = b / 5, wc = b % 5;
  u64 f0 = 0, f1 = 0, f2 = 0;
  unsigned fo0 = 0, fo1 = 0, fo2 = 0;
  u32x4 gr[2], cq[10];
  const int go = lane < 40 ? lane : 0;
  auto ld_gr = [&]() {
    gr[0] = __builtin_amdgcn_raw_buffer_load_b128(rws, W_CSD + (id.cb * 80 + go) * 16, 0, AUX_SC1);
    gr[1] = __builtin_amdgcn_raw_buffer_load_b128(rws, W_CSD + (id.cb * 80 + 40 + go) * 16, 0, AUX_SC1);
  };
  auto ld_cq = [&]() {
#pragma unroll
    for (int k = 0; k < 10; ++k) cq[k] = __builtin_amdgcn_raw_buffer_load_b128(rws, W_CSP + ((k * 24 + pcl) * 32 + lane) * 16, 0, AUX_SC1);
  };
  const cptr wsb = (cptr)(id.kq < 2 ? p.wsr : p.wsi) + (id.tt * 128 * 4096 + (id.kq & 1) * 2048 + id.et * 128);
  sfor<0, 48>([&](auto ic) __attribute__((always_inline)) {
    constexpr int i = decltype(ic)::v;
    if constexpr (i == 8) wait_flag(p.ws, fo0, tag, f0);
    if constexpr (i == 35) { wait_flag(p.ws, fo1, tag, f1); wait_flag(p.ws, fo2, tag, f2); }
    if constexpr (i == 19 || i == 35 || i == 47) wait_flag(p.ws, fo0, tag, f0);
    bar();
    STAMP(i);
    if constexpr (i == 0) {
      if (b < 240 && lane < 32) {
        LAS float* cp = (LAS float*)(L + T_CP + lane * 4);
        const float u0 = (cp[0] + cp[64]) + (cp[128] + cp[192]), u1 = (cp[32] + cp[96]) + (cp[160] + cp[224]);
        const u32x4 o = {__builtin_bit_cast(unsigned, u0), tlo, __builtin_bit_cast(unsigned, u1), thi};
        __builtin_amdgcn_raw_buffer_store_b128(o, rws, W_CSP + (b * 32 + lane) * 16, 0, AUX_SC1);
      }
    }
    if constexpr (i == DRAIN_G) st_flag(p.ws, W_FG + b * 8, tag, lane);
    if constexpr (i == 5) ld_gr();
    if constexpr (i == 6) {
      const int l20 = lane < 20 ? lane : 0;
      fo0 = W_FG + ((l20 >> 1) * 24 + pcl + (l20 & 1)) * 8;
      f0 = ld_flag(p.ws, fo0);
    }
    if constexpr (i == 8) {
      wait_tags<2>(gr, tlo, thi, ld_gr);
      *(LAS unsigned*)(L + T_CS + go * 4) = gr[0][0];
      *(LAS unsigned*)(L + T_CS + (40 + go) * 4) = gr[1][0];
    }
    if constexpr (i == 8) ld_cq();
    if constexpr (i == 12) {
      if (b < 240) {
        const cptr sn = (cptr)p.wdn + (wk * 32 * 1280 + wc * 256);
        const unsigned vn = (lane >> 4) * 1280 + (lane & 15) * 16;
        SELNT(NT_W, dma4)(sn, vn, vn + 4096, vn + 2 * 4096, vn + 3 * 4096, lb + T_BIG);
        SELNT(NT_W, dma4)(sn + 16 * 1280, vn, vn + 4096, vn + 2 * 4096, vn + 3 * 4096, lb + T_BIG + 4096);
      }
    }
    if constexpr (i == 10) wait_tags<10>(cq, tlo, thi, ld_cq);
    if constexpr (i == 11) {
#pragma unroll
      for (int m = 0; m < 2; ++m)
#pragma unroll
        for (int h = 0; h < 2; ++h) {
          const unsigned col = (lane >> 5) * 64 + h * 32 + (lane & 31) - (id.cb & 1) * 32;
          const float v = ((asf(cq[m][2 * h]) + asf(cq[2 + m][2 * h])) + (asf(cq[4 + m][2 * h]) + asf(cq[6 + m][2 * h]))) + asf(cq[8 + m][2 * h]);
          *(LAS float*)(col < 96 ? L + T_CG + (m * 96 + col) * 4 : L + T_PAD) = v;
        }
    }
    if constexpr (i >= 19 && i < 23) {
      if (b < 240) {
        LAS char* a = L + T_BIG + (4 * (lane >> 4) + ((lane & 15) >> 2)) * 256 + 8 * (lane & 3);
#pragma unroll
        for (int c = (i - 19) * 2; c < (i - 19) * 2 + 2; ++c) {
          const bf16x8 f = cat(ldstr(a + c * 32), ldstr(a + 4096 + c * 32));
          const int eg = wc * 8 + c, cc = eg / 5, g5 = eg - cc * 5;
          __builtin_amdgcn_raw_buffer_store_b128(__builtin_bit_cast(u32x4, f), rws, lane * 16 + W_WDT + ((cc * 48 + wk) * 5 + g5) * 1024, 0, AUX_SC1);
        }
      }
    }
    if constexpr (i == 14) st_flag(p.ws, W_FB + (id.rb * 16 + id.cb) * 8, tag, lane);
    if constexpr (i == 16) code_touch();
    if constexpr (i == 26) { wait_vm<0>(); st_flag(p.ws, W_FA + b * 8, tag, lane); }
    if constexpr (i == 17) {
      fo0 = W_FB + ((8 * (id.kq & 1) + (lane & 7)) * 16 + 8 * (id.kq >> 1) + 2 * id.et + ((lane >> 3) & 1)) * 8;
      f0 = ld_flag(p.ws, fo0);
    }
    if constexpr (i == 31) {
      fo1 = W_FA + ((lane < 48 ? lane : 0) * 5 + (c80 * 5) / 8) * 8;
      fo2 = W_FA + ((lane < 48 ? lane : 0) * 5 + (c80 * 5 + 4) / 8) * 8;
      f1 = ld_flag(p.ws, fo1); f2 = ld_flag(p.ws, fo2);
    }
    if constexpr (i == 27) st_flag(p.ws, W_FH + (id.rb * 16 + id.cb) * 8, tag, lane);
    if constexpr (i == 30) code_touch();
    if constexpr (i == 32) { fo0 = W_FH + (id.tt * 16 + (lane & 15)) * 8; f0 = ld_flag(p.ws, fo0); }
    if constexpr (i == 35) {
      gr[0] = __builtin_amdgcn_raw_buffer_load_b128(rws, W_FA + lane * 32, 0, AUX_SC1);
      gr[1] = __builtin_amdgcn_raw_buffer_load_b128(rws, W_FA + lane * 32 + 16, 0, AUX_SC1);
    }
    if constexpr (i == 37) {
      const bool all = __all(((gr[0][0] ^ tlo) | (gr[0][1] ^ thi) | (gr[0][2] ^ tlo) | (gr[0][3] ^ thi) | (gr[1][0] ^ tlo) | (gr[1][1] ^ thi) | (gr[1][2] ^ tlo) | (gr[1][3] ^ thi)) == 0);
      if (b == 0) {
        if (!all) {
#pragma nounroll
          for (int k = 0; k < 4; ++k) wait_flag(p.ws, W_FA + (k * 64 + lane) * 8, tag, ~tag);
        }
        st_flag(p.ws, W_EPOCH, tag, lane);
      }
    }
    if constexpr (i == DRAIN_P) st_flag(p.ws, W_FP + (tile * 4 + id.kq) * 8, tag, lane);
    if constexpr (i == 45) { fo0 = W_FP + (tile * 4 + (lane & 3)) * 8; f0 = ld_flag(p.ws, fo0); }
    if constexpr (WT0 >= 0 && i >= WT0 && i < WT0 + 8 * WTS && (i - WT0) % WTS == 0) {
      constexpr int n = (i - WT0) / WTS;
      touch(wsb + ((n & 1) * 64 * 4096 + (n >> 1) * 512), lane * 4096, lb + T_PAD);
    }
  });
  __builtin_amdgcn_s_waitcnt(0x0F70);
}

__global__ __launch_bounds__(NTHR, 2) void fused_kernel(Params p) {
  extern __shared__ __attribute__((aligned(16))) char smem[];
  u64 pc;
  asm volatile("s_getpc_b64 %0" : "=s"(pc));
  LAS char* L = (LAS char*)smem;
  const unsigned lb = (unsigned)(size_t)L;
  const int lane = threadIdx.x & 63, w = __builtin_amdgcn_readfirstlane(threadIdx.x >> 6);
  const int b = blockIdx.x, g = b & 7, rr = b >> 3;
  Ids id;
  id.rb = (g >> 1) * 4 + (rr >> 3);
  id.cb = (g & 1) * 8 + (rr & 7);
  id.tt = (g & 1) * 8 + (rr & 7);
  id.et = rr >> 3;
  id.kq = g >> 1;
  if (w >= 4) {
    const int ld = w - 4;
    if (ld < 3) loader(p, L, lb, id, ld, lane);
    else helper(p, L, lb, id, lane, pc);
    const int n0 = ld * 64 + lane, n1 = n0 + 256, n2 = ld < 2 ? n0 + 512 : n0;
    unsigned x0 = (n0 / 10) * 1120 + n0 * 16, x1 = (n1 / 10) * 1120 + n1 * 16, x2 = (n2 / 10) * 1120 + n2 * 16;
    PIN(x0); PIN(x1); PIN(x2);
    const cptr so = (cptr)p.x + ((id.tt * 128 + (id.kq >> 1) * 64) * 1280 + (id.et * 2 + (id.kq & 1)) * 160);
    wait_vm<22>();
    bar();
    if (w == 7) { STAMP(48); }
    dma1(so, x0, lb + X_TILE + ld * 1024); dma1(so, x1, lb + X_TILE + (ld + 4) * 1024); dma1(so, x2, lb + X_TILE + (ld < 2 ? ld + 8 : ld) * 1024);
    wait_vm<3>();
    bar();
    if (w == 7) { STAMP(49); }
    aux_tail(p, L, ld, lane);
  } else compute(p, L, id, w);
  const int tid = w * 64 + fresh_lane();
  GAS char* const ob = (GAS char*)p.out + (size_t)(id.tt * 128 + (id.kq >> 1) * 64) * 1280 + (id.et * 160 + (id.kq & 1) * 80) * 2;
  unsigned o0 = (tid / 10) * 1120 + tid * 16, o1 = ((tid + 512) / 10) * 1120 + (tid + 512) * 16;
  PIN(o0); PIN(o1);
  bar();
  const u32x4 v0 = *(LAS u32x4*)(L + X_TILE + tid * 16);
  if (w < 2) {
    const u32x4 v1 = *(LAS u32x4*)(L + X_TILE + 8192 + tid * 16);
    *(GAS u32x4*)(ob + o1) = v1;
  }
  *(GAS u32x4*)(ob + o0) = v0;
}
}

extern "C" void kernel_launch(void* const* d_in, const int* in_sizes, int n_in, void* d_out, int out_size, void* d_ws, size_t ws_size,
                              hipStream_t stream) {
  Params p;
  p.x = (const u16*)d_in[0];
  p.wsr = (const u16*)d_in[1];
  p.wsi = (const u16*)d_in[2];
  p.wdr = (const u16*)d_in[3];
  p.wdi = (const u16*)d_in[4];
  p.wg = (const u16*)d_in[6];
  p.wu = (const u16*)d_in[7];
  p.wdn = (const u16*)d_in[8];
  p.out = (u16*)d_out;
  p.ws = (unsigned char*)d_ws;
  (void)hipFuncSetAttribute((const void*)fused_kernel, hipFuncAttributeMaxDynamicSharedMemorySize, LDS_BYTES);
  fused_kernel<<<256, NTHR, LDS_BYTES, stream>>>(p);
}
```

```cpp
#include <hip/hip_runtime.h>
#include <hip/hip_bf16.h>

typedef unsigned short u16;
typedef unsigned long long u64;
typedef short bf16x8 __attribute__((ext_vector_type(8)));
typedef short bf16x4 __attribute__((ext_vector_type(4)));
typedef float f32x4 __attribute__((ext_vector_type(4)));
typedef float f32x2 __attribute__((ext_vector_type(2)));
typedef unsigned u32x4 __attribute__((ext_vector_type(4)));
typedef unsigned u32x2 __attribute__((ext_vector_type(2)));
typedef int i32x4 __attribute__((ext_vector_type(4)));
typedef __bf16 bf2v __attribute__((ext_vector_type(2)));
#define LAS __attribute__((address_space(3)))
#define GAS __attribute__((address_space(1)))

namespace {
constexpr int S = 2048, D = 640, F = 1536;
constexpr int NTHR = 512;
constexpr int ARING = 0;
constexpr int BRING = 65536;
constexpr int TAB = 147456;
constexpr int T_ST = TAB;
constexpr int T_CS = TAB + 1024;
constexpr int T_CP = TAB + 1344;
constexpr int T_CG = TAB + 2368;
constexpr int T_PAD = TAB + 3328;
constexpr int T_EK = TAB + 12288;
constexpr int T_BIG = TAB + 4096;
constexpr int X_TILE = ARING + 3 * 16384;
constexpr int LDS_BYTES = 163840;
constexpr unsigned W_EPOCH = 0;
constexpr unsigned W_FA = 4096, W_FB = W_FA + 2048, W_FH = W_FB + 2048, W_FP = W_FH + 2048, W_FG = W_FP + 2048;
constexpr unsigned W_CSP = 139264;
constexpr unsigned W_ST = 81920;
constexpr unsigned W_CSD = W_ST + 32768;
constexpr unsigned W_ABT = 262144;
constexpr unsigned W_H = W_ABT + 2u * 4 * 64 * 10 * 1024;
constexpr unsigned W_WDT = W_H + 32u * 96 * 2 * 1024;
constexpr unsigned W_GU = W_WDT + 8u * 48 * 5 * 1024;
constexpr unsigned W_P = W_GU + 16u * 20 * 12 * 1024;
constexpr unsigned W_END = W_P + 256u * 4 * 10 * 1024;
constexpr unsigned W_STAMP = W_END;
constexpr int AUX_SC1 = 16;
#define DRAIN_G 3
#define PRO 2
#define DRAIN_P 41
#define CODE_BYTES 32512
#define WT0 -1
#define WTS 2
#define MAP 1
#define ROT (3 * (b >> 6) + ((b >> 3) & 7))
#define NT_W 1
#define NT_G 1
#define SELNT(c, f) ((c) ? f##nt : f)
#define GB 8

struct Params {
  const u16 *x, *wsr, *wsi, *wdr, *wdi, *wg, *wu, *wdn;
  u16* out;
  unsigned char* ws;
};

typedef const unsigned char* cptr;
#define DMA_BODY(POL) \
__device__ __forceinline__ void dma2##POL(cptr base, unsigned v0, unsigned v1, unsigned lds) { \
  asm volatile("s_mov_b32 m0, %3\n\ts_nop 0\n\tglobal_load_lds_dwordx4 %0, %2 " #POL "\n\tglobal_load_lds_dwordx4 %1, %2 offset:1024 " #POL \
               ::"v"(v0), "v"(v1), "s"(base), "s"(lds) : "memory"); \
} \
__device__ __forceinline__ void dma3##POL(cptr base, unsigned v0, unsigned v1, unsigned v2, unsigned lds) { \
  asm volatile("s_mov_b32 m0, %4\n\ts_nop 0\n\tglobal_load_lds_dwordx4 %0, %3 " #POL "\n\tglobal_load_lds_dwordx4 %1, %3 offset:1024 " #POL "\n\t" \
               "global_load_lds_dwordx4 %2, %3 offset:2048 " #POL ::"v"(v0), "v"(v1), "v"(v2), "s"(base), "s"(lds) : "memory"); \
} \
__device__ __forceinline__ void dma4##POL(cptr base, unsigned v0, unsigned v1, unsigned v2, unsigned v3, unsigned lds) { \
  asm volatile("s_mov_b32 m0, %5\n\ts_nop 0\n\tglobal_load_lds_dwordx4 %0, %4 " #POL "\n\tglobal_load_lds_dwordx4 %1, %4 offset:1024 " #POL "\n\t" \
               "global_load_lds_dwordx4 %2, %4 offset:2048 " #POL "\n\tglobal_load_lds_dwordx4 %3, %4 offset:3072 " #POL \
               ::"v"(v0), "v"(v1), "v"(v2), "v"(v3), "s"(base), "s"(lds) : "memory"); \
}
DMA_BODY()
DMA_BODY(nt)
DMA_BODY(sc1)
#define dma4s dma4sc1
__device__ __forceinline__ void dma1(cptr base, unsigned v, unsigned lds) {
  asm volatile("s_mov_b32 m0, %2\n\ts_nop 0\n\tglobal_load_lds_dwordx4 %0, %1" ::"v"(v), "s"(base), "s"(lds) : "memory");
}
__device__ __forceinline__ void dma4c(cptr base, unsigned v, unsigned lds) {
  asm volatile("s_mov_b32 m0, %2\n\ts_nop 0\n\tglobal_load_lds_dwordx4 %0, %1 sc1\n\tglobal_load_lds_dwordx4 %0, %1 offset:1024 sc1\n\t"
               "global_load_lds_dwordx4 %0, %1 offset:2048 sc1\n\tglobal_load_lds_dwordx4 %0, %1 offset:3072 sc1"
               ::"v"(v), "s"(base), "s"(lds) : "memory");
}
__device__ __forceinline__ void dma2c(cptr base, unsigned v, unsigned lds) {
  asm volatile("s_mov_b32 m0, %2\n\ts_nop 0\n\tglobal_load_lds_dwordx4 %0, %1 sc1\n\tglobal_load_lds_dwordx4 %0, %1 offset:1024 sc1"
               ::"v"(v), "s"(base), "s"(lds) : "memory");
}
__device__ __forceinline__ void touch(cptr base, unsigned voff, unsigned pad) {
  asm volatile("s_mov_b32 m0, %2\n\ts_nop 0\n\tglobal_load_lds_dword %0, %1" ::"v"(voff), "s"(base), "s"(pad) : "memory");
}
template <int N> __device__ __forceinline__ void wait_vm() { asm volatile("s_waitcnt vmcnt(%0)" ::"i"(N) : "memory"); }
#define SCHED0() __builtin_amdgcn_sched_barrier(0)
__device__ __forceinline__ void bar() {
  SCHED0();
  __builtin_amdgcn_s_waitcnt(0xC07F);
  asm volatile("s_barrier" ::: "memory");
  SCHED0();
}
__device__ __forceinline__ f32x4 mfma(bf16x8 a, bf16x8 b, f32x4 c) { return __builtin_amdgcn_mfma_f32_16x16x32_bf16(a, b, c, 0, 0, 0); }
__device__ __forceinline__ bf16x8 ldsr(LAS char* p) { return *(LAS bf16x8*)p; }
__device__ __forceinline__ bf16x4 ldstr(LAS char* p) { return __builtin_amdgcn_ds_read_tr16_b64_v4i16((LAS bf16x4*)p); }
__device__ __forceinline__ bf16x8 cat(bf16x4 a, bf16x4 b) { return __builtin_shufflevector(a, b, 0, 1, 2, 3, 4, 5, 6, 7); }
__device__ __forceinline__ unsigned pack2(float a, float b) {
  f32x2 v = {a, b};
  bf2v r = __builtin_convertvector(v, bf2v);
  return __builtin_bit_cast(unsigned, r);
}
__device__ __forceinline__ float asf(unsigned v) { return __builtin_bit_cast(float, v); }
__device__ __forceinline__ float bflo(unsigned v) { return __builtin_bit_cast(float, v << 16); }
__device__ __forceinline__ float bfhi(unsigned v) { return __builtin_bit_cast(float, v & 0xffff0000u); }
template <int CTRL> __device__ __forceinline__ float dpp_add(float v) {
  return v + __builtin_bit_cast(float, __builtin_amdgcn_update_dpp(0, __builtin_bit_cast(int, v), CTRL, 0xf, 0xf, true));
}
__device__ __forceinline__ float row8_sum(float v) {
  v = dpp_add<0xB1>(v); v = dpp_add<0x4E>(v); v = dpp_add<0x141>(v);
  return v;
}
__device__ __forceinline__ float dot_ones(unsigned v, float acc) {
  return __builtin_amdgcn_fdot2_f32_bf16(__builtin_bit_cast(bf2v, v), __builtin_bit_cast(bf2v, 0x3f803f80u), acc, false);
}
__device__ __forceinline__ u64 ld_flag(const unsigned char* ws, unsigned off) {
  return __hip_atomic_load((GAS u64*)(ws + off), __ATOMIC_RELAXED, __HIP_MEMORY_SCOPE_AGENT);
}
__device__ __forceinline__ u64 next_tag(u64 prev) { return prev * 6364136223846793005ull + 1442695040888963407ull; }

#define STAMP(i) do { } while (0)

struct Ids {
  int rb, cb, tt, et, kq;
};

template <int I> struct IC { static constexpr int v = I; };
template <int I, int N, class Fn> __device__ __forceinline__ void sfor(Fn&& f) {
  if constexpr (I < N) { f(IC<I>{}); sfor<I + 1, N>(f); }
}
__device__ __forceinline__ int fresh_lane() {
  int l;
  asm volatile("v_mbcnt_lo_u32_b32 %0, -1, 0\n\tv_mbcnt_hi_u32_b32 %0, -1, %0" : "=v"(l));
  return l;
}
__device__ __forceinline__ float sel(float a, float b, u64 m) {
  float o;
  asm("v_cndmask_b32_e64 %0, %1, %2, %3" : "=v"(o) : "v"(a), "v"(b), "s"(m));
  return o;
}
__device__ __forceinline__ float pick4(f32x4 v) {
  float a = v[0], b = v[1], c = v[2], d = v[3];
  asm volatile("s_nop 7" : "+v"(a), "+v"(b), "+v"(c), "+v"(d));
  return sel(sel(a, b, 0xAAAAAAAAAAAAAAAAull), sel(c, d, 0xAAAAAAAAAAAAAAAAull), 0xCCCCCCCCCCCCCCCCull);
}
#define PIN(x) asm volatile("" : "+v"(x))
template <int N>
__device__ __forceinline__ void finish(f32x4 (&dk)[N], const f32x4 (&d1)[N], f32x4 (&ys)[N], LAS char* xp) {
  f32x4 ex[N];
  u32x2 xv[N];
#pragma unroll
  for (int g = 0; g < N; ++g) xv[g] = *(volatile LAS u32x2*)(xp + g * 32);
  SCHED0();
#pragma unroll
  for (int g = 0; g < N; ++g) { dk[g] += d1[g]; ex[g] = dk[g] * -1.4426950409f; }
  SCHED0();
#pragma unroll
  for (int g = 0; g < N; ++g) {
#pragma unroll
    for (int e = 0; e < 4; ++e) ex[g][e] = __builtin_amdgcn_exp2f(ex[g][e]);
    dk[g] *= ys[g];
  }
  SCHED0();
#pragma unroll
  for (int g = 0; g < N; ++g) ex[g] += 1.f;
  SCHED0();
#pragma unroll
  for (int g = 0; g < N; ++g) {
#pragma unroll
    for (int e = 0; e < 4; ++e) ex[g][e] = __builtin_amdgcn_rcpf(ex[g][e]);
    ys[g] = f32x4{bflo(xv[g][0]), bfhi(xv[g][0]), bflo(xv[g][1]), bfhi(xv[g][1])};
  }
  SCHED0();
#pragma unroll
  for (int g = 0; g < N; ++g) {
    const f32x4 o = dk[g] * ex[g] + ys[g];
    *(volatile LAS u32x2*)(xp + g * 32) = u32x2{pack2(o[0], o[1]), pack2(o[2], o[3])};
  }
}

__device__ __forceinline__ void compute(const Params& p, LAS char* L, const Ids& id, int w) {
  int lane = fresh_lane(), r = lane & 15, q = lane >> 4;
  const __amdgpu_buffer_rsrc_t rws = __builtin_amdgcn_make_buffer_rsrc((void*)p.ws, 0, (int)W_END, 0x00020000);
  const f32x4 z4 = {0.f, 0.f, 0.f, 0.f};

  const bf16x8 ones = {0x3f80, 0x3f80, 0x3f80, 0x3f80, 0x3f80, 0x3f80, 0x3f80, 0x3f80};
  if (blockIdx.x < 240) {
    const int pc = blockIdx.x % 48, pr = blockIdx.x / 48;
    const unsigned va = (lane >> 3) * 3072 + (lane & 7) * 16;
    SELNT(NT_G, dma4)((cptr)(pc < 24 ? p.wg : p.wu) + ((pr * 128 + w * 32) * 3072 + (pc % 24) * 128), va, va + 23552, va + 2 * 23552, va + 3 * 23552,
         (unsigned)(size_t)L + BRING + (w ? w * 20480 + 10240 : 0));
    LAS char* a = L + BRING + (w ? w * 20480 + 10240 : 0) + (8 * q + (r >> 2)) * 128 + 8 * (r & 3);
    const unsigned go = lane * 16 + W_GU + (((pc % 24) * 4 * 20 + pr * 4 + w) * 2 + pc / 24) * 1024;
    wait_vm<0>();
    bf16x8 f[4];
#pragma unroll
    for (int c = 0; c < 4; ++c) f[c] = cat(ldstr(a + c * 32), ldstr(a + 512 + c * 32));
    f32x4 acc[4];
#pragma unroll
    for (int c = 0; c < 4; ++c) acc[c] = mfma(ones, f[c], z4);
    SCHED0();
#pragma unroll
    for (int c = 0; c < 4; ++c) __builtin_amdgcn_raw_buffer_store_b128(__builtin_bit_cast(u32x4, f[c]), rws, go + c * 40960, 0, AUX_SC1);
    SCHED0();
    float v01 = (q & 1) ? acc[1][0] : acc[0][0], v23 = (q & 1) ? acc[3][0] : acc[2][0];
    PIN(v01); PIN(v23);
    *(LAS float*)(L + T_CP + (w * 64 + lane) * 4) = (q & 2) ? v23 : v01;
  }

  bf16x8 xr[20][2];
  f32x4 accP[5][2], st[4];
  float cs[5];
  {
    f32x4 accS[2], accQ[2];
    bf16x8 pb[2][5];
    const int oK0 = r * 128 + ((q ^ (r >> 1)) & 7) * 16, oK1 = oK0 ^ 64;
    LAS char* xA[2] = {L + ARING + w * 4096 + oK0, L + ARING + w * 4096 + oK1};
    LAS char* pB[2] = {L + BRING + 10240 + oK0, L + BRING + 10240 + oK1};
    PIN(xA[0]); PIN(xA[1]); PIN(pB[0]); PIN(pB[1]);
    auto rdx = [&](int i, int ks, int t) { xr[2 * i + ks][t] = ldsr(xA[ks] + (i & 3) * 16384 + t * 2048); };
    auto rdw = [&](int i, int ks, int g) { pb[ks][g] = ldsr(pB[ks] + ((i & 3) ? (i & 3) * 20480 - 10240 : 0) + g * 2048); };
    auto stat = [&](int s, int k) {
      if (k < 2) accS[k] = mfma(xr[s][k], ones, s == 0 ? z4 : accS[k]);
      else accQ[k - 2] = mfma(xr[s][k - 2], xr[s][k - 2], s == 0 ? z4 : accQ[k - 2]);
    };
#pragma unroll
    for (int i = 0; i < 10; ++i) {
      if (i == DRAIN_G) wait_vm<0>();
      bar();
#pragma unroll
      for (int k = 0; k < 10; ++k) {
        if (i > 0) accP[k >> 1][k & 1] = mfma(xr[2 * i - 1][k & 1], pb[1][k >> 1], accP[k >> 1][k & 1]);
        if (k & 1) rdw(i, 0, k >> 1);
        else if (k < 8) rdx(i, k >> 2, (k >> 1) & 1);
        SCHED0();
      }
#pragma unroll
      for (int k = 0; k < 18; ++k) {
        if (k < 2) stat(2 * i, k);
        else if (k < 12) {
          accP[(k - 2) >> 1][k & 1] = mfma(xr[2 * i][k & 1], pb[0][(k - 2) >> 1], i == 0 ? z4 : accP[(k - 2) >> 1][k & 1]);
          if (k & 1) rdw(i, 1, (k - 2) >> 1);
        } else if (k < 14) stat(2 * i, k - 10);
        else stat(2 * i + 1, k - 14);
        SCHED0();
      }
    }
    lane = fresh_lane(); r = lane & 15; q = lane >> 4;
    const bool diag = (r >> 2) == q;
    LAS char* const tw = diag ? L + T_ST + (w * 32 + r) * 4 : L + T_PAD;
    LAS char* const te = diag ? L + T_EK + (w * 32 + r) * 16 : L + T_PAD;
    const float sg = id.cb >= 8 ? -1.f : 1.f;
    float sm[2], sq[2], rs[2];
#pragma unroll
    for (int k = 0; k < 10; ++k) {
      accP[k >> 1][k & 1] = mfma(xr[19][k & 1], pb[1][k >> 1], accP[k >> 1][k & 1]);
      if (k < 2) {
#pragma unroll
        for (int g = 3 * k; g < 3 * k + 3 && g < 5; ++g) cs[g] = *(LAS float*)(L + T_CS + (g * 16 + r) * 4);
      }
      if (k == 1 || k == 2) {
        const int t = k - 1;
        sm[t] = pick4(accS[t]) * (-1.0f / D); sq[t] = pick4(accQ[t]) * (1.0f / D);
        PIN(sm[t]); PIN(sq[t]);
      }
      if (k == 3) {
#pragma unroll
        for (int t = 0; t < 2; ++t) { sq[t] = fmaxf(sq[t] - sm[t] * sm[t], 0.f) + 1e-5f; PIN(sq[t]); }
#pragma unroll
        for (int t = 0; t < 2; ++t) { rs[t] = __builtin_amdgcn_rsqf(sq[t]); PIN(rs[t]); }
      }
      if (k == 4) {
#pragma unroll
        for (int t = 0; t < 2; ++t) {
          const float v = rs[t] * sg;
          *(LAS float*)(tw + t * 64) = v; *(LAS float*)(tw + 512 + t * 64) = v * sm[t];
        }
      }
      if (k == 5) {
#pragma unroll
        for (int n = 0; n < 4; ++n) st[n] = *(LAS f32x4*)(L + T_ST + (n >> 1) * 512 + (w * 32 + (n & 1) * 16 + 4 * q) * 4);
      }
      if (k == 6 || k == 7) {
        const int t = k - 6;
        *(LAS f32x4*)(te + t * 256) = f32x4{sm[t], -2.302208198f * rs[t], -0.1029432397f * rs[t] * rs[t] * rs[t], sq[t]};
      }
      SCHED0();
    }
  }
  LAS char* const fB0 = L + BRING + lane * 16;
  LAS char* const fB3 = L + BRING + 61440 + lane * 16;
  auto fragB = [&](int stage, int n) -> bf16x8 { return (stage & 3) == 3 ? ldsr(fB3 + n * 1024) : ldsr(fB0 + (stage & 3) * 20480 + n * 1024); };
  LAS char* const xB0 = L + ARING + w * 4096 + r * 128 + ((q ^ (r >> 1)) & 7) * 16;
  LAS char* const xB1 = L + ARING + w * 4096 + ((r * 128 + ((q ^ (r >> 1)) & 7) * 16) ^ 64);
  const unsigned abBase = W_ABT + (unsigned)((((id.cb >> 3) * 4 + ((id.cb & 7) >> 1)) * 64 + id.rb * 4 + w) * 10 + (id.cb & 1) * 5) * 1024u + lane * 16;

  u32x4 abo;
  f32x4 accH[2][4];
  bf16x8 wb[4][2];
  const unsigned hbase = W_H + (unsigned)(((id.rb * 2 + (w >> 1)) * 96 + id.cb * 6) * 2 + (w & 1)) * 1024u + lane * 16;
  float gv[8], uv[8], wv[8];
  f32x4 cgv, cuv, ek[2];
  auto h_tab = [&](int c) {
    cgv = *(LAS f32x4*)(L + T_CG + (c * 16 + 4 * q) * 4);
    cuv = *(LAS f32x4*)(L + T_CG + 384 + (c * 16 + 4 * q) * 4);
  };
  auto h_op = [&](int c, int n) {
    const int ph = n >> 3, x = n & 7, t = x >> 2, e = x & 3;
    if (ph == 0) { gv[x] = accH[c & 1][t][e] + ek[t][0] * cgv[e]; PIN(gv[x]); uv[x] = accH[c & 1][2 + t][e] + ek[t][0] * cuv[e]; PIN(uv[x]); }
    if (ph == 1) { wv[x] = gv[x] * (ek[t][1] + ek[t][2] * (gv[x] * gv[x])); PIN(wv[x]); }
    if (ph == 2) { wv[x] = __builtin_amdgcn_exp2f(wv[x]); PIN(wv[x]); uv[x] *= gv[x]; PIN(uv[x]); }
    if (ph == 3) { wv[x] = __builtin_amdgcn_rcpf(wv[x] * ek[t][3] + ek[t][3]); PIN(wv[x]); }
    if (ph == 4) {
      uv[x] *= wv[x]; PIN(uv[x]);
      if (x == 7) {
        u32x4 o = {pack2(uv[0], uv[1]), pack2(uv[2], uv[3]), pack2(uv[4], uv[5]), pack2(uv[6], uv[7])};
        __builtin_amdgcn_raw_buffer_store_b128(o, rws, hbase + c * 2048, 0, AUX_SC1);
      }
    }
  };
  const int wm = w >> 1, wn = w & 1;
  LAS char *aBase0, *aBase1, *yBase;
  bf16x8 xa0[4], xa1[4], yb0[5], yb1[5];
  auto rdg = [&](int g, int m) { wb[g & 3][m] = fragB(10 + g / 10, (g % 10) * 2 + m); };
  bar();
#pragma unroll
  for (int g = 0; g < 3; ++g) { rdg(g, 0); rdg(g, 1); }
  sfor<0, 120>([&](auto gi) __attribute__((always_inline)) {
    constexpr int g = decltype(gi)::v, c = g / 20, s = g % 20;
    if (g == 110) {
      const int l2 = fresh_lane(), r2 = l2 & 15, oK0 = r2 * 128 + (((l2 >> 4) ^ (r2 >> 1)) & 7) * 16;
      aBase0 = L + ARING + wm * 8192 + oK0; aBase1 = L + ARING + wm * 8192 + (oK0 ^ 64); yBase = L + BRING + wn * 5120 + l2 * 16;
    }
    if (g % 10 == 8) {
      if (g == 38) wait_vm<0>();
      bar();
    }
#pragma unroll
    for (int k = 0; k < 4; ++k) {
      accH[c & 1][k] = mfma(wb[g & 3][k >> 1], xr[s][k & 1], s == 0 ? z4 : accH[c & 1][k]);
      bool rd = true;
      if (g >= 118) {
        const int n = (g - 118) * 4 + k;
        if (n < 4) xa0[n] = ldsr(aBase0 + 2 * 16384 + n * 2048); else yb0[n - 4] = ldsr(yBase + 2 * 20480 + (n - 4) * 1024);
      } else if (g % 10 == 8) rdg(g + 2 + (k >> 1), k & 1);
      else if (g % 10 != 7 && k < 2) rdg(g + 3, k);
      else rd = false;
      if (!rd) {
        const int n = s % 10 == 7 ? 2 * s + k : 2 * s + k - 2;
        if (c == 0) {
          if (n < 25) {
            const int gg = n / 5, ph = n % 5;
            if (ph < 4) {
              const int t = ph >> 1, e = (ph & 1) * 2;
              unsigned pk = pack2(accP[gg][t][e] * st[t][e] + st[2 + t][e] * cs[gg], accP[gg][t][e + 1] * st[t][e + 1] + st[2 + t][e + 1] * cs[gg]);
              PIN(pk);
              abo[ph] = pk;
            } else __builtin_amdgcn_raw_buffer_store_b128(abo, rws, abBase + gg * 1024, 0, AUX_SC1);
          }
          if (n >= 10 && n < 26 && !(n & 1)) {
            const int m = (n - 10) >> 1;
            xr[14 + (m >> 1)][m & 1] = ldsr(((m & 2) ? xB1 : xB0) + ((7 + (m >> 2)) & 3) * 16384 + (m & 1) * 2048);
          }
          if (n == 34) ek[0] = *(LAS f32x4*)(L + T_EK + (w * 32 + r) * 16);
          if (n == 35) ek[1] = *(LAS f32x4*)(L + T_EK + (w * 32 + 16 + r) * 16);
        } else if (c < 5) h_op(c - 1, n);
        else if (n < 36) { h_op(4, n); if (n >= 32) h_op(4, n + 4); }
        if (n == (c == 0 ? 38 : 36)) h_tab(c);
      }
      SCHED0();
    }
  });

  f32x4 accY[5][4];
  auto y_stage = [&](int i, int h0) {
    LAS char* a0 = aBase0 + ((i + 2) & 3) * 16384; LAS char* a1 = aBase1 + ((i + 2) & 3) * 16384;
    LAS char* b = yBase + ((i + 2) & 3) * 20480;
    PIN(a0); PIN(a1); PIN(b);
    bar();
    int n = h0;
#pragma unroll
    for (int k = 0; k < 20; ++k) {
      accY[k >> 2][k & 3] = mfma(yb1[k >> 2], xa1[k & 3], accY[k >> 2][k & 3]);
      if (k < 4) xa0[k] = ldsr(a0 + k * 2048);
      else if (k < 9) yb0[k - 4] = ldsr(b + (k - 4) * 1024);
      else if (k < 11) yb1[k - 9] = ldsr(b + 10240 + (k - 9) * 1024);
      else if (k == 12) yb1[2] = ldsr(b + 10240 + 2048);
      else if (k == 16) yb1[3] = ldsr(b + 10240 + 3072);
      else if (n < 40) h_op(5, n++);
      SCHED0();
    }
#pragma unroll
    for (int k = 0; k < 20; ++k) {
      accY[k >> 2][k & 3] = mfma(yb0[k >> 2], xa0[k & 3], accY[k >> 2][k & 3]);
      if (k == 0) yb1[4] = ldsr(b + 10240 + 4096);
      else if (k < 5) xa1[k - 1] = ldsr(a1 + (k - 1) * 2048);
      else if (n < 40) h_op(5, n++);
      SCHED0();
    }
  };
#pragma unroll
  for (int k = 0; k < 20; ++k) {
    if (k == 0) yb0[4] = ldsr(yBase + 2 * 20480 + 4096);
    accY[k >> 2][k & 3] = mfma(yb0[k >> 2], xa0[k & 3], z4);
    if (k < 4) xa1[k] = ldsr(aBase1 + 2 * 16384 + k * 2048);
    else if (k < 9) yb1[k - 4] = ldsr(yBase + 2 * 20480 + 10240 + (k - 4) * 1024);
    else h_op(5, k - 9);
    SCHED0();
  }
  y_stage(1, 11); y_stage(2, 33);
#pragma nounroll
  for (int i = 3; i < 16; ++i) {
    if (i == 5) wait_vm<0>();
    y_stage(i, 40);
  }

  lane = fresh_lane(); r = lane & 15; q = lane >> 4;
  const int tile = id.tt * 4 + id.et;
  f32x4 accD[5][2];
#pragma unroll
  for (int g = 0; g < 5; ++g) { accD[g][0] = z4; accD[g][1] = z4; }
  LAS char* const hB = L + ARING + wm * 1024 + wn * 8192 + lane * 16 + wn * 8;
  LAS char* const dB = fB0 + wn * 10240;
  bf16x8 hh[2][2], dw[2][5];
  auto d_rd = [&](LAS char* a, LAS char* a2, LAS char* b, int ks, int m) {
    if (m < 2) hh[ks][m] = cat(*(volatile LAS bf16x4*)((m ? a2 : a) + ks * 4096), *(volatile LAS bf16x4*)((m ? a2 : a) + ks * 4096 + 2048));
    else dw[ks][m - 2] = ldsr(b + ks * 5120 + (m - 2) * 1024);
  };
  auto d_mm = [&](int ks, int k) { accD[k >> 1][k & 1] = mfma(dw[ks][k >> 1], hh[ks][k & 1], accD[k >> 1][k & 1]); };
  const unsigned pbase = W_P + (unsigned)(((tile * 4 + w) * 4 + id.kq) * 10) * 1024u + lane * 16;
  auto p_store = [&](int u) {
    u32x4 o[5];
#pragma unroll
    for (int g = 0; g < 5; ++g) {
      const f32x4 a = accY[g][2 * u], b = accY[g][2 * u + 1];
      o[g] = u32x4{pack2(a[0], a[1]), pack2(a[2], a[3]), pack2(b[0], b[1]), pack2(b[2], b[3])};
    }
    if (w != id.kq) {
#pragma unroll
      for (int g = 0; g < 5; ++g) __builtin_amdgcn_raw_buffer_store_b128(o[g], rws, pbase + (u * 5 + g) * 1024, 0, AUX_SC1);
    } else {
#pragma unroll
      for (int g = 0; g < 5; ++g) *(LAS u32x4*)(L + T_BIG + (u * 5 + g) * 1024 + lane * 16) = o[g];
    }
  };
  auto d_second = [&](LAS char* a, LAS char* a2, LAS char* b) {
#pragma unroll
    for (int k = 0; k < 10; ++k) {
      d_mm(0, k);
      if (k < 2) d_rd(a, a2, b, 1, k); else if (k < 4) d_rd(a, a2, b, 1, k + 3);
      SCHED0();
    }
  };
  auto d_stage = [&](int i) {
    LAS char* a = hB + ((i + 2) & 3) * 16384; LAS char* a2 = a + (8 - 16 * wn); LAS char* b = dB + ((i + 2) & 3) * 20480;
    PIN(a); PIN(a2); PIN(b);
    bar();
#pragma unroll
    for (int k = 0; k < 10; ++k) {
      d_mm(1, k);
      if (k < 7) d_rd(a, a2, b, 0, k); else d_rd(a, a2, b, 1, k - 5);
      SCHED0();
    }
    d_second(a, a2, b);
  };
  {
    LAS char* a = hB + 2 * 16384; LAS char* a2 = a + (8 - 16 * wn); LAS char* b = dB + 2 * 20480;
    PIN(a); PIN(a2); PIN(b);
    bar();
#pragma unroll
    for (int k = 0; k < 20; ++k) {
      accY[k >> 2][k & 3] = mfma(yb1[k >> 2], xa1[k & 3], accY[k >> 2][k & 3]);
      if (k < 7) d_rd(a, a2, b, 0, k); else if (k < 10) d_rd(a, a2, b, 1, k - 5);
      SCHED0();
    }
    d_second(a, a2, b);
  }
  p_store(0);
  p_store(1);
#pragma nounroll
  for (int i = 1; i < 12; ++i) {
    if (i == DRAIN_P - 38) wait_vm<0>();
    d_stage(i);
  }
#pragma unroll
  for (int k = 0; k < 10; ++k) {
    d_mm(1, k < 5 ? 2 * k + 1 : 2 * (9 - k));
    if (k >= 5) *(LAS f32x4*)(fB0 + ((w ^ 1) * 5 + k - 5) * 1024) = accD[k - 5][1];
    SCHED0();
  }
  LAS char* dp = fB0 + w * 5120;
  *(LAS f32x4*)(L + ARING + w * 2048 + 1024 + lane * 16) = accD[4][0];
  *(LAS f32x4*)(L + ARING + w * 2048 + lane * 16) = accD[3][0];
  LAS char* xp = L + X_TILE + (wm * 32 + 8 * (r >> 2) + 4 * wn + (r & 3)) * 160 + q * 8;
  PIN(xp); PIN(dp);
  bar();
  f32x4 dk[3], ys[3];
#pragma unroll
  for (int g = 0; g < 3; ++g) dk[g] = *(LAS f32x4*)(dp + g * 1024);
#pragma unroll
  for (int g = 0; g < 3; ++g) ys[g] = *(LAS f32x4*)(dp + 61440 + g * 1024);
  const f32x4 own[3] = {accD[0][0], accD[1][0], accD[2][0]};
  finish<3>(dk, own, ys, xp);
}

__device__ __forceinline__ void aux_tail(const Params& p, LAS char* L, int t, int lane) {
  const int po = (t >> 1) * 5120 + lane * 16 + (t & 1) * 8;
  u32x2 y[4][5];
#pragma unroll
  for (int g = 0; g < 5; ++g) {
    y[0][g] = *(volatile LAS u32x2*)(L + T_BIG + po + g * 1024);
    y[1][g] = *(volatile LAS u32x2*)(L + BRING + 2 * 20480 + po + g * 1024);
    y[2][g] = *(volatile LAS u32x2*)(L + BRING + 2 * 20480 + 10240 + po + g * 1024);
    y[3][g] = *(volatile LAS u32x2*)(L + ARING + 2 * 16384 + po + g * 1024);
  }
  SCHED0();
  LAS char* dp = L + ARING + t * 2048 + lane * 16;
  f32x4 ys[2];
#pragma unroll
  for (int g = 0; g < 5; ++g) {
    f32x4 v[4];
#pragma unroll
    for (int k = 0; k < 4; ++k) v[k] = f32x4{bflo(y[k][g][0]), bfhi(y[k][g][0]), bflo(y[k][g][1]), bfhi(y[k][g][1])};
    const f32x4 sum = (v[0] + v[1]) + (v[2] + v[3]);
    if (g < 3) *(LAS f32x4*)(L + BRING + 61440 + (t * 5 + g) * 1024 + lane * 16) = sum;
    else ys[g - 3] = sum;
  }
  const int r = lane & 15;
  LAS char* xp = L + X_TILE + ((t >> 1) * 32 + 8 * (r >> 2) + 4 * (t & 1) + (r & 3)) * 160 + (lane >> 4) * 8 + 96;
  LAS char* dq = L + BRING + t * 5120 + 3072 + lane * 16;
  PIN(xp); PIN(dp); PIN(dq); PIN(ys[0]); PIN(ys[1]);
  wait_vm<0>();
  bar();
  if (t == 3) { STAMP(50); }
  f32x4 dk[2], own[2];
#pragma unroll
  for (int g = 0; g < 2; ++g) { dk[g] = *(LAS f32x4*)(dq + g * 1024); own[g] = *(LAS f32x4*)(dp + g * 1024); }
  finish<2>(dk, own, ys, xp);
}

__device__ __forceinline__ void st_flag(unsigned char* ws, unsigned off, u64 v, int lane) {
  if (lane == 0) __hip_atomic_store((GAS u64*)(ws + off), v, __ATOMIC_RELAXED, __HIP_MEMORY_SCOPE_AGENT);
}
__device__ __forceinline__ void wait_flag(const unsigned char* ws, unsigned off, u64 tag, u64 seen) {
  if (!__all(seen == tag)) {
#pragma nounroll
    for (int spin = 0; spin < (1 << 16); ++spin)
      if (__all(ld_flag(ws, off) == tag)) break;
  }
  __builtin_amdgcn_fence(__ATOMIC_ACQUIRE, "wavefront");
}
template <int N, class Fn>
__device__ __forceinline__ void wait_tags(u32x4 (&g)[N], unsigned tlo, unsigned thi, Fn&& reload) {
  auto good = [&]() {
    unsigned bad = 0;
#pragma unroll
    for (int k = 0; k < N; ++k) bad |= (g[k][1] ^ tlo) | (g[k][3] ^ thi);
    return __all(bad == 0);
  };
  if (!good()) {
#pragma nounroll
    for (int spin = 0; spin < (1 << 16); ++spin) {
      reload();
      if (good()) break;
    }
  }
}
__device__ __forceinline__ void loader(const Params& p, LAS char* L, unsigned lb, const Ids& id, int ld, int lane) {
  const int b = blockIdx.x;
  const unsigned vl = lane * 16;
  const unsigned c0 = ((lane & 7) ^ (lane >> 4)) * 16, c1 = c0 ^ 64;
  const int rl = 8 * (lane >> 5) + ((lane >> 3) & 3);
  const int rot = ROT;
  {
    const unsigned vd0 = (lane >> 3) * 1280 + c0, vd1 = (lane >> 3) * 1280 + c1 + 9216;
    const unsigned v0 = ld < 2 ? rl * 1280 + c0 : vd0, v1 = ld < 2 ? (rl + 16) * 1280 + c1 - 1024 : vd1,
                   v2 = ld < 2 ? (rl + 4) * 1280 + c0 - 2048 : vd0 + 2 * 9216, v3 = ld < 2 ? (rl + 20) * 1280 + c1 - 3072 : vd1 + 2 * 9216;
    const cptr bd = (cptr)(id.cb < 8 ? p.wdr : p.wdi) + (id.cb & 7) * 80 * 1280;
    const cptr b1 = ld < 2 ? (cptr)p.x + (id.rb * 128 + ld * 64) * 1280 : bd;
    int kc = rot % 10;
#pragma nounroll
    for (int j = 0; j < 10; ++j) {
      if (j == 2 || j == 3) { if (ld == 2) wait_vm<10>(); else wait_vm<8>(); bar(); }
      if (j > 4) { if (ld == 2) wait_vm<20>(); else wait_vm<16>(); bar(); }
      const unsigned lw = lb + BRING + ((j & 3) ? (j & 3) * 20480 : 10240);
      const unsigned l = ld < 2 ? lb + ARING + (j & 3) * 16384 + ld * 8192 : lw;
      dma4(b1 + kc * 128, v0, v1, v2, v3, l);
      dma4(b1 + (kc * 128 + 32 * 1280), v0, v1, v2, v3, l + 4096);
      if (ld == 2) dma2(b1 + (kc * 128 + 64 * 1280), v0, v1, l + 8192);
      kc = kc == 9 ? 0 : kc + 1;
    }
    wait_vm<16>(); bar();
  }
  const unsigned vw0 = rl * 4096 + c0, vw1 = (rl + 16) * 4096 + c1 - 1024, vw2 = (rl + 4) * 4096 + c0 - 2048, vw3 = (rl + 20) * 4096 + c1 - 3072;
  const unsigned u0 = ld ? vl : vw0, u1 = ld ? vl : vw1, u2 = ld ? vl : vw2, u3 = ld ? vl : vw3;
  const cptr bw = (cptr)(id.kq < 2 ? p.wsr : p.wsi) + (id.tt * 128 * 4096 + (id.kq & 1) * 2048);
  const int g0 = ld ? 2 * ld - 2 : 4;
  const unsigned l0 = lb + BRING + g0 * 4096, l1 = ld ? l0 + 4096 : lb + ARING, ls1 = ld ? 20480 : 16384, l2 = lb + ARING + (ld + 1) * 4096;
  auto yk = [&](int y) { return (y & ~3) | ((y + id.et) & 3); };
  {
    const cptr bg = p.ws + W_GU + (unsigned)id.cb * 245760u;
    const int ka[2] = {((g0 + rot) % 10) * 4096, ((g0 + rot + 5) % 10) * 4096}, kb[2] = {((g0 + 1 + rot) % 10) * 4096, ((g0 + rot + 6) % 10) * 4096};
#pragma nounroll
    for (int j = 10; j < 22; ++j) {
      if (j != 11) {
        if (j <= 12 || ld == 0) wait_vm<8>(); else wait_vm<16>();
        bar();
      }
      const cptr so = bg + ((j - 10) >> 1) * 40960;
      dma4c(so + ((j & 1) ? ka[1] : ka[0]), vl, l0 + (j & 3) * 20480);
      if (ld) dma4c(so + ((j & 1) ? kb[1] : kb[0]), vl, l1 + (j & 3) * 20480);
      const int n = (j - 16) * 3 + ld;
      if (n >= 0 && n < 16) dma4(bw + (yk(n >> 2) * 128 + (n & 3) * 32 * 4096), vw0, vw1, vw2, vw3, lb + ARING + (((n >> 2) + 2) & 3) * 16384 + (n & 3) * 4096);
    }
  }
  const int c80 = id.et * 2 + (id.kq & 1);
  const cptr bh = p.ws + W_H + (unsigned)(id.tt * 2 + (id.kq >> 1)) * 196608u;
  const cptr bab = p.ws + W_ABT + ((unsigned)(((id.kq >> 1) * 4 + id.et) * 64 + (id.kq & 1) * 32) * 10240u + g0 * 4096);
  const cptr bwd = p.ws + W_WDT + (unsigned)c80 * 245760u;
  const cptr y1 = ld ? bab + 4096 : bw, y2 = bw + (ld + 1) * 32 * 4096;
  const int ym1 = ld ? 20480 : 128;
  const cptr d0 = bwd + g0 * 4096, d1 = ld ? d0 + 4096 : bh, d2 = bh + (ld + 1) * 4096;
#pragma nounroll
  for (int i = 19; i < 35; ++i) {
    if (i >= 26) wait_vm<24>(); else if (ld == 0) wait_vm<8>(); else wait_vm<16>();
    bar();
    const int k = yk(i - 19), sl = (i + 3) & 3;
    dma4c(bab + k * 20480, vl, l0 + sl * 20480);
    if (i >= 23 || ld) dma4s(y1 + k * ym1, u0, u1, u2, u3, l1 + sl * ls1);
    if (i >= 23) dma4(y2 + k * 128, vw0, vw1, vw2, vw3, l2 + sl * 16384);
  }
#pragma nounroll
  for (int i = 35; i < 47; ++i) {
    wait_vm<24>();
    bar();
    const int sl = (i + 3) & 3;
    dma4c(d0 + (i - 35) * 20480, vl, l0 + sl * 20480);
    dma4c(d1 + (i - 35) * ls1, vl, l1 + sl * ls1);
    dma4c(d2 + (i - 35) * 16384, vl, l2 + sl * 16384);
  }
  wait_vm<24>(); bar();
  const cptr bp = p.ws + W_P + ((unsigned)(((id.tt * 4 + id.et) * 4 + id.kq) * 4 * 10) * 1024u + ((id.kq + 1 + ld) & 3) * 10240);
  const unsigned l = lb + (ld == 0 ? ARING + 2 * 16384 : BRING + 2 * 20480 + (ld - 1) * 10240);
  dma4c(bp, vl, l); dma4c(bp + 4096, vl, l + 4096); dma2c(bp + 8192, vl, l + 8192);
}

__device__ __forceinline__ void helper(const Params& p, LAS char* L, unsigned lb, const Ids& id, int lane, u64 pc) {
  const int b = blockIdx.x;
  const __amdgpu_buffer_rsrc_t rws = __builtin_amdgcn_make_buffer_rsrc((void*)p.ws, 0, (int)W_END, 0x00020000);
  const u64 tq0 = __builtin_amdgcn_s_memrealtime();
  if (lane == 0) __hip_atomic_store((GAS u64*)(p.ws + W_END + b * 8), tq0, __ATOMIC_RELAXED, __HIP_MEMORY_SCOPE_AGENT);
  u64 tq1 = tq0;
  __builtin_amdgcn_s_setprio(3);
  const u64 prev = ld_flag(p.ws, W_EPOCH);
  const int r8 = lane >> 3;
  u32x4 dv[10];
  const GAS char* ds = (const GAS char*)(b < 128 ? p.wdr : p.wdi) + (((b & 127) * 5 + (r8 < 5 ? r8 : 4)) * 1280 + (lane & 7) * 16);
#pragma unroll
  for (int i = 0; i < 10; ++i) dv[i] = *(const GAS u32x4*)(ds + i * 128);
  auto code_touch = [&]() {
    const unsigned off = (((b >> 3) % ((CODE_BYTES + 8191) / 8192)) * 64 + lane) * 128;
    touch((cptr)(pc & ~255ull), off < CODE_BYTES ? off : 0, lb + T_PAD);
  };
  code_touch();
  const u64 tag = next_tag(prev);
  const unsigned tlo = (unsigned)tag, thi = (unsigned)(tag >> 32);
  {
    float t4[4] = {0.f, 0.f, 0.f, 0.f};
#pragma unroll
    for (int i = 0; i < 10; ++i)
#pragma unroll
      for (int e = 0; e < 4; ++e) t4[e] = dot_ones(dv[i][e], t4[e]);
    const unsigned sm = __builtin_bit_cast(unsigned, row8_sum((t4[0] + t4[1]) + (t4[2] + t4[3])));
    const u32x4 o = {sm, tlo, sm, thi};
    if ((lane & 7) == 0 && r8 < 5) __builtin_amdgcn_raw_buffer_store_b128(o, rws, W_CSD + (b * 5 + r8) * 16, 0, AUX_SC1);
  }
  const int pcl = (id.cb * 3) >> 1, tile = id.tt * 4 + id.et, c80 = id.et * 2 + (id.kq & 1);
  const int wk = b / 5, wc = b % 5;
  u64 f0 = 0, f1 = 0, f2 = 0;
  unsigned fo0 = 0, fo1 = 0, fo2 = 0;
  u32x4 gr[2], cq[10];
  const int go = lane < 40 ? lane : 0;
  auto ld_gr = [&]() {
    gr[0] = __builtin_amdgcn_raw_buffer_load_b128(rws, W_CSD + (id.cb * 80 + go) * 16, 0, AUX_SC1);
    gr[1] = __builtin_amdgcn_raw_buffer_load_b128(rws, W_CSD + (id.cb * 80 + 40 + go) * 16, 0, AUX_SC1);
  };
  auto ld_cq = [&]() {
#pragma unroll
    for (int k = 0; k < 10; ++k) cq[k] = __builtin_amdgcn_raw_buffer_load_b128(rws, W_CSP + ((k * 24 + pcl) * 32 + lane) * 16, 0, AUX_SC1);
  };
  const cptr wsb = (cptr)(id.kq < 2 ? p.wsr : p.wsi) + (id.tt * 128 * 4096 + (id.kq & 1) * 2048 + id.et * 128);
  sfor<0, 48>([&](auto ic) __attribute__((always_inline)) {
    constexpr int i = decltype(ic)::v;
    if constexpr (i == 8) wait_flag(p.ws, fo0, tag, f0);
    if constexpr (i == 35) { wait_flag(p.ws, fo1, tag, f1); wait_flag(p.ws, fo2, tag, f2); }
    if constexpr (i == 19 || i == 35 || i == 47) wait_flag(p.ws, fo0, tag, f0);
    bar();
    if constexpr (i == -1) tq1 = __builtin_amdgcn_s_memrealtime();
    if constexpr (i == 0) {
      if (b < 240 && lane < 32) {
        LAS float* cp = (LAS float*)(L + T_CP + lane * 4);
        const float u0 = (cp[0] + cp[64]) + (cp[128] + cp[192]), u1 = (cp[32] + cp[96]) + (cp[160] + cp[224]);
        const u32x4 o = {__builtin_bit_cast(unsigned, u0), tlo, __builtin_bit_cast(unsigned, u1), thi};
        __builtin_amdgcn_raw_buffer_store_b128(o, rws, W_CSP + (b * 32 + lane) * 16, 0, AUX_SC1);
      }
    }
    if constexpr (i == DRAIN_G) st_flag(p.ws, W_FG + b * 8, tag, lane);
    if constexpr (i == 5) ld_gr();
    if constexpr (i == 6) {
      const int l20 = lane < 20 ? lane : 0;
      fo0 = W_FG + ((l20 >> 1) * 24 + pcl + (l20 & 1)) * 8;
      f0 = ld_flag(p.ws, fo0);
    }
    if constexpr (i == 8) {
      wait_tags<2>(gr, tlo, thi, ld_gr);
      *(LAS unsigned*)(L + T_CS + go * 4) = gr[0][0];
      *(LAS unsigned*)(L + T_CS + (40 + go) * 4) = gr[1][0];
    }
    if constexpr (i == 8) ld_cq();
    if constexpr (i == 12) {
      if (b < 240) {
        const cptr sn = (cptr)p.wdn + (wk * 32 * 1280 + wc * 256);
        const unsigned vn = (lane >> 4) * 1280 + (lane & 15) * 16;
        SELNT(NT_W, dma4)(sn, vn, vn + 4096, vn + 2 * 4096, vn + 3 * 4096, lb + T_BIG);
        SELNT(NT_W, dma4)(sn + 16 * 1280, vn, vn + 4096, vn + 2 * 4096, vn + 3 * 4096, lb + T_BIG + 4096);
      }
    }
    if constexpr (i == 10) wait_tags<10>(cq, tlo, thi, ld_cq);
    if constexpr (i == 11) {
#pragma unroll
      for (int m = 0; m < 2; ++m)
#pragma unroll
        for (int h = 0; h < 2; ++h) {
          const unsigned col = (lane >> 5) * 64 + h * 32 + (lane & 31) - (id.cb & 1) * 32;
          const float v = ((asf(cq[m][2 * h]) + asf(cq[2 + m][2 * h])) + (asf(cq[4 + m][2 * h]) + asf(cq[6 + m][2 * h]))) + asf(cq[8 + m][2 * h]);
          *(LAS float*)(col < 96 ? L + T_CG + (m * 96 + col) * 4 : L + T_PAD) = v;
        }
    }
    if constexpr (i >= 19 && i < 23) {
      if (b < 240) {
        LAS char* a = L + T_BIG + (4 * (lane >> 4) + ((lane & 15) >> 2)) * 256 + 8 * (lane & 3);
#pragma unroll
        for (int c = (i - 19) * 2; c < (i - 19) * 2 + 2; ++c) {
          const bf16x8 f = cat(ldstr(a + c * 32), ldstr(a + 4096 + c * 32));
          const int eg = wc * 8 + c, cc = eg / 5, g5 = eg - cc * 5;
          __builtin_amdgcn_raw_buffer_store_b128(__builtin_bit_cast(u32x4, f), rws, lane * 16 + W_WDT + ((cc * 48 + wk) * 5 + g5) * 1024, 0, AUX_SC1);
        }
      }
    }
    if constexpr (i == 14) st_flag(p.ws, W_FB + (id.rb * 16 + id.cb) * 8, tag, lane);
    if constexpr (i == 16) code_touch();
    if constexpr (i == 26) { wait_vm<0>(); st_flag(p.ws, W_FA + b * 8, tag, lane); }
    if constexpr (i == 17) {
      fo0 = W_FB + ((8 * (id.kq & 1) + (lane & 7)) * 16 + 8 * (id.kq >> 1) + 2 * id.et + ((lane >> 3) & 1)) * 8;
      f0 = ld_flag(p.ws, fo0);
    }
    if constexpr (i == 31) {
      fo1 = W_FA + ((lane < 48 ? lane : 0) * 5 + (c80 * 5) / 8) * 8;
      fo2 = W_FA + ((lane < 48 ? lane : 0) * 5 + (c80 * 5 + 4) / 8) * 8;
      f1 = ld_flag(p.ws, fo1); f2 = ld_flag(p.ws, fo2);
    }
    if constexpr (i == 27) st_flag(p.ws, W_FH + (id.rb * 16 + id.cb) * 8, tag, lane);
    if constexpr (i == 30) code_touch();
    if constexpr (i == 32) { fo0 = W_FH + (id.tt * 16 + (lane & 15)) * 8; f0 = ld_flag(p.ws, fo0); }
    if constexpr (i == 35) {
      gr[0] = __builtin_amdgcn_raw_buffer_load_b128(rws, W_FA + lane * 32, 0, AUX_SC1);
      gr[1] = __builtin_amdgcn_raw_buffer_load_b128(rws, W_FA + lane * 32 + 16, 0, AUX_SC1);
    }
    if constexpr (i == 37) {
      const bool all = __all(((gr[0][0] ^ tlo) | (gr[0][1] ^ thi) | (gr[0][2] ^ tlo) | (gr[0][3] ^ thi) | (gr[1][0] ^ tlo) | (gr[1][1] ^ thi) | (gr[1][2] ^ tlo) | (gr[1][3] ^ thi)) == 0);
      if (b == 0) {
        if (!all) {
#pragma nounroll
          for (int k = 0; k < 4; ++k) wait_flag(p.ws, W_FA + (k * 64 + lane) * 8, tag, ~tag);
        }
        st_flag(p.ws, W_EPOCH, tag, lane);
      }
    }
    if constexpr (i == DRAIN_P) st_flag(p.ws, W_FP + (tile * 4 + id.kq) * 8, tag, lane);
    if constexpr (i == 45) { fo0 = W_FP + (tile * 4 + (lane & 3)) * 8; f0 = ld_flag(p.ws, fo0); }
    if constexpr (WT0 >= 0 && i >= WT0 && i < WT0 + 8 * WTS && (i - WT0) % WTS == 0) {
      constexpr int n = (i - WT0) / WTS;
      touch(wsb + ((n & 1) * 64 * 4096 + (n >> 1) * 512), lane * 4096, lb + T_PAD);
    }
  });
  { u64 mn = ~0ull, mx = 0;
    for (int k = 0; k < 4; ++k) { u64 v = ld_flag(p.ws, W_END + (k * 64 + lane) * 8); mn = v < mn ? v : mn; mx = v > mx ? v : mx; }
    for (int o = 32; o; o >>= 1) { u64 a = __shfl_xor(mn, o), c = __shfl_xor(mx, o); mn = a < mn ? a : mn; mx = c > mx ? c : mx; }
    u64 d = mx - mn; if (d > 2000) d = 2000;
    *(LAS u64*)(L + TAB + 14336) = b == 5 ? d * 5 : 0; }
  __builtin_amdgcn_s_waitcnt(0x0F70);
}

__global__ __launch_bounds__(NTHR, 2) void fused_kernel(Params p) {
  extern __shared__ __attribute__((aligned(16))) char smem[];
  u64 pc;
  asm volatile("s_getpc_b64 %0" : "=s"(pc));
  LAS char* L = (LAS char*)smem;
  const unsigned lb = (unsigned)(size_t)L;
  const int lane = threadIdx.x & 63, w = __builtin_amdgcn_readfirstlane(threadIdx.x >> 6);
  const int b = blockIdx.x, g = b & 7, rr = b >> 3;
  Ids id;
  id.rb = (g >> 1) * 4 + (rr >> 3);
  id.cb = (g & 1) * 8 + (rr & 7);
  id.tt = (g & 1) * 8 + (rr & 7);
  id.et = rr >> 3;
  id.kq = g >> 1;
  if (w >= 4) {
    const int ld = w - 4;
    if (ld < 3) loader(p, L, lb, id, ld, lane);
    else helper(p, L, lb, id, lane, pc);
    const int n0 = ld * 64 + lane, n1 = n0 + 256, n2 = ld < 2 ? n0 + 512 : n0;
    unsigned x0 = (n0 / 10) * 1120 + n0 * 16, x1 = (n1 / 10) * 1120 + n1 * 16, x2 = (n2 / 10) * 1120 + n2 * 16;
    PIN(x0); PIN(x1); PIN(x2);
    const cptr so = (cptr)p.x + ((id.tt * 128 + (id.kq >> 1) * 64) * 1280 + (id.et * 2 + (id.kq & 1)) * 160);
    wait_vm<22>();
    bar();
    if (w == 7) { STAMP(48); }
    dma1(so, x0, lb + X_TILE + ld * 1024); dma1(so, x1, lb + X_TILE + (ld + 4) * 1024); dma1(so, x2, lb + X_TILE + (ld < 2 ? ld + 8 : ld) * 1024);
    wait_vm<3>();
    bar();
    if (w == 7) { STAMP(49); }
    aux_tail(p, L, ld, lane);
  } else compute(p, L, id, w);
  const int tid = w * 64 + fresh_lane();
  GAS char* const ob = (GAS char*)p.out + (size_t)(id.tt * 128 + (id.kq >> 1) * 64) * 1280 + (id.et * 160 + (id.kq & 1) * 80) * 2;
  unsigned o0 = (tid / 10) * 1120 + tid * 16, o1 = ((tid + 512) / 10) * 1120 + (tid + 512) * 16;
  PIN(o0); PIN(o1);
  bar();
  const u32x4 v0 = *(LAS u32x4*)(L + X_TILE + tid * 16);
  if (w < 2) {
    const u32x4 v1 = *(LAS u32x4*)(L + X_TILE + 8192 + tid * 16);
    *(GAS u32x4*)(ob + o1) = v1;
  }
  *(GAS u32x4*)(ob + o0) = v0;
  if (w == 7) { const u64 d = *(LAS u64*)(L + TAB + 14336); const u64 t0 = __builtin_amdgcn_s_memrealtime(); while (__builtin_amdgcn_s_memrealtime() - t0 < d) __builtin_amdgcn_s_sleep(1); }
}
}

extern "C" void kernel_launch(void* const* d_in, const int* in_sizes, int n_in, void* d_out, int out_size, void* d_ws, size_t ws_size,
                              hipStream_t stream) {
  Params p;
  p.x = (const u16*)d_in[0];
  p.wsr = (const u16*)d_in[1];
  p.wsi = (const u16*)d_in[2];
  p.wdr = (const u16*)d_in[3];
  p.wdi = (const u16*)d_in[4];
  p.wg = (const u16*)d_in[6];
  p.wu = (const u16*)d_in[7];
  p.wdn = (const u16*)d_in[8];
  p.out = (u16*)d_out;
  p.ws = (unsigned char*)d_ws;
  (void)hipFuncSetAttribute((const void*)fused_kernel, hipFuncAttributeMaxDynamicSharedMemorySize, LDS_BYTES);
  fused_kernel<<<256, NTHR, LDS_BYTES, stream>>>(p);
}
```

```cpp
#include <hip/hip_runtime.h>
#include <hip/hip_bf16.h>

typedef unsigned short u16;
typedef unsigned long long u64;
typedef short bf16x8 __attribute__((ext_vector_type(8)));
typedef short bf16x4 __attribute__((ext_vector_type(4)));
typedef float f32x4 __attribute__((ext_vector_type(4)));
typedef float f32x2 __attribute__((ext_vector_type(2)));
typedef unsigned u32x4 __attribute__((ext_vector_type(4)));
typedef unsigned u32x2 __attribute__((ext_vector_type(2)));
typedef int i32x4 __attribute__((ext_vector_type(4)));
typedef __bf16 bf2v __attribute__((ext_vector_type(2)));
#define LAS __attribute__((address_space(3)))
#define GAS __attribute__((address_space(1)))

namespace {
constexpr int S = 2048, D = 640, F = 1536;
constexpr int NTHR = 512;
constexpr int ARING = 0;
constexpr int BRING = 65536;
constexpr int TAB = 147456;
constexpr int T_ST = TAB;
constexpr int T_CS = TAB + 1024;
constexpr int T_CP = TAB + 1344;
constexpr int T_CG = TAB + 2368;
constexpr int T_PAD = TAB + 3328;
constexpr int T_EK = TAB + 12288;
constexpr int T_BIG = TAB + 4096;
constexpr int X_TILE = ARING + 3 * 16384;
constexpr int LDS_BYTES = 163840;
constexpr unsigned W_EPOCH = 0;
constexpr unsigned W_FA = 4096, W_FB = W_FA + 2048, W_FH = W_FB + 2048, W_FP = W_FH + 2048, W_FG = W_FP + 2048;
constexpr unsigned W_CSP = 139264;
constexpr unsigned W_ST = 81920;
constexpr unsigned W_CSD = W_ST + 32768;
constexpr unsigned W_ABT = 262144;
constexpr unsigned W_H = W_ABT + 2u * 4 * 64 * 10 * 1024;
constexpr unsigned W_WDT = W_H + 32u * 96 * 2 * 1024;
constexpr unsigned W_GU = W_WDT + 8u * 48 * 5 * 1024;
constexpr unsigned W_P = W_GU + 16u * 20 * 12 * 1024;
constexpr unsigned W_END = W_P + 256u * 4 * 10 * 1024;
constexpr unsigned W_STAMP = W_END;
constexpr int AUX_SC1 = 16;
#define DRAIN_G 3
#define PRO 2
#define DRAIN_P 41
#define CODE_BYTES 32512
#define WT0 -1
#define WTS 2
#define MAP 1
#define ROT (3 * (b >> 6) + ((b >> 3) & 7))
#define NT_W 1
#define NT_G 1
#define SELNT(c, f) ((c) ? f##nt : f)
#define GB 8

struct Params {
  const u16 *x, *wsr, *wsi, *wdr, *wdi, *wg, *wu, *wdn;
  u16* out;
  unsigned char* ws;
};

typedef const unsigned char* cptr;
#define DMA_BODY(POL) \
__device__ __forceinline__ void dma2##POL(cptr base, unsigned v0, unsigned v1, unsigned lds) { \
  asm volatile("s_mov_b32 m0, %3\n\ts_nop 0\n\tglobal_load_lds_dwordx4 %0, %2 " #POL "\n\tglobal_load_lds_dwordx4 %1, %2 offset:1024 " #POL \
               ::"v"(v0), "v"(v1), "s"(base), "s"(lds) : "memory"); \
} \
__device__ __forceinline__ void dma3##POL(cptr base, unsigned v0, unsigned v1, unsigned v2, unsigned lds) { \
  asm volatile("s_mov_b32 m0, %4\n\ts_nop 0\n\tglobal_load_lds_dwordx4 %0, %3 " #POL "\n\tglobal_load_lds_dwordx4 %1, %3 offset:1024 " #POL "\n\t" \
               "global_load_lds_dwordx4 %2, %3 offset:2048 " #POL ::"v"(v0), "v"(v1), "v"(v2), "s"(base), "s"(lds) : "memory"); \
} \
__device__ __forceinline__ void dma4##POL(cptr base, unsigned v0, unsigned v1, unsigned v2, unsigned v3, unsigned lds) { \
  asm volatile("s_mov_b32 m0, %5\n\ts_nop 0\n\tglobal_load_lds_dwordx4 %0, %4 " #POL "\n\tglobal_load_lds_dwordx4 %1, %4 offset:1024 " #POL "\n\t" \
               "global_load_lds_dwordx4 %2, %4 offset:2048 " #POL "\n\tglobal_load_lds_dwordx4 %3, %4 offset:3072 " #POL \
               ::"v"(v0), "v"(v1), "v"(v2), "v"(v3), "s"(base), "s"(lds) : "memory"); \
}
DMA_BODY()
DMA_BODY(nt)
DMA_BODY(sc1)
#define dma4s dma4sc1
__device__ __forceinline__ void dma1(cptr base, unsigned v, unsigned lds) {
  asm volatile("s_mov_b32 m0, %2\n\ts_nop 0\n\tglobal_load_lds_dwordx4 %0, %1" ::"v"(v), "s"(base), "s"(lds) : "memory");
}
__device__ __forceinline__ void dma4c(cptr base, unsigned v, unsigned lds) {
  asm volatile("s_mov_b32 m0, %2\n\ts_nop 0\n\tglobal_load_lds_dwordx4 %0, %1 sc1\n\tglobal_load_lds_dwordx4 %0, %1 offset:1024 sc1\n\t"
               "global_load_lds_dwordx4 %0, %1 offset:2048 sc1\n\tglobal_load_lds_dwordx4 %0, %1 offset:3072 sc1"
               ::"v"(v), "s"(base), "s"(lds) : "memory");
}
__device__ __forceinline__ void dma2c(cptr base, unsigned v, unsigned lds) {
  asm volatile("s_mov_b32 m0, %2\n\ts_nop 0\n\tglobal_load_lds_dwordx4 %0, %1 sc1\n\tglobal_load_lds_dwordx4 %0, %1 offset:1024 sc1"
               ::"v"(v), "s"(base), "s"(lds) : "memory");
}
__device__ __forceinline__ void touch(cptr base, unsigned voff, unsigned pad) {
  asm volatile("s_mov_b32 m0, %2\n\ts_nop 0\n\tglobal_load_lds_dword %0, %1" ::"v"(voff), "s"(base), "s"(pad) : "memory");
}
template <int N> __device__ __forceinline__ void wait_vm() { asm volatile("s_waitcnt vmcnt(%0)" ::"i"(N) : "memory"); }
#define SCHED0() __builtin_amdgcn_sched_barrier(0)
__device__ __forceinline__ void bar() {
  SCHED0();
  __builtin_amdgcn_s_waitcnt(0xC07F);
  asm volatile("s_barrier" ::: "memory");
  SCHED0();
}
__device__ __forceinline__ f32x4 mfma(bf16x8 a, bf16x8 b, f32x4 c) { return __builtin_amdgcn_mfma_f32_16x16x32_bf16(a, b, c, 0, 0, 0); }
__device__ __forceinline__ bf16x8 ldsr(LAS char* p) { return *(LAS bf16x8*)p; }
__device__ __forceinline__ bf16x4 ldstr(LAS char* p) { return __builtin_amdgcn_ds_read_tr16_b64_v4i16((LAS bf16x4*)p); }
__device__ __forceinline__ bf16x8 cat(bf16x4 a, bf16x4 b) { return __builtin_shufflevector(a, b, 0, 1, 2, 3, 4, 5, 6, 7); }
__device__ __forceinline__ unsigned pack2(float a, float b) {
  f32x2 v = {a, b};
  bf2v r = __builtin_convertvector(v, bf2v);
  return __builtin_bit_cast(unsigned, r);
}
__device__ __forceinline__ float asf(unsigned v) { return __builtin_bit_cast(float, v); }
__device__ __forceinline__ float bflo(unsigned v) { return __builtin_bit_cast(float, v << 16); }
__device__ __forceinline__ float bfhi(unsigned v) { return __builtin_bit_cast(float, v & 0xffff0000u); }
template <int CTRL> __device__ __forceinline__ float dpp_add(float v) {
  return v + __builtin_bit_cast(float, __builtin_amdgcn_update_dpp(0, __builtin_bit_cast(int, v), CTRL, 0xf, 0xf, true));
}
__device__ __forceinline__ float row8_sum(float v) {
  v = dpp_add<0xB1>(v); v = dpp_add<0x4E>(v); v = dpp_add<0x141>(v);
  return v;
}
__device__ __forceinline__ float dot_ones(unsigned v, float acc) {
  return __builtin_amdgcn_fdot2_f32_bf16(__builtin_bit_cast(bf2v, v), __builtin_bit_cast(bf2v, 0x3f803f80u), acc, false);
}
__device__ __forceinline__ u64 ld_flag(const unsigned char* ws, unsigned off) {
  return __hip_atomic_load((GAS u64*)(ws + off), __ATOMIC_RELAXED, __HIP_MEMORY_SCOPE_AGENT);
}
__device__ __forceinline__ u64 next_tag(u64 prev) { return prev * 6364136223846793005ull + 1442695040888963407ull; }

#define STAMP(i) do { } while (0)

struct Ids {
  int rb, cb, tt, et, kq;
};

template <int I> struct IC { static constexpr int v = I; };
template <int I, int N, class Fn> __device__ __forceinline__ void sfor(Fn&& f) {
  if constexpr (I < N) { f(IC<I>{}); sfor<I + 1, N>(f); }
}
__device__ __forceinline__ int fresh_lane() {
  int l;
  asm volatile("v_mbcnt_lo_u32_b32 %0, -1, 0\n\tv_mbcnt_hi_u32_b32 %0, -1, %0" : "=v"(l));
  return l;
}
__device__ __forceinline__ float sel(float a, float b, u64 m) {
  float o;
  asm("v_cndmask_b32_e64 %0, %1, %2, %3" : "=v"(o) : "v"(a), "v"(b), "s"(m));
  return o;
}
__device__ __forceinline__ float pick4(f32x4 v) {
  float a = v[0], b = v[1], c = v[2], d = v[3];
  asm volatile("s_nop 7" : "+v"(a), "+v"(b), "+v"(c), "+v"(d));
  return sel(sel(a, b, 0xAAAAAAAAAAAAAAAAull), sel(c, d, 0xAAAAAAAAAAAAAAAAull), 0xCCCCCCCCCCCCCCCCull);
}
#define PIN(x) asm volatile("" : "+v"(x))
template <int N>
__device__ __forceinline__ void finish(f32x4 (&dk)[N], const f32x4 (&d1)[N], f32x4 (&ys)[N], LAS char* xp) {
  f32x4 ex[N];
  u32x2 xv[N];
#pragma unroll
  for (int g = 0; g < N; ++g) xv[g] = *(volatile LAS u32x2*)(xp + g * 32);
  SCHED0();
#pragma unroll
  for (int g = 0; g < N; ++g) { dk[g] += d1[g]; ex[g] = dk[g] * -1.4426950409f; }
  SCHED0();
#pragma unroll
  for (int g = 0; g < N; ++g) {
#pragma unroll
    for (int e = 0; e < 4; ++e) ex[g][e] = __builtin_amdgcn_exp2f(ex[g][e]);
    dk[g] *= ys[g];
  }
  SCHED0();
#pragma unroll
  for (int g = 0; g < N; ++g) ex[g] += 1.f;
  SCHED0();
#pragma unroll
  for (int g = 0; g < N; ++g) {
#pragma unroll
    for (int e = 0; e < 4; ++e) ex[g][e] = __builtin_amdgcn_rcpf(ex[g][e]);
    ys[g] = f32x4{bflo(xv[g][0]), bfhi(xv[g][0]), bflo(xv[g][1]), bfhi(xv[g][1])};
  }
  SCHED0();
#pragma unroll
  for (int g = 0; g < N; ++g) {
    const f32x4 o = dk[g] * ex[g] + ys[g];
    *(volatile LAS u32x2*)(xp + g * 32) = u32x2{pack2(o[0], o[1]), pack2(o[2], o[3])};
  }
}

__device__ __forceinline__ void compute(const Params& p, LAS char* L, const Ids& id, int w) {
  int lane = fresh_lane(), r = lane & 15, q = lane >> 4;
  const __amdgpu_buffer_rsrc_t rws = __builtin_amdgcn_make_buffer_rsrc((void*)p.ws, 0, (int)W_END, 0x00020000);
  const f32x4 z4 = {0.f, 0.f, 0.f, 0.f};

  const bf16x8 ones = {0x3f80, 0x3f80, 0x3f80, 0x3f80, 0x3f80, 0x3f80, 0x3f80, 0x3f80};
  if (blockIdx.x < 240) {
    const int pc = blockIdx.x % 48, pr = blockIdx.x / 48;
    const unsigned va = (lane >> 3) * 3072 + (lane & 7) * 16;
    SELNT(NT_G, dma4)((cptr)(pc < 24 ? p.wg : p.wu) + ((pr * 128 + w * 32) * 3072 + (pc % 24) * 128), va, va + 23552, va + 2 * 23552, va + 3 * 23552,
         (unsigned)(size_t)L + BRING + (w ? w * 20480 + 10240 : 0));
    LAS char* a = L + BRING + (w ? w * 20480 + 10240 : 0) + (8 * q + (r >> 2)) * 128 + 8 * (r & 3);
    const unsigned go = lane * 16 + W_GU + (((pc % 24) * 4 * 20 + pr * 4 + w) * 2 + pc / 24) * 1024;
    wait_vm<0>();
    bf16x8 f[4];
#pragma unroll
    for (int c = 0; c < 4; ++c) f[c] = cat(ldstr(a + c * 32), ldstr(a + 512 + c * 32));
    f32x4 acc[4];
#pragma unroll
    for (int c = 0; c < 4; ++c) acc[c] = mfma(ones, f[c], z4);
    SCHED0();
#pragma unroll
    for (int c = 0; c < 4; ++c) __builtin_amdgcn_raw_buffer_store_b128(__builtin_bit_cast(u32x4, f[c]), rws, go + c * 40960, 0, AUX_SC1);
    SCHED0();
    float v01 = (q & 1) ? acc[1][0] : acc[0][0], v23 = (q & 1) ? acc[3][0] : acc[2][0];
    PIN(v01); PIN(v23);
    *(LAS float*)(L + T_CP + (w * 64 + lane) * 4) = (q & 2) ? v23 : v01;
  }

  bf16x8 xr[20][2];
  f32x4 accP[5][2], st[4];
  float cs[5];
  {
    f32x4 accS[2], accQ[2];
    bf16x8 pb[2][5];
    const int oK0 = r * 128 + ((q ^ (r >> 1)) & 7) * 16, oK1 = oK0 ^ 64;
    LAS char* xA[2] = {L + ARING + w * 4096 + oK0, L + ARING + w * 4096 + oK1};
    LAS char* pB[2] = {L + BRING + 10240 + oK0, L + BRING + 10240 + oK1};
    PIN(xA[0]); PIN(xA[1]); PIN(pB[0]); PIN(pB[1]);
    auto rdx = [&](int i, int ks, int t) { xr[2 * i + ks][t] = ldsr(xA[ks] + (i & 3) * 16384 + t * 2048); };
    auto rdw = [&](int i, int ks, int g) { pb[ks][g] = ldsr(pB[ks] + ((i & 3) ? (i & 3) * 20480 - 10240 : 0) + g * 2048); };
    auto stat = [&](int s, int k) {
      if (k < 2) accS[k] = mfma(xr[s][k], ones, s == 0 ? z4 : accS[k]);
      else accQ[k - 2] = mfma(xr[s][k - 2], xr[s][k - 2], s == 0 ? z4 : accQ[k - 2]);
    };
#pragma unroll
    for (int i = 0; i < 10; ++i) {
      if (i == DRAIN_G) wait_vm<0>();
      bar();
#pragma unroll
      for (int k = 0; k < 10; ++k) {
        if (i > 0) accP[k >> 1][k & 1] = mfma(xr[2 * i - 1][k & 1], pb[1][k >> 1], accP[k >> 1][k & 1]);
        if (k & 1) rdw(i, 0, k >> 1);
        else if (k < 8) rdx(i, k >> 2, (k >> 1) & 1);
        SCHED0();
      }
#pragma unroll
      for (int k = 0; k < 18; ++k) {
        if (k < 2) stat(2 * i, k);
        else if (k < 12) {
          accP[(k - 2) >> 1][k & 1] = mfma(xr[2 * i][k & 1], pb[0][(k - 2) >> 1], i == 0 ? z4 : accP[(k - 2) >> 1][k & 1]);
          if (k & 1) rdw(i, 1, (k - 2) >> 1);
        } else if (k < 14) stat(2 * i, k - 10);
        else stat(2 * i + 1, k - 14);
        SCHED0();
      }
    }
    lane = fresh_lane(); r = lane & 15; q = lane >> 4;
    const bool diag = (r >> 2) == q;
    LAS char* const tw = diag ? L + T_ST + (w * 32 + r) * 4 : L + T_PAD;
    LAS char* const te = diag ? L + T_EK + (w * 32 + r) * 16 : L + T_PAD;
    const float sg = id.cb >= 8 ? -1.f : 1.f;
    float sm[2], sq[2], rs[2];
#pragma unroll
    for (int k = 0; k < 10; ++k) {
      accP[k >> 1][k & 1] = mfma(xr[19][k & 1], pb[1][k >> 1], accP[k >> 1][k & 1]);
      if (k < 2) {
#pragma unroll
        for (int g = 3 * k; g < 3 * k + 3 && g < 5; ++g) cs[g] = *(LAS float*)(L + T_CS + (g * 16 + r) * 4);
      }
      if (k == 1 || k == 2) {
        const int t = k - 1;
        sm[t] = pick4(accS[t]) * (-1.0f / D); sq[t] = pick4(accQ[t]) * (1.0f / D);
        PIN(sm[t]); PIN(sq[t]);
      }
      if (k == 3) {
#pragma unroll
        for (int t = 0; t < 2; ++t) { sq[t] = fmaxf(sq[t] - sm[t] * sm[t], 0.f) + 1e-5f; PIN(sq[t]); }
#pragma unroll
        for (int t = 0; t < 2; ++t) { rs[t] = __builtin_amdgcn_rsqf(sq[t]); PIN(rs[t]); }
      }
      if (k == 4) {
#pragma unroll
        for (int t = 0; t < 2; ++t) {
          const float v = rs[t] * sg;
          *(LAS float*)(tw + t * 64) = v; *(LAS float*)(tw + 512 + t * 64) = v * sm[t];
        }
      }
      if (k == 5) {
#pragma unroll
        for (int n = 0; n < 4; ++n) st[n] = *(LAS f32x4*)(L + T_ST + (n >> 1) * 512 + (w * 32 + (n & 1) * 16 + 4 * q) * 4);
      }
      if (k == 6 || k == 7) {
        const int t = k - 6;
        *(LAS f32x4*)(te + t * 256) = f32x4{sm[t], -2.302208198f * rs[t], -0.1029432397f * rs[t] * rs[t] * rs[t], sq[t]};
      }
      SCHED0();
    }
  }
  LAS char* const fB0 = L + BRING + lane * 16;
  LAS char* const fB3 = L + BRING + 61440 + lane * 16;
  auto fragB = [&](int stage, int n) -> bf16x8 { return (stage & 3) == 3 ? ldsr(fB3 + n * 1024) : ldsr(fB0 + (stage & 3) * 20480 + n * 1024); };
  LAS char* const xB0 = L + ARING + w * 4096 + r * 128 + ((q ^ (r >> 1)) & 7) * 16;
  LAS char* const xB1 = L + ARING + w * 4096 + ((r * 128 + ((q ^ (r >> 1)) & 7) * 16) ^ 64);
  const unsigned abBase = W_ABT + (unsigned)((((id.cb >> 3) * 4 + ((id.cb & 7) >> 1)) * 64 + id.rb * 4 + w) * 10 + (id.cb & 1) * 5) * 1024u + lane * 16;

  u32x4 abo;
  f32x4 accH[2][4];
  bf16x8 wb[4][2];
  const unsigned hbase = W_H + (unsigned)(((id.rb * 2 + (w >> 1)) * 96 + id.cb * 6) * 2 + (w & 1)) * 1024u + lane * 16;
  float gv[8], uv[8], wv[8];
  f32x4 cgv, cuv, ek[2];
  auto h_tab = [&](int c) {
    cgv = *(LAS f32x4*)(L + T_CG + (c * 16 + 4 * q) * 4);
    cuv = *(LAS f32x4*)(L + T_CG + 384 + (c * 16 + 4 * q) * 4);
  };
  auto h_op = [&](int c, int n) {
    const int ph = n >> 3, x = n & 7, t = x >> 2, e = x & 3;
    if (ph == 0) { gv[x] = accH[c & 1][t][e] + ek[t][0] * cgv[e]; PIN(gv[x]); uv[x] = accH[c & 1][2 + t][e] + ek[t][0] * cuv[e]; PIN(uv[x]); }
    if (ph == 1) { wv[x] = gv[x] * (ek[t][1] + ek[t][2] * (gv[x] * gv[x])); PIN(wv[x]); }
    if (ph == 2) { wv[x] = __builtin_amdgcn_exp2f(wv[x]); PIN(wv[x]); uv[x] *= gv[x]; PIN(uv[x]); }
    if (ph == 3) { wv[x] = __builtin_amdgcn_rcpf(wv[x] * ek[t][3] + ek[t][3]); PIN(wv[x]); }
    if (ph == 4) {
      uv[x] *= wv[x]; PIN(uv[x]);
      if (x == 7) {
        u32x4 o = {pack2(uv[0], uv[1]), pack2(uv[2], uv[3]), pack2(uv[4], uv[5]), pack2(uv[6], uv[7])};
        __builtin_amdgcn_raw_buffer_store_b128(o, rws, hbase + c * 2048, 0, AUX_SC1);
      }
    }
  };
  const int wm = w >> 1, wn = w & 1;
  LAS char *aBase0, *aBase1, *yBase;
  bf16x8 xa0[4], xa1[4], yb0[5], yb1[5];
  auto rdg = [&](int g, int m) { wb[g & 3][m] = fragB(10 + g / 10, (g % 10) * 2 + m); };
  bar();
#pragma unroll
  for (int g = 0; g < 3; ++g) { rdg(g, 0); rdg(g, 1); }
  sfor<0, 120>([&](auto gi) __attribute__((always_inline)) {
    constexpr int g = decltype(gi)::v, c = g / 20, s = g % 20;
    if (g == 110) {
      const int l2 = fresh_lane(), r2 = l2 & 15, oK0 = r2 * 128 + (((l2 >> 4) ^ (r2 >> 1)) & 7) * 16;
      aBase0 = L + ARING + wm * 8192 + oK0; aBase1 = L + ARING + wm * 8192 + (oK0 ^ 64); yBase = L + BRING + wn * 5120 + l2 * 16;
    }
    if (g % 10 == 8) {
      if (g == 38) wait_vm<0>();
      bar();
    }
#pragma unroll
    for (int k = 0; k < 4; ++k) {
      accH[c & 1][k] = mfma(wb[g & 3][k >> 1], xr[s][k & 1], s == 0 ? z4 : accH[c & 1][k]);
      bool rd = true;
      if (g >= 118) {
        const int n = (g - 118) * 4 + k;
        if (n < 4) xa0[n] = ldsr(aBase0 + 2 * 16384 + n * 2048); else yb0[n - 4] = ldsr(yBase + 2 * 20480 + (n - 4) * 1024);
      } else if (g % 10 == 8) rdg(g + 2 + (k >> 1), k & 1);
      else if (g % 10 != 7 && k < 2) rdg(g + 3, k);
      else rd = false;
      if (!rd) {
        const int n = s % 10 == 7 ? 2 * s + k : 2 * s + k - 2;
        if (c == 0) {
          if (n < 25) {
            const int gg = n / 5, ph = n % 5;
            if (ph < 4) {
              const int t = ph >> 1, e = (ph & 1) * 2;
              unsigned pk = pack2(accP[gg][t][e] * st[t][e] + st[2 + t][e] * cs[gg], accP[gg][t][e + 1] * st[t][e + 1] + st[2 + t][e + 1] * cs[gg]);
              PIN(pk);
              abo[ph] = pk;
            } else __builtin_amdgcn_raw_buffer_store_b128(abo, rws, abBase + gg * 1024, 0, AUX_SC1);
          }
          if (n >= 10 && n < 26 && !(n & 1)) {
            const int m = (n - 10) >> 1;
            xr[14 + (m >> 1)][m & 1] = ldsr(((m & 2) ? xB1 : xB0) + ((7 + (m >> 2)) & 3) * 16384 + (m & 1) * 2048);
          }
          if (n == 34) ek[0] = *(LAS f32x4*)(L + T_EK + (w * 32 + r) * 16);
          if (n == 35) ek[1] = *(LAS f32x4*)(L + T_EK + (w * 32 + 16 + r) * 16);
        } else if (c < 5) h_op(c - 1, n);
        else if (n < 36) { h_op(4, n); if (n >= 32) h_op(4, n + 4); }
        if (n == (c == 0 ? 38 : 36)) h_tab(c);
      }
      SCHED0();
    }
  });

  f32x4 accY[5][4];
  auto y_stage = [&](int i, int h0) {
    LAS char* a0 = aBase0 + ((i + 2) & 3) * 16384; LAS char* a1 = aBase1 + ((i + 2) & 3) * 16384;
    LAS char* b = yBase + ((i + 2) & 3) * 20480;
    PIN(a0); PIN(a1); PIN(b);
    bar();
    int n = h0;
#pragma unroll
    for (int k = 0; k < 20; ++k) {
      accY[k >> 2][k & 3] = mfma(yb1[k >> 2], xa1[k & 3], accY[k >> 2][k & 3]);
      if (k < 4) xa0[k] = ldsr(a0 + k * 2048);
      else if (k < 9) yb0[k - 4] = ldsr(b + (k - 4) * 1024);
      else if (k < 11) yb1[k - 9] = ldsr(b + 10240 + (k - 9) * 1024);
      else if (k == 12) yb1[2] = ldsr(b + 10240 + 2048);
      else if (k == 16) yb1[3] = ldsr(b + 10240 + 3072);
      else if (n < 40) h_op(5, n++);
      SCHED0();
    }
#pragma unroll
    for (int k = 0; k < 20; ++k) {
      accY[k >> 2][k & 3] = mfma(yb0[k >> 2], xa0[k & 3], accY[k >> 2][k & 3]);
      if (k == 0) yb1[4] = ldsr(b + 10240 + 4096);
      else if (k < 5) xa1[k - 1] = ldsr(a1 + (k - 1) * 2048);
      else if (n < 40) h_op(5, n++);
      SCHED0();
    }
  };
#pragma unroll
  for (int k = 0; k < 20; ++k) {
    if (k == 0) yb0[4] = ldsr(yBase + 2 * 20480 + 4096);
    accY[k >> 2][k & 3] = mfma(yb0[k >> 2], xa0[k & 3], z4);
    if (k < 4) xa1[k] = ldsr(aBase1 + 2 * 16384 + k * 2048);
    else if (k < 9) yb1[k - 4] = ldsr(yBase + 2 * 20480 + 10240 + (k - 4) * 1024);
    else h_op(5, k - 9);
    SCHED0();
  }
  y_stage(1, 11); y_stage(2, 33);
#pragma nounroll
  for (int i = 3; i < 16; ++i) {
    if (i == 5) wait_vm<0>();
    y_stage(i, 40);
  }

  lane = fresh_lane(); r = lane & 15; q = lane >> 4;
  const int tile = id.tt * 4 + id.et;
  f32x4 accD[5][2];
#pragma unroll
  for (int g = 0; g < 5; ++g) { accD[g][0] = z4; accD[g][1] = z4; }
  LAS char* const hB = L + ARING + wm * 1024 + wn * 8192 + lane * 16 + wn * 8;
  LAS char* const dB = fB0 + wn * 10240;
  bf16x8 hh[2][2], dw[2][5];
  auto d_rd = [&](LAS char* a, LAS char* a2, LAS char* b, int ks, int m) {
    if (m < 2) hh[ks][m] = cat(*(volatile LAS bf16x4*)((m ? a2 : a) + ks * 4096), *(volatile LAS bf16x4*)((m ? a2 : a) + ks * 4096 + 2048));
    else dw[ks][m - 2] = ldsr(b + ks * 5120 + (m - 2) * 1024);
  };
  auto d_mm = [&](int ks, int k) { accD[k >> 1][k & 1] = mfma(dw[ks][k >> 1], hh[ks][k & 1], accD[k >> 1][k & 1]); };
  const unsigned pbase = W_P + (unsigned)(((tile * 4 + w) * 4 + id.kq) * 10) * 1024u + lane * 16;
  auto p_store = [&](int u) {
    u32x4 o[5];
#pragma unroll
    for (int g = 0; g < 5; ++g) {
      const f32x4 a = accY[g][2 * u], b = accY[g][2 * u + 1];
      o[g] = u32x4{pack2(a[0], a[1]), pack2(a[2], a[3]), pack2(b[0], b[1]), pack2(b[2], b[3])};
    }
    if (w != id.kq) {
#pragma unroll
      for (int g = 0; g < 5; ++g) __builtin_amdgcn_raw_buffer_store_b128(o[g], rws, pbase + (u * 5 + g) * 1024, 0, AUX_SC1);
    } else {
#pragma unroll
      for (int g = 0; g < 5; ++g) *(LAS u32x4*)(L + T_BIG + (u * 5 + g) * 1024 + lane * 16) = o[g];
    }
  };
  auto d_second = [&](LAS char* a, LAS char* a2, LAS char* b) {
#pragma unroll
    for (int k = 0; k < 10; ++k) {
      d_mm(0, k);
      if (k < 2) d_rd(a, a2, b, 1, k); else if (k < 4) d_rd(a, a2, b, 1, k + 3);
      SCHED0();
    }
  };
  auto d_stage = [&](int i) {
    LAS char* a = hB + ((i + 2) & 3) * 16384; LAS char* a2 = a + (8 - 16 * wn); LAS char* b = dB + ((i + 2) & 3) * 20480;
    PIN(a); PIN(a2); PIN(b);
    bar();
#pragma unroll
    for (int k = 0; k < 10; ++k) {
      d_mm(1, k);
      if (k < 7) d_rd(a, a2, b, 0, k); else d_rd(a, a2, b, 1, k - 5);
      SCHED0();
    }
    d_second(a, a2, b);
  };
  {
    LAS char* a = hB + 2 * 16384; LAS char* a2 = a + (8 - 16 * wn); LAS char* b = dB + 2 * 20480;
    PIN(a); PIN(a2); PIN(b);
    bar();
#pragma unroll
    for (int k = 0; k < 20; ++k) {
      accY[k >> 2][k & 3] = mfma(yb1[k >> 2], xa1[k & 3], accY[k >> 2][k & 3]);
      if (k < 7) d_rd(a, a2, b, 0, k); else if (k < 10) d_rd(a, a2, b, 1, k - 5);
      SCHED0();
    }
    d_second(a, a2, b);
  }
  p_store(0);
  p_store(1);
#pragma nounroll
  for (int i = 1; i < 12; ++i) {
    if (i == DRAIN_P - 38) wait_vm<0>();
    d_stage(i);
  }
#pragma unroll
  for (int k = 0; k < 10; ++k) {
    d_mm(1, k < 5 ? 2 * k + 1 : 2 * (9 - k));
    if (k >= 5) *(LAS f32x4*)(fB0 + ((w ^ 1) * 5 + k - 5) * 1024) = accD[k - 5][1];
    SCHED0();
  }
  LAS char* dp = fB0 + w * 5120;
  *(LAS f32x4*)(L + ARING + w * 2048 + 1024 + lane * 16) = accD[4][0];
  *(LAS f32x4*)(L + ARING + w * 2048 + lane * 16) = accD[3][0];
  LAS char* xp = L + X_TILE + (wm * 32 + 8 * (r >> 2) + 4 * wn + (r & 3)) * 160 + q * 8;
  PIN(xp); PIN(dp);
  bar();
  f32x4 dk[3], ys[3];
#pragma unroll
  for (int g = 0; g < 3; ++g) dk[g] = *(LAS f32x4*)(dp + g * 1024);
#pragma unroll
  for (int g = 0; g < 3; ++g) ys[g] = *(LAS f32x4*)(dp + 61440 + g * 1024);
  const f32x4 own[3] = {accD[0][0], accD[1][0], accD[2][0]};
  finish<3>(dk, own, ys, xp);
}

__device__ __forceinline__ void aux_tail(const Params& p, LAS char* L, int t, int lane) {
  const int po = (t >> 1) * 5120 + lane * 16 + (t & 1) * 8;
  u32x2 y[4][5];
#pragma unroll
  for (int g = 0; g < 5; ++g) {
    y[0][g] = *(volatile LAS u32x2*)(L + T_BIG + po + g * 1024);
    y[1][g] = *(volatile LAS u32x2*)(L + BRING + 2 * 20480 + po + g * 1024);
    y[2][g] = *(volatile LAS u32x2*)(L + BRING + 2 * 20480 + 10240 + po + g * 1024);
    y[3][g] = *(volatile LAS u32x2*)(L + ARING + 2 * 16384 + po + g * 1024);
  }
  SCHED0();
  LAS char* dp = L + ARING + t * 2048 + lane * 16;
  f32x4 ys[2];
#pragma unroll
  for (int g = 0; g < 5; ++g) {
    f32x4 v[4];
#pragma unroll
    for (int k = 0; k < 4; ++k) v[k] = f32x4{bflo(y[k][g][0]), bfhi(y[k][g][0]), bflo(y[k][g][1]), bfhi(y[k][g][1])};
    const f32x4 sum = (v[0] + v[1]) + (v[2] + v[3]);
    if (g < 3) *(LAS f32x4*)(L + BRING + 61440 + (t * 5 + g) * 1024 + lane * 16) = sum;
    else ys[g - 3] = sum;
  }
  const int r = lane & 15;
  LAS char* xp = L + X_TILE + ((t >> 1) * 32 + 8 * (r >> 2) + 4 * (t & 1) + (r & 3)) * 160 + (lane >> 4) * 8 + 96;
  LAS char* dq = L + BRING + t * 5120 + 3072 + lane * 16;
  PIN(xp); PIN(dp); PIN(dq); PIN(ys[0]); PIN(ys[1]);
  wait_vm<0>();
  bar();
  if (t == 3) { STAMP(50); }
  f32x4 dk[2], own[2];
#pragma unroll
  for (int g = 0; g < 2; ++g) { dk[g] = *(LAS f32x4*)(dq + g * 1024); own[g] = *(LAS f32x4*)(dp + g * 1024); }
  finish<2>(dk, own, ys, xp);
}

__device__ __forceinline__ void st_flag(unsigned char* ws, unsigned off, u64 v, int lane) {
  if (lane == 0) __hip_atomic_store((GAS u64*)(ws + off), v, __ATOMIC_RELAXED, __HIP_MEMORY_SCOPE_AGENT);
}
__device__ __forceinline__ void wait_flag(const unsigned char* ws, unsigned off, u64 tag, u64 seen) {
  if (!__all(seen == tag)) {
#pragma nounroll
    for (int spin = 0; spin < (1 << 16); ++spin)
      if (__all(ld_flag(ws, off) == tag)) break;
  }
  __builtin_amdgcn_fence(__ATOMIC_ACQUIRE, "wavefront");
}
template <int N, class Fn>
__device__ __forceinline__ void wait_tags(u32x4 (&g)[N], unsigned tlo, unsigned thi, Fn&& reload) {
  auto good = [&]() {
    unsigned bad = 0;
#pragma unroll
    for (int k = 0; k < N; ++k) bad |= (g[k][1] ^ tlo) | (g[k][3] ^ thi);
    return __all(bad == 0);
  };
  if (!good()) {
#pragma nounroll
    for (int spin = 0; spin < (1 << 16); ++spin) {
      reload();
      if (good()) break;
    }
  }
}
__device__ __forceinline__ void loader(const Params& p, LAS char* L, unsigned lb, const Ids& id, int ld, int lane) {
  const int b = blockIdx.x;
  const unsigned vl = lane * 16;
  const unsigned c0 = ((lane & 7) ^ (lane >> 4)) * 16, c1 = c0 ^ 64;
  const int rl = 8 * (lane >> 5) + ((lane >> 3) & 3);
  const int rot = ROT;
  {
    const unsigned vd0 = (lane >> 3) * 1280 + c0, vd1 = (lane >> 3) * 1280 + c1 + 9216;
    const unsigned v0 = ld < 2 ? rl * 1280 + c0 : vd0, v1 = ld < 2 ? (rl + 16) * 1280 + c1 - 1024 : vd1,
                   v2 = ld < 2 ? (rl + 4) * 1280 + c0 - 2048 : vd0 + 2 * 9216, v3 = ld < 2 ? (rl + 20) * 1280 + c1 - 3072 : vd1 + 2 * 9216;
    const cptr bd = (cptr)(id.cb < 8 ? p.wdr : p.wdi) + (id.cb & 7) * 80 * 1280;
    const cptr b1 = ld < 2 ? (cptr)p.x + (id.rb * 128 + ld * 64) * 1280 : bd;
    int kc = rot % 10;
#pragma nounroll
    for (int j = 0; j < 10; ++j) {
      if (j == 2 || j == 3) { if (ld == 2) wait_vm<10>(); else wait_vm<8>(); bar(); }
      if (j > 4) { if (ld == 2) wait_vm<20>(); else wait_vm<16>(); bar(); }
      const unsigned lw = lb + BRING + ((j & 3) ? (j & 3) * 20480 : 10240);
      const unsigned l = ld < 2 ? lb + ARING + (j & 3) * 16384 + ld * 8192 : lw;
      dma4(b1 + kc * 128, v0, v1, v2, v3, l);
      dma4(b1 + (kc * 128 + 32 * 1280), v0, v1, v2, v3, l + 4096);
      if (ld == 2) dma2(b1 + (kc * 128 + 64 * 1280), v0, v1, l + 8192);
      kc = kc == 9 ? 0 : kc + 1;
    }
    wait_vm<16>(); bar();
  }
  const unsigned vw0 = rl * 4096 + c0, vw1 = (rl + 16) * 4096 + c1 - 1024, vw2 = (rl + 4) * 4096 + c0 - 2048, vw3 = (rl + 20) * 4096 + c1 - 3072;
  const unsigned u0 = ld ? vl : vw0, u1 = ld ? vl : vw1, u2 = ld ? vl : vw2, u3 = ld ? vl : vw3;
  const cptr bw = (cptr)(id.kq < 2 ? p.wsr : p.wsi) + (id.tt * 128 * 4096 + (id.kq & 1) * 2048);
  const int g0 = ld ? 2 * ld - 2 : 4;
  const unsigned l0 = lb + BRING + g0 * 4096, l1 = ld ? l0 + 4096 : lb + ARING, ls1 = ld ? 20480 : 16384, l2 = lb + ARING + (ld + 1) * 4096;
  auto yk = [&](int y) { return (y & ~3) | ((y + id.et) & 3); };
  {
    const cptr bg = p.ws + W_GU + (unsigned)id.cb * 245760u;
    const int ka[2] = {((g0 + rot) % 10) * 4096, ((g0 + rot + 5) % 10) * 4096}, kb[2] = {((g0 + 1 + rot) % 10) * 4096, ((g0 + rot + 6) % 10) * 4096};
#pragma nounroll
    for (int j = 10; j < 22; ++j) {
      if (j != 11) {
        if (j <= 12 || ld == 0) wait_vm<8>(); else wait_vm<16>();
        bar();
      }
      const cptr so = bg + ((j - 10) >> 1) * 40960;
      dma4c(so + ((j & 1) ? ka[1] : ka[0]), vl, l0 + (j & 3) * 20480);
      if (ld) dma4c(so + ((j & 1) ? kb[1] : kb[0]), vl, l1 + (j & 3) * 20480);
      const int n = (j - 16) * 3 + ld;
      if (n >= 0 && n < 16) dma4(bw + (yk(n >> 2) * 128 + (n & 3) * 32 * 4096), vw0, vw1, vw2, vw3, lb + ARING + (((n >> 2) + 2) & 3) * 16384 + (n & 3) * 4096);
    }
  }
  const int c80 = id.et * 2 + (id.kq & 1);
  const cptr bh = p.ws + W_H + (unsigned)(id.tt * 2 + (id.kq >> 1)) * 196608u;
  const cptr bab = p.ws + W_ABT + ((unsigned)(((id.kq >> 1) * 4 + id.et) * 64 + (id.kq & 1) * 32) * 10240u + g0 * 4096);
  const cptr bwd = p.ws + W_WDT + (unsigned)c80 * 245760u;
  const cptr y1 = ld ? bab + 4096 : bw, y2 = bw + (ld + 1) * 32 * 4096;
  const int ym1 = ld ? 20480 : 128;
  const cptr d0 = bwd + g0 * 4096, d1 = ld ? d0 + 4096 : bh, d2 = bh + (ld + 1) * 4096;
#pragma nounroll
  for (int i = 19; i < 35; ++i) {
    if (i >= 26) wait_vm<24>(); else if (ld == 0) wait_vm<8>(); else wait_vm<16>();
    bar();
    const int k = yk(i - 19), sl = (i + 3) & 3;
    dma4c(bab + k * 20480, vl, l0 + sl * 20480);
    if (i >= 23 || ld) dma4s(y1 + k * ym1, u0, u1, u2, u3, l1 + sl * ls1);
    if (i >= 23) dma4(y2 + k * 128, vw0, vw1, vw2, vw3, l2 + sl * 16384);
  }
#pragma nounroll
  for (int i = 35; i < 47; ++i) {
    wait_vm<24>();
    bar();
    const int sl = (i + 3) & 3;
    dma4c(d0 + (i - 35) * 20480, vl, l0 + sl * 20480);
    dma4c(d1 + (i - 35) * ls1, vl, l1 + sl * ls1);
    dma4c(d2 + (i - 35) * 16384, vl, l2 + sl * 16384);
  }
  wait_vm<24>(); bar();
  const cptr bp = p.ws + W_P + ((unsigned)(((id.tt * 4 + id.et) * 4 + id.kq) * 4 * 10) * 1024u + ((id.kq + 1 + ld) & 3) * 10240);
  const unsigned l = lb + (ld == 0 ? ARING + 2 * 16384 : BRING + 2 * 20480 + (ld - 1) * 10240);
  dma4c(bp, vl, l); dma4c(bp + 4096, vl, l + 4096); dma2c(bp + 8192, vl, l + 8192);
}

__device__ __forceinline__ void helper(const Params& p, LAS char* L, unsigned lb, const Ids& id, int lane, u64 pc) {
  const int b = blockIdx.x;
  const __amdgpu_buffer_rsrc_t rws = __builtin_amdgcn_make_buffer_rsrc((void*)p.ws, 0, (int)W_END, 0x00020000);
  STAMP(60);
  __builtin_amdgcn_s_setprio(3);
  const u64 prev = ld_flag(p.ws, W_EPOCH);
  const int r8 = lane >> 3;
  u32x4 dv[10];
  const GAS char* ds = (const GAS char*)(b < 128 ? p.wdr : p.wdi) + (((b & 127) * 5 + (r8 < 5 ? r8 : 4)) * 1280 + (lane & 7) * 16);
#pragma unroll
  for (int i = 0; i < 10; ++i) dv[i] = *(const GAS u32x4*)(ds + i * 128);
  auto code_touch = [&]() {
    const unsigned off = (((b >> 3) % ((CODE_BYTES + 8191) / 8192)) * 64 + lane) * 128;
    touch((cptr)(pc & ~255ull), off < CODE_BYTES ? off : 0, lb + T_PAD);
  };
  code_touch();
  const u64 tag = next_tag(prev);
  const unsigned tlo = (unsigned)tag, thi = (unsigned)(tag >> 32);
  {
    float t4[4] = {0.f, 0.f, 0.f, 0.f};
#pragma unroll
    for (int i = 0; i < 10; ++i)
#pragma unroll
      for (int e = 0; e < 4; ++e) t4[e] = dot_ones(dv[i][e], t4[e]);
    const unsigned sm = __builtin_bit_cast(unsigned, row8_sum((t4[0] + t4[1]) + (t4[2] + t4[3])));
    const u32x4 o = {sm, tlo, sm, thi};
    if ((lane & 7) == 0 && r8 < 5) __builtin_amdgcn_raw_buffer_store_b128(o, rws, W_CSD + (b * 5 + r8) * 16, 0, AUX_SC1);
  }
  const int pcl = (id.cb * 3) >> 1, tile = id.tt * 4 + id.et, c80 = id.et * 2 + (id.kq & 1);
  const int wk = b / 5, wc = b % 5;
  u64 f0 = 0, f1 = 0, f2 = 0;
  unsigned fo0 = 0, fo1 = 0, fo2 = 0;
  u32x4 gr[2], cq[10];
  const int go = lane < 40 ? lane : 0;
  auto ld_gr = [&]() {
    gr[0] = __builtin_amdgcn_raw_buffer_load_b128(rws, W_CSD + (id.cb * 80 + go) * 16, 0, AUX_SC1);
    gr[1] = __builtin_amdgcn_raw_buffer_load_b128(rws, W_CSD + (id.cb * 80 + 40 + go) * 16, 0, AUX_SC1);
  };
  auto ld_cq = [&]() {
#pragma unroll
    for (int k = 0; k < 10; ++k) cq[k] = __builtin_amdgcn_raw_buffer_load_b128(rws, W_CSP + ((k * 24 + pcl) * 32 + lane) * 16, 0, AUX_SC1);
  };
  const cptr wsb = (cptr)(id.kq < 2 ? p.wsr : p.wsi) + (id.tt * 128 * 4096 + (id.kq & 1) * 2048 + id.et * 128);
  sfor<0, 48>([&](auto ic) __attribute__((always_inline)) {
    constexpr int i = decltype(ic)::v;
    if constexpr (i == 8) wait_flag(p.ws, fo0, tag, f0);
    if constexpr (i == 35) { wait_flag(p.ws, fo1, tag, f1); wait_flag(p.ws, fo2, tag, f2); }
    if constexpr (i == 19 || i == 35 || i == 47) wait_flag(p.ws, fo0, tag, f0);
    bar();
    STAMP(i);
    if constexpr (i == 0) {
      if (b < 240 && lane < 32) {
        LAS float* cp = (LAS float*)(L + T_CP + lane * 4);
        const float u0 = (cp[0] + cp[64]) + (cp[128] + cp[192]), u1 = (cp[32] + cp[96]) + (cp[160] + cp[224]);
        const u32x4 o = {__builtin_bit_cast(unsigned, u0), tlo, __builtin_bit_cast(unsigned, u1), thi};
        __builtin_amdgcn_raw_buffer_store_b128(o, rws, W_CSP + (b * 32 + lane) * 16, 0, AUX_SC1);
      }
    }
    if constexpr (i == DRAIN_G) st_flag(p.ws, W_FG + b * 8, tag, lane);
    if constexpr (i == 5) ld_gr();
    if constexpr (i == 6) {
      const int l20 = lane < 20 ? lane : 0;
      fo0 = W_FG + ((l20 >> 1) * 24 + pcl + (l20 & 1)) * 8;
      f0 = ld_flag(p.ws, fo0);
    }
    if constexpr (i == 8) {
      wait_tags<2>(gr, tlo, thi, ld_gr);
      *(LAS unsigned*)(L + T_CS + go * 4) = gr[0][0];
      *(LAS unsigned*)(L + T_CS + (40 + go) * 4) = gr[1][0];
    }
    if constexpr (i == 8) ld_cq();
    if constexpr (i == 12) {
      if (b < 240) {
        const cptr sn = (cptr)p.wdn + (wk * 32 * 1280 + wc * 256);
        const unsigned vn = (lane >> 4) * 1280 + (lane & 15) * 16;
        SELNT(NT_W, dma4)(sn, vn, vn + 4096, vn + 2 * 4096, vn + 3 * 4096, lb + T_BIG);
        SELNT(NT_W, dma4)(sn + 16 * 1280, vn, vn + 4096, vn + 2 * 4096, vn + 3 * 4096, lb + T_BIG + 4096);
      }
    }
    if constexpr (i == 10) wait_tags<10>(cq, tlo, thi, ld_cq);
    if constexpr (i == 11) {
#pragma unroll
      for (int m = 0; m < 2; ++m)
#pragma unroll
        for (int h = 0; h < 2; ++h) {
          const unsigned col = (lane >> 5) * 64 + h * 32 + (lane & 31) - (id.cb & 1) * 32;
          const float v = ((asf(cq[m][2 * h]) + asf(cq[2 + m][2 * h])) + (asf(cq[4 + m][2 * h]) + asf(cq[6 + m][2 * h]))) + asf(cq[8 + m][2 * h]);
          *(LAS float*)(col < 96 ? L + T_CG + (m * 96 + col) * 4 : L + T_PAD) = v;
        }
    }
    if constexpr (i >= 19 && i < 23) {
      if (b < 240) {
        LAS char* a = L + T_BIG + (4 * (lane >> 4) + ((lane & 15) >> 2)) * 256 + 8 * (lane & 3);
#pragma unroll
        for (int c = (i - 19) * 2; c < (i - 19) * 2 + 2; ++c) {
          const bf16x8 f = cat(ldstr(a + c * 32), ldstr(a + 4096 + c * 32));
          const int eg = wc * 8 + c, cc = eg / 5, g5 = eg - cc * 5;
          __builtin_amdgcn_raw_buffer_store_b128(__builtin_bit_cast(u32x4, f), rws, lane * 16 + W_WDT + ((cc * 48 + wk) * 5 + g5) * 1024, 0, AUX_SC1);
        }
      }
    }
    if constexpr (i == 14) st_flag(p.ws, W_FB + (id.rb * 16 + id.cb) * 8, tag, lane);
    if constexpr (i == 16) code_touch();
    if constexpr (i == 26) { wait_vm<0>(); st_flag(p.ws, W_FA + b * 8, tag, lane); }
    if constexpr (i == 17) {
      fo0 = W_FB + ((8 * (id.kq & 1) + (lane & 7)) * 16 + 8 * (id.kq >> 1) + 2 * id.et + ((lane >> 3) & 1)) * 8;
      f0 = ld_flag(p.ws, fo0);
    }
    if constexpr (i == 31) {
      fo1 = W_FA + ((lane < 48 ? lane : 0) * 5 + (c80 * 5) / 8) * 8;
      fo2 = W_FA + ((lane < 48 ? lane : 0) * 5 + (c80 * 5 + 4) / 8) * 8;
      f1 = ld_flag(p.ws, fo1); f2 = ld_flag(p.ws, fo2);
    }
    if constexpr (i == 27) st_flag(p.ws, W_FH + (id.rb * 16 + id.cb) * 8, tag, lane);
    if constexpr (i == 30) code_touch();
    if constexpr (i == 32) { fo0 = W_FH + (id.tt * 16 + (lane & 15)) * 8; f0 = ld_flag(p.ws, fo0); }
    if constexpr (i == 35) {
      gr[0] = __builtin_amdgcn_raw_buffer_load_b128(rws, W_FA + lane * 32, 0, AUX_SC1);
      gr[1] = __builtin_amdgcn_raw_buffer_load_b128(rws, W_FA + lane * 32 + 16, 0, AUX_SC1);
    }
    if constexpr (i == 37) {
      const bool all = __all(((gr[0][0] ^ tlo) | (gr[0][1] ^ thi) | (gr[0][2] ^ tlo) | (gr[0][3] ^ thi) | (gr[1][0] ^ tlo) | (gr[1][1] ^ thi) | (gr[1][2] ^ tlo) | (gr[1][3] ^ thi)) == 0);
      if (b == 0) {
        if (!all) {
#pragma nounroll
          for (int k = 0; k < 4; ++k) wait_flag(p.ws, W_FA + (k * 64 + lane) * 8, tag, ~tag);
        }
        st_flag(p.ws, W_EPOCH, tag, lane);
      }
    }
    if constexpr (i == DRAIN_P) st_flag(p.ws, W_FP + (tile * 4 + id.kq) * 8, tag, lane);
    if constexpr (i == 45) { fo0 = W_FP + (tile * 4 + (lane & 3)) * 8; f0 = ld_flag(p.ws, fo0); }
    if constexpr (WT0 >= 0 && i >= WT0 && i < WT0 + 8 * WTS && (i - WT0) % WTS == 0) {
      constexpr int n = (i - WT0) / WTS;
      touch(wsb + ((n & 1) * 64 * 4096 + (n >> 1) * 512), lane * 4096, lb + T_PAD);
    }
  });
  __builtin_amdgcn_s_waitcnt(0x0F70);
}

__global__ __launch_bounds__(NTHR, 2) void fused_kernel(Params p) {
  extern __shared__ __attribute__((aligned(16))) char smem[];
  u64 pc;
  asm volatile("s_getpc_b64 %0" : "=s"(pc));
  LAS char* L = (LAS char*)smem;
  const unsigned lb = (unsigned)(size_t)L;
  const int lane = threadIdx.x & 63, w = __builtin_amdgcn_readfirstlane(threadIdx.x >> 6);
  const int b = blockIdx.x, g = b & 7, rr = b >> 3;
  Ids id;
  id.rb = (g >> 1) * 4 + (rr >> 3);
  id.cb = (g & 1) * 8 + (rr & 7);
  id.tt = (g & 1) * 8 + (rr & 7);
  id.et = rr >> 3;
  id.kq = g >> 1;
  if (w >= 4) {
    const int ld = w - 4;
    if (ld < 3) loader(p, L, lb, id, ld, lane);
    else helper(p, L, lb, id, lane, pc);
    const int n0 = ld * 64 + lane, n1 = n0 + 256, n2 = ld < 2 ? n0 + 512 : n0;
    unsigned x0 = (n0 / 10) * 1120 + n0 * 16, x1 = (n1 / 10) * 1120 + n1 * 16, x2 = (n2 / 10) * 1120 + n2 * 16;
    PIN(x0); PIN(x1); PIN(x2);
    const cptr so = (cptr)p.x + ((id.tt * 128 + (id.kq >> 1) * 64) * 1280 + (id.et * 2 + (id.kq & 1)) * 160);
    wait_vm<22>();
    bar();
    if (w == 7) { STAMP(48); }
    dma1(so, x0, lb + X_TILE + ld * 1024); dma1(so, x1, lb + X_TILE + (ld + 4) * 1024); dma1(so, x2, lb + X_TILE + (ld < 2 ? ld + 8 : ld) * 1024);
    wait_vm<3>();
    bar();
    if (w == 7) { STAMP(49); }
    aux_tail(p, L, ld, lane);
  } else compute(p, L, id, w);
  const int tid = w * 64 + fresh_lane();
  GAS char* const ob = (GAS char*)p.out + (size_t)(id.tt * 128 + (id.kq >> 1) * 64) * 1280 + (id.et * 160 + (id.kq & 1) * 80) * 2;
  unsigned o0 = (tid / 10) * 1120 + tid * 16, o1 = ((tid + 512) / 10) * 1120 + (tid + 512) * 16;
  PIN(o0); PIN(o1);
  bar();
  const u32x4 v0 = *(LAS u32x4*)(L + X_TILE + tid * 16);
  if (w < 2) {
    const u32x4 v1 = *(LAS u32x4*)(L + X_TILE + 8192 + tid * 16);
    *(GAS u32x4*)(ob + o1) = v1;
  }
  *(GAS u32x4*)(ob + o0) = v0;
}
}

extern "C" void kernel_launch(void* const* d_in, const int* in_sizes, int n_in, void* d_out, int out_size, void* d_ws, size_t ws_size,
                              hipStream_t stream) {
  Params p;
  p.x = (const u16*)d_in[0];
  p.wsr = (const u16*)d_in[1];
  p.wsi = (const u16*)d_in[2];
  p.wdr = (const u16*)d_in[3];
  p.wdi = (const u16*)d_in[4];
  p.wg = (const u16*)d_in[6];
  p.wu = (const u16*)d_in[7];
  p.wdn = (const u16*)d_in[8];
  p.out = (u16*)d_out;
  p.ws = (unsigned char*)d_ws;
  (void)hipFuncSetAttribute((const void*)fused_kernel, hipFuncAttributeMaxDynamicSharedMemorySize, LDS_BYTES);
  fused_kernel<<<256, NTHR, LDS_BYTES, stream>>>(p);
}
```

```cpp
#include <hip/hip_runtime.h>
#include <hip/hip_bf16.h>

typedef unsigned short u16;
typedef unsigned long long u64;
typedef short bf16x8 __attribute__((ext_vector_type(8)));
typedef short bf16x4 __attribute__((ext_vector_type(4)));
typedef float f32x4 __attribute__((ext_vector_type(4)));
typedef float f32x2 __attribute__((ext_vector_type(2)));
typedef unsigned u32x4 __attribute__((ext_vector_type(4)));
typedef unsigned u32x2 __attribute__((ext_vector_type(2)));
typedef int i32x4 __attribute__((ext_vector_type(4)));
typedef __bf16 bf2v __attribute__((ext_vector_type(2)));
#define LAS __attribute__((address_space(3)))
#define GAS __attribute__((address_space(1)))

namespace {
constexpr int S = 2048, D = 640, F = 1536;
constexpr int NTHR = 512;
constexpr int ARING = 0;
constexpr int BRING = 65536;
constexpr int TAB = 147456;
constexpr int T_ST = TAB;
constexpr int T_CS = TAB + 1024;
constexpr int T_CP = TAB + 1344;
constexpr int T_CG = TAB + 2368;
constexpr int T_PAD = TAB + 3328;
constexpr int T_EK = TAB + 12288;
constexpr int T_BIG = TAB + 4096;
constexpr int X_TILE = ARING + 3 * 16384;
constexpr int LDS_BYTES = 163840;
constexpr unsigned W_EPOCH = 0;
constexpr unsigned W_FA = 4096, W_FB = W_FA + 2048, W_FH = W_FB + 2048, W_FP = W_FH + 2048, W_FG = W_FP + 2048;
constexpr unsigned W_CSP = 139264;
constexpr unsigned W_ST = 81920;
constexpr unsigned W_CSD = W_ST + 32768;
constexpr unsigned W_ABT = 262144;
constexpr unsigned W_H = W_ABT + 2u * 4 * 64 * 10 * 1024;
constexpr unsigned W_WDT = W_H + 32u * 96 * 2 * 1024;
constexpr unsigned W_GU = W_WDT + 8u * 48 * 5 * 1024;
constexpr unsigned W_P = W_GU + 16u * 20 * 12 * 1024;
constexpr unsigned W_END = W_P + 256u * 4 * 10 * 1024;
constexpr unsigned W_STAMP = W_END;
constexpr int AUX_SC1 = 16;
#define DRAIN_G 3
#define PRO 2
#define DRAIN_P 41
#define CODE_BYTES 33536
#define WT0 -1
#define WTS 2
#define MAP 1
#define ROT (3 * (b >> 6) + ((b >> 3) & 7))
#define NT_W 0
#define NT_G 0
#define SELNT(c, f) ((c) ? f##nt : f)
#define GB 8

struct Params {
  const u16 *x, *wsr, *wsi, *wdr, *wdi, *wg, *wu, *wdn;
  u16* out;
  unsigned char* ws;
};

typedef const unsigned char* cptr;
#define DMA_BODY(POL) \
__device__ __forceinline__ void dma2##POL(cptr base, unsigned v0, unsigned v1, unsigned lds) { \
  asm volatile("s_mov_b32 m0, %3\n\ts_nop 0\n\tglobal_load_lds_dwordx4 %0, %2 " #POL "\n\tglobal_load_lds_dwordx4 %1, %2 offset:1024 " #POL \
               ::"v"(v0), "v"(v1), "s"(base), "s"(lds) : "memory"); \
} \
__device__ __forceinline__ void dma3##POL(cptr base, unsigned v0, unsigned v1, unsigned v2, unsigned lds) { \
  asm volatile("s_mov_b32 m0, %4\n\ts_nop 0\n\tglobal_load_lds_dwordx4 %0, %3 " #POL "\n\tglobal_load_lds_dwordx4 %1, %3 offset:1024 " #POL "\n\t" \
               "global_load_lds_dwordx4 %2, %3 offset:2048 " #POL ::"v"(v0), "v"(v1), "v"(v2), "s"(base), "s"(lds) : "memory"); \
} \
__device__ __forceinline__ void dma4##POL(cptr base, unsigned v0, unsigned v1, unsigned v2, unsigned v3, unsigned lds) { \
  asm volatile("s_mov_b32 m0, %5\n\ts_nop 0\n\tglobal_load_lds_dwordx4 %0, %4 " #POL "\n\tglobal_load_lds_dwordx4 %1, %4 offset:1024 " #POL "\n\t" \
               "global_load_lds_dwordx4 %2, %4 offset:2048 " #POL "\n\tglobal_load_lds_dwordx4 %3, %4 offset:3072 " #POL \
               ::"v"(v0), "v"(v1), "v"(v2), "v"(v3), "s"(base), "s"(lds) : "memory"); \
}
DMA_BODY()
DMA_BODY(nt)
DMA_BODY(sc1)
#define dma4s dma4sc1
__device__ __forceinline__ void dma1(cptr base, unsigned v, unsigned lds) {
  asm volatile("s_mov_b32 m0, %2\n\ts_nop 0\n\tglobal_load_lds_dwordx4 %0, %1" ::"v"(v), "s"(base), "s"(lds) : "memory");
}
__device__ __forceinline__ void dma4c(cptr base, unsigned v, unsigned lds) {
  asm volatile("s_mov_b32 m0, %2\n\ts_nop 0\n\tglobal_load_lds_dwordx4 %0, %1 sc1\n\tglobal_load_lds_dwordx4 %0, %1 offset:1024 sc1\n\t"
               "global_load_lds_dwordx4 %0, %1 offset:2048 sc1\n\tglobal_load_lds_dwordx4 %0, %1 offset:3072 sc1"
               ::"v"(v), "s"(base), "s"(lds) : "memory");
}
__device__ __forceinline__ void dma2c(cptr base, unsigned v, unsigned lds) {
  asm volatile("s_mov_b32 m0, %2\n\ts_nop 0\n\tglobal_load_lds_dwordx4 %0, %1 sc1\n\tglobal_load_lds_dwordx4 %0, %1 offset:1024 sc1"
               ::"v"(v), "s"(base), "s"(lds) : "memory");
}
__device__ __forceinline__ void touch(cptr base, unsigned voff, unsigned pad) {
  asm volatile("s_mov_b32 m0, %2\n\ts_nop 0\n\tglobal_load_lds_dword %0, %1" ::"v"(voff), "s"(base), "s"(pad) : "memory");
}
template <int N> __device__ __forceinline__ void wait_vm() { asm volatile("s_waitcnt vmcnt(%0)" ::"i"(N) : "memory"); }
#define SCHED0() __builtin_amdgcn_sched_barrier(0)
__device__ __forceinline__ void bar() {
  SCHED0();
  __builtin_amdgcn_s_waitcnt(0xC07F);
  asm volatile("s_barrier" ::: "memory");
  SCHED0();
}
__device__ __forceinline__ f32x4 mfma(bf16x8 a, bf16x8 b, f32x4 c) { return __builtin_amdgcn_mfma_f32_16x16x32_bf16(a, b, c, 0, 0, 0); }
__device__ __forceinline__ bf16x8 ldsr(LAS char* p) { return *(LAS bf16x8*)p; }
__device__ __forceinline__ bf16x4 ldstr(LAS char* p) { return __builtin_amdgcn_ds_read_tr16_b64_v4i16((LAS bf16x4*)p); }
__device__ __forceinline__ bf16x8 cat(bf16x4 a, bf16x4 b) { return __builtin_shufflevector(a, b, 0, 1, 2, 3, 4, 5, 6, 7); }
__device__ __forceinline__ unsigned pack2(float a, float b) {
  f32x2 v = {a, b};
  bf2v r = __builtin_convertvector(v, bf2v);
  return __builtin_bit_cast(unsigned, r);
}
__device__ __forceinline__ float asf(unsigned v) { return __builtin_bit_cast(float, v); }
__device__ __forceinline__ float bflo(unsigned v) { return __builtin_bit_cast(float, v << 16); }
__device__ __forceinline__ float bfhi(unsigned v) { return __builtin_bit_cast(float, v & 0xffff0000u); }
template <int CTRL> __device__ __forceinline__ float dpp_add(float v) {
  return v + __builtin_bit_cast(float, __builtin_amdgcn_update_dpp(0, __builtin_bit_cast(int, v), CTRL, 0xf, 0xf, true));
}
__device__ __forceinline__ float row8_sum(float v) {
  v = dpp_add<0xB1>(v); v = dpp_add<0x4E>(v); v = dpp_add<0x141>(v);
  return v;
}
__device__ __forceinline__ float dot_ones(unsigned v, float acc) {
  return __builtin_amdgcn_fdot2_f32_bf16(__builtin_bit_cast(bf2v, v), __builtin_bit_cast(bf2v, 0x3f803f80u), acc, false);
}
__device__ __forceinline__ u64 ld_flag(const unsigned char* ws, unsigned off) {
  return __hip_atomic_load((GAS u64*)(ws + off), __ATOMIC_RELAXED, __HIP_MEMORY_SCOPE_AGENT);
}
__device__ __forceinline__ u64 next_tag(u64 prev) { return prev * 6364136223846793005ull + 1442695040888963407ull; }

#define STAMP(i) do { } while (0)

struct Ids {
  int rb, cb, tt, et, kq;
};

template <int I> struct IC { static constexpr int v = I; };
template <int I, int N, class Fn> __device__ __forceinline__ void sfor(Fn&& f) {
  if constexpr (I < N) { f(IC<I>{}); sfor<I + 1, N>(f); }
}
__device__ __forceinline__ int fresh_lane() {
  int l;
  asm volatile("v_mbcnt_lo_u32_b32 %0, -1, 0\n\tv_mbcnt_hi_u32_b32 %0, -1, %0" : "=v"(l));
  return l;
}
__device__ __forceinline__ float sel(float a, float b, u64 m) {
  float o;
  asm("v_cndmask_b32_e64 %0, %1, %2, %3" : "=v"(o) : "v"(a), "v"(b), "s"(m));
  return o;
}
__device__ __forceinline__ float pick4(f32x4 v) {
  float a = v[0], b = v[1], c = v[2], d = v[3];
  asm volatile("s_nop 7" : "+v"(a), "+v"(b), "+v"(c), "+v"(d));
  return sel(sel(a, b, 0xAAAAAAAAAAAAAAAAull), sel(c, d, 0xAAAAAAAAAAAAAAAAull), 0xCCCCCCCCCCCCCCCCull);
}
#define PIN(x) asm volatile("" : "+v"(x))
template <int N>
__device__ __forceinline__ void finish(f32x4 (&dk)[N], const f32x4 (&d1)[N], f32x4 (&ys)[N], LAS char* xp) {
  f32x4 ex[N];
  u32x2 xv[N];
#pragma unroll
  for (int g = 0; g < N; ++g) xv[g] = *(volatile LAS u32x2*)(xp + g * 32);
  SCHED0();
#pragma unroll
  for (int g = 0; g < N; ++g) { dk[g] += d1[g]; ex[g] = dk[g] * -1.4426950409f; }
  SCHED0();
#pragma unroll
  for (int g = 0; g < N; ++g) {
#pragma unroll
    for (int e = 0; e < 4; ++e) ex[g][e] = __builtin_amdgcn_exp2f(ex[g][e]);
    dk[g] *= ys[g];
  }
  SCHED0();
#pragma unroll
  for (int g = 0; g < N; ++g) ex[g] += 1.f;
  SCHED0();
#pragma unroll
  for (int g = 0; g < N; ++g) {
#pragma unroll
    for (int e = 0; e < 4; ++e) ex[g][e] = __builtin_amdgcn_rcpf(ex[g][e]);
    ys[g] = f32x4{bflo(xv[g][0]), bfhi(xv[g][0]), bflo(xv[g][1]), bfhi(xv[g][1])};
  }
  SCHED0();
#pragma unroll
  for (int g = 0; g < N; ++g) {
    const f32x4 o = dk[g] * ex[g] + ys[g];
    *(volatile LAS u32x2*)(xp + g * 32) = u32x2{pack2(o[0], o[1]), pack2(o[2], o[3])};
  }
}

__device__ __forceinline__ void compute(const Params& p, LAS char* L, const Ids& id, int w) {
  int lane = fresh_lane(), r = lane & 15, q = lane >> 4;
  const __amdgpu_buffer_rsrc_t rws = __builtin_amdgcn_make_buffer_rsrc((void*)p.ws, 0, (int)W_END, 0x00020000);
  const f32x4 z4 = {0.f, 0.f, 0.f, 0.f};

  const bf16x8 ones = {0x3f80, 0x3f80, 0x3f80, 0x3f80, 0x3f80, 0x3f80, 0x3f80, 0x3f80};
  if (blockIdx.x < 240) {
    const int pc = blockIdx.x % 48, pr = blockIdx.x / 48;
    LAS char* a = L + BRING + (w ? w * 20480 + 10240 : 0) + (8 * q + (r >> 2)) * 128 + 8 * (r & 3);
    const unsigned go = lane * 16 + W_GU + (((pc % 24) * 4 * 20 + pr * 4 + w) * 2 + pc / 24) * 1024;
    wait_vm<0>();
    bf16x8 f[4];
#pragma unroll
    for (int c = 0; c < 4; ++c) f[c] = cat(ldstr(a + c * 32), ldstr(a + 512 + c * 32));
    f32x4 acc[4];
#pragma unroll
    for (int c = 0; c < 4; ++c) acc[c] = mfma(ones, f[c], z4);
    SCHED0();
#pragma unroll
    for (int c = 0; c < 4; ++c) __builtin_amdgcn_raw_buffer_store_b128(__builtin_bit_cast(u32x4, f[c]), rws, go + c * 40960, 0, AUX_SC1);
    SCHED0();
    float v01 = (q & 1) ? acc[1][0] : acc[0][0], v23 = (q & 1) ? acc[3][0] : acc[2][0];
    PIN(v01); PIN(v23);
    *(LAS float*)(L + T_CP + (w * 64 + lane) * 4) = (q & 2) ? v23 : v01;
  }

  bf16x8 xr[20][2];
  f32x4 accP[5][2], st[4];
  float cs[5];
  {
    f32x4 accS[2], accQ[2];
    bf16x8 pb[2][5];
    const int oK0 = r * 128 + ((q ^ (r >> 1)) & 7) * 16, oK1 = oK0 ^ 64;
    LAS char* xA[2] = {L + ARING + w * 4096 + oK0, L + ARING + w * 4096 + oK1};
    LAS char* pB[2] = {L + BRING + 10240 + oK0, L + BRING + 10240 + oK1};
    PIN(xA[0]); PIN(xA[1]); PIN(pB[0]); PIN(pB[1]);
    auto rdx = [&](int i, int ks, int t) { xr[2 * i + ks][t] = ldsr(xA[ks] + (i & 3) * 16384 + t * 2048); };
    auto rdw = [&](int i, int ks, int g) { pb[ks][g] = ldsr(pB[ks] + ((i & 3) ? (i & 3) * 20480 - 10240 : 0) + g * 2048); };
    auto stat = [&](int s, int k) {
      if (k < 2) accS[k] = mfma(xr[s][k], ones, s == 0 ? z4 : accS[k]);
      else accQ[k - 2] = mfma(xr[s][k - 2], xr[s][k - 2], s == 0 ? z4 : accQ[k - 2]);
    };
#pragma unroll
    for (int i = 0; i < 10; ++i) {
      if (i == DRAIN_G) wait_vm<0>();
      bar();
#pragma unroll
      for (int k = 0; k < 10; ++k) {
        if (i > 0) accP[k >> 1][k & 1] = mfma(xr[2 * i - 1][k & 1], pb[1][k >> 1], accP[k >> 1][k & 1]);
        if (k & 1) rdw(i, 0, k >> 1);
        else if (k < 8) rdx(i, k >> 2, (k >> 1) & 1);
        SCHED0();
      }
#pragma unroll
      for (int k = 0; k < 18; ++k) {
        if (k < 2) stat(2 * i, k);
        else if (k < 12) {
          accP[(k - 2) >> 1][k & 1] = mfma(xr[2 * i][k & 1], pb[0][(k - 2) >> 1], i == 0 ? z4 : accP[(k - 2) >> 1][k & 1]);
          if (k & 1) rdw(i, 1, (k - 2) >> 1);
        } else if (k < 14) stat(2 * i, k - 10);
        else stat(2 * i + 1, k - 14);
        SCHED0();
      }
    }
    lane = fresh_lane(); r = lane & 15; q = lane >> 4;
    const bool diag = (r >> 2) == q;
    LAS char* const tw = diag ? L + T_ST + (w * 32 + r) * 4 : L + T_PAD;
    LAS char* const te = diag ? L + T_EK + (w * 32 + r) * 16 : L + T_PAD;
    const float sg = id.cb >= 8 ? -1.f : 1.f;
    float sm[2], sq[2], rs[2];
#pragma unroll
    for (int k = 0; k < 10; ++k) {
      accP[k >> 1][k & 1] = mfma(xr[19][k & 1], pb[1][k >> 1], accP[k >> 1][k & 1]);
      if (k < 2) {
#pragma unroll
        for (int g = 3 * k; g < 3 * k + 3 && g < 5; ++g) cs[g] = *(LAS float*)(L + T_CS + (g * 16 + r) * 4);
      }
      if (k == 1 || k == 2) {
        const int t = k - 1;
        sm[t] = pick4(accS[t]) * (-1.0f / D); sq[t] = pick4(accQ[t]) * (1.0f / D);
        PIN(sm[t]); PIN(sq[t]);
      }
      if (k == 3) {
#pragma unroll
        for (int t = 0; t < 2; ++t) { sq[t] = fmaxf(sq[t] - sm[t] * sm[t], 0.f) + 1e-5f; PIN(sq[t]); }
#pragma unroll
        for (int t = 0; t < 2; ++t) { rs[t] = __builtin_amdgcn_rsqf(sq[t]); PIN(rs[t]); }
      }
      if (k == 4) {
#pragma unroll
        for (int t = 0; t < 2; ++t) {
          const float v = rs[t] * sg;
          *(LAS float*)(tw + t * 64) = v; *(LAS float*)(tw + 512 + t * 64) = v * sm[t];
        }
      }
      if (k == 5) {
#pragma unroll
        for (int n = 0; n < 4; ++n) st[n] = *(LAS f32x4*)(L + T_ST + (n >> 1) * 512 + (w * 32 + (n & 1) * 16 + 4 * q) * 4);
      }
      if (k == 6 || k == 7) {
        const int t = k - 6;
        *(LAS f32x4*)(te + t * 256) = f32x4{sm[t], -2.302208198f * rs[t], -0.1029432397f * rs[t] * rs[t] * rs[t], sq[t]};
      }
      SCHED0();
    }
  }
  LAS char* const fB0 = L + BRING + lane * 16;
  LAS char* const fB3 = L + BRING + 61440 + lane * 16;
  auto fragB = [&](int stage, int n) -> bf16x8 { return (stage & 3) == 3 ? ldsr(fB3 + n * 1024) : ldsr(fB0 + (stage & 3) * 20480 + n * 1024); };
  LAS char* const xB0 = L + ARING + w * 4096 + r * 128 + ((q ^ (r >> 1)) & 7) * 16;
  LAS char* const xB1 = L + ARING + w * 4096 + ((r * 128 + ((q ^ (r >> 1)) & 7) * 16) ^ 64);
  const unsigned abBase = W_ABT + (unsigned)((((id.cb >> 3) * 4 + ((id.cb & 7) >> 1)) * 64 + id.rb * 4 + w) * 10 + (id.cb & 1) * 5) * 1024u + lane * 16;

  u32x4 abo;
  f32x4 accH[2][4];
  bf16x8 wb[4][2];
  const unsigned hbase = W_H + (unsigned)(((id.rb * 2 + (w >> 1)) * 96 + id.cb * 6) * 2 + (w & 1)) * 1024u + lane * 16;
  float gv[8], uv[8], wv[8];
  f32x4 cgv, cuv, ek[2];
  auto h_tab = [&](int c) {
    cgv = *(LAS f32x4*)(L + T_CG + (c * 16 + 4 * q) * 4);
    cuv = *(LAS f32x4*)(L + T_CG + 384 + (c * 16 + 4 * q) * 4);
  };
  auto h_op = [&](int c, int n) {
    const int ph = n >> 3, x = n & 7, t = x >> 2, e = x & 3;
    if (ph == 0) { gv[x] = accH[c & 1][t][e] + ek[t][0] * cgv[e]; PIN(gv[x]); uv[x] = accH[c & 1][2 + t][e] + ek[t][0] * cuv[e]; PIN(uv[x]); }
    if (ph == 1) { wv[x] = gv[x] * (ek[t][1] + ek[t][2] * (gv[x] * gv[x])); PIN(wv[x]); }
    if (ph == 2) { wv[x] = __builtin_amdgcn_exp2f(wv[x]); PIN(wv[x]); uv[x] *= gv[x]; PIN(uv[x]); }
    if (ph == 3) { wv[x] = __builtin_amdgcn_rcpf(wv[x] * ek[t][3] + ek[t][3]); PIN(wv[x]); }
    if (ph == 4) {
      uv[x] *= wv[x]; PIN(uv[x]);
      if (x == 7) {
        u32x4 o = {pack2(uv[0], uv[1]), pack2(uv[2], uv[3]), pack2(uv[4], uv[5]), pack2(uv[6], uv[7])};
        __builtin_amdgcn_raw_buffer_store_b128(o, rws, hbase + c * 2048, 0, AUX_SC1);
      }
    }
  };
  const int wm = w >> 1, wn = w & 1;
  LAS char *aBase0, *aBase1, *yBase;
  bf16x8 xa0[4], xa1[4], yb0[5], yb1[5];
  auto rdg = [&](int g, int m) { wb[g & 3][m] = fragB(10 + g / 10, (g % 10) * 2 + m); };
  bar();
#pragma unroll
  for (int g = 0; g < 3; ++g) { rdg(g, 0); rdg(g, 1); }
  sfor<0, 120>([&](auto gi) __attribute__((always_inline)) {
    constexpr int g = decltype(gi)::v, c = g / 20, s = g % 20;
    if (g == 110) {
      const int l2 = fresh_lane(), r2 = l2 & 15, oK0 = r2 * 128 + (((l2 >> 4) ^ (r2 >> 1)) & 7) * 16;
      aBase0 = L + ARING + wm * 8192 + oK0; aBase1 = L + ARING + wm * 8192 + (oK0 ^ 64); yBase = L + BRING + wn * 5120 + l2 * 16;
    }
    if (g % 10 == 8) {
      if (g == 38) wait_vm<0>();
      bar();
    }
#pragma unroll
    for (int k = 0; k < 4; ++k) {
      accH[c & 1][k] = mfma(wb[g & 3][k >> 1], xr[s][k & 1], s == 0 ? z4 : accH[c & 1][k]);
      bool rd = true;
      if (g >= 118) {
        const int n = (g - 118) * 4 + k;
        if (n < 4) xa0[n] = ldsr(aBase0 + 2 * 16384 + n * 2048); else yb0[n - 4] = ldsr(yBase + 2 * 20480 + (n - 4) * 1024);
      } else if (g % 10 == 8) rdg(g + 2 + (k >> 1), k & 1);
      else if (g % 10 != 7 && k < 2) rdg(g + 3, k);
      else rd = false;
      if (!rd) {
        const int n = s % 10 == 7 ? 2 * s + k : 2 * s + k - 2;
        if (c == 0) {
          if (n < 25) {
            const int gg = n / 5, ph = n % 5;
            if (ph < 4) {
              const int t = ph >> 1, e = (ph & 1) * 2;
              unsigned pk = pack2(accP[gg][t][e] * st[t][e] + st[2 + t][e] * cs[gg], accP[gg][t][e + 1] * st[t][e + 1] + st[2 + t][e + 1] * cs[gg]);
              PIN(pk);
              abo[ph] = pk;
            } else __builtin_amdgcn_raw_buffer_store_b128(abo, rws, abBase + gg * 1024, 0, AUX_SC1);
          }
          if (n >= 10 && n < 26 && !(n & 1)) {
            const int m = (n - 10) >> 1;
            xr[14 + (m >> 1)][m & 1] = ldsr(((m & 2) ? xB1 : xB0) + ((7 + (m >> 2)) & 3) * 16384 + (m & 1) * 2048);
          }
          if (n == 34) ek[0] = *(LAS f32x4*)(L + T_EK + (w * 32 + r) * 16);
          if (n == 35) ek[1] = *(LAS f32x4*)(L + T_EK + (w * 32 + 16 + r) * 16);
        } else if (c < 5) h_op(c - 1, n);
        else if (n < 36) { h_op(4, n); if (n >= 32) h_op(4, n + 4); }
        if (n == (c == 0 ? 38 : 36)) h_tab(c);
      }
      SCHED0();
    }
  });

  f32x4 accY[5][4];
  auto y_stage = [&](int i, int h0) {
    LAS char* a0 = aBase0 + ((i + 2) & 3) * 16384; LAS char* a1 = aBase1 + ((i + 2) & 3) * 16384;
    LAS char* b = yBase + ((i + 2) & 3) * 20480;
    PIN(a0); PIN(a1); PIN(b);
    bar();
    int n = h0;
#pragma unroll
    for (int k = 0; k < 20; ++k) {
      accY[k >> 2][k & 3] = mfma(yb1[k >> 2], xa1[k & 3], accY[k >> 2][k & 3]);
      if (k < 4) xa0[k] = ldsr(a0 + k * 2048);
      else if (k < 9) yb0[k - 4] = ldsr(b + (k - 4) * 1024);
      else if (k < 11) yb1[k - 9] = ldsr(b + 10240 + (k - 9) * 1024);
      else if (k == 12) yb1[2] = ldsr(b + 10240 + 2048);
      else if (k == 16) yb1[3] = ldsr(b + 10240 + 3072);
      else if (n < 40) h_op(5, n++);
      SCHED0();
    }
#pragma unroll
    for (int k = 0; k < 20; ++k) {
      accY[k >> 2][k & 3] = mfma(yb0[k >> 2], xa0[k & 3], accY[k >> 2][k & 3]);
      if (k == 0) yb1[4] = ldsr(b + 10240 + 4096);
      else if (k < 5) xa1[k - 1] = ldsr(a1 + (k - 1) * 2048);
      else if (n < 40) h_op(5, n++);
      SCHED0();
    }
  };
#pragma unroll
  for (int k = 0; k < 20; ++k) {
    if (k == 0) yb0[4] = ldsr(yBase + 2 * 20480 + 4096);
    accY[k >> 2][k & 3] = mfma(yb0[k >> 2], xa0[k & 3], z4);
    if (k < 4) xa1[k] = ldsr(aBase1 + 2 * 16384 + k * 2048);
    else if (k < 9) yb1[k - 4] = ldsr(yBase + 2 * 20480 + 10240 + (k - 4) * 1024);
    else h_op(5, k - 9);
    SCHED0();
  }
  y_stage(1, 11); y_stage(2, 33);
#pragma nounroll
  for (int i = 3; i < 16; ++i) {
    if (i == 5) wait_vm<0>();
    y_stage(i, 40);
  }

  lane = fresh_lane(); r = lane & 15; q = lane >> 4;
  const int tile = id.tt * 4 + id.et;
  f32x4 accD[5][2];
#pragma unroll
  for (int g = 0; g < 5; ++g) { accD[g][0] = z4; accD[g][1] = z4; }
  LAS char* const hB = L + ARING + wm * 1024 + wn * 8192 + lane * 16 + wn * 8;
  LAS char* const dB = fB0 + wn * 10240;
  bf16x8 hh[2][2], dw[2][5];
  auto d_rd = [&](LAS char* a, LAS char* a2, LAS char* b, int ks, int m) {
    if (m < 2) hh[ks][m] = cat(*(volatile LAS bf16x4*)((m ? a2 : a) + ks * 4096), *(volatile LAS bf16x4*)((m ? a2 : a) + ks * 4096 + 2048));
    else dw[ks][m - 2] = ldsr(b + ks * 5120 + (m - 2) * 1024);
  };
  auto d_mm = [&](int ks, int k) { accD[k >> 1][k & 1] = mfma(dw[ks][k >> 1], hh[ks][k & 1], accD[k >> 1][k & 1]); };
  const unsigned pbase = W_P + (unsigned)(((tile * 4 + w) * 4 + id.kq) * 10) * 1024u + lane * 16;
  auto p_store = [&](int u) {
    u32x4 o[5];
#pragma unroll
    for (int g = 0; g < 5; ++g) {
      const f32x4 a = accY[g][2 * u], b = accY[g][2 * u + 1];
      o[g] = u32x4{pack2(a[0], a[1]), pack2(a[2], a[3]), pack2(b[0], b[1]), pack2(b[2], b[3])};
    }
    if (w != id.kq) {
#pragma unroll
      for (int g = 0; g < 5; ++g) __builtin_amdgcn_raw_buffer_store_b128(o[g], rws, pbase + (u * 5 + g) * 1024, 0, AUX_SC1);
    } else {
#pragma unroll
      for (int g = 0; g < 5; ++g) *(LAS u32x4*)(L + T_BIG + (u * 5 + g) * 1024 + lane * 16) = o[g];
    }
  };
  auto d_second = [&](LAS char* a, LAS char* a2, LAS char* b) {
#pragma unroll
    for (int k = 0; k < 10; ++k) {
      d_mm(0, k);
      if (k < 2) d_rd(a, a2, b, 1, k); else if (k < 4) d_rd(a, a2, b, 1, k + 3);
      SCHED0();
    }
  };
  auto d_stage = [&](int i) {
    LAS char* a = hB + ((i + 2) & 3) * 16384; LAS char* a2 = a + (8 - 16 * wn); LAS char* b = dB + ((i + 2) & 3) * 20480;
    PIN(a); PIN(a2); PIN(b);
    bar();
#pragma unroll
    for (int k = 0; k < 10; ++k) {
      d_mm(1, k);
      if (k < 7) d_rd(a, a2, b, 0, k); else d_rd(a, a2, b, 1, k - 5);
      SCHED0();
    }
    d_second(a, a2, b);
  };
  {
    LAS char* a = hB + 2 * 16384; LAS char* a2 = a + (8 - 16 * wn); LAS char* b = dB + 2 * 20480;
    PIN(a); PIN(a2); PIN(b);
    bar();
#pragma unroll
    for (int k = 0; k < 20; ++k) {
      accY[k >> 2][k & 3] = mfma(yb1[k >> 2], xa1[k & 3], accY[k >> 2][k & 3]);
      if (k < 7) d_rd(a, a2, b, 0, k); else if (k < 10) d_rd(a, a2, b, 1, k - 5);
      SCHED0();
    }
    d_second(a, a2, b);
  }
  p_store(0);
  p_store(1);
#pragma nounroll
  for (int i = 1; i < 12; ++i) {
    if (i == DRAIN_P - 38) wait_vm<0>();
    d_stage(i);
  }
#pragma unroll
  for (int k = 0; k < 10; ++k) {
    d_mm(1, k < 5 ? 2 * k + 1 : 2 * (9 - k));
    if (k >= 5) *(LAS f32x4*)(fB0 + ((w ^ 1) * 5 + k - 5) * 1024) = accD[k - 5][1];
    SCHED0();
  }
  LAS char* dp = fB0 + w * 5120;
  *(LAS f32x4*)(L + ARING + w * 2048 + 1024 + lane * 16) = accD[4][0];
  *(LAS f32x4*)(L + ARING + w * 2048 + lane * 16) = accD[3][0];
  LAS char* xp = L + X_TILE + (wm * 32 + 8 * (r >> 2) + 4 * wn + (r & 3)) * 160 + q * 8;
  PIN(xp); PIN(dp);
  bar();
  f32x4 dk[3], ys[3];
#pragma unroll
  for (int g = 0; g < 3; ++g) dk[g] = *(LAS f32x4*)(dp + g * 1024);
#pragma unroll
  for (int g = 0; g < 3; ++g) ys[g] = *(LAS f32x4*)(dp + 61440 + g * 1024);
  const f32x4 own[3] = {accD[0][0], accD[1][0], accD[2][0]};
  finish<3>(dk, own, ys, xp);
}

__device__ __forceinline__ void aux_tail(const Params& p, LAS char* L, int t, int lane) {
  const int po = (t >> 1) * 5120 + lane * 16 + (t & 1) * 8;
  u32x2 y[4][5];
#pragma unroll
  for (int g = 0; g < 5; ++g) {
    y[0][g] = *(volatile LAS u32x2*)(L + T_BIG + po + g * 1024);
    y[1][g] = *(volatile LAS u32x2*)(L + BRING + 2 * 20480 + po + g * 1024);
    y[2][g] = *(volatile LAS u32x2*)(L + BRING + 2 * 20480 + 10240 + po + g * 1024);
    y[3][g] = *(volatile LAS u32x2*)(L + ARING + 2 * 16384 + po + g * 1024);
  }
  SCHED0();
  LAS char* dp = L + ARING + t * 2048 + lane * 16;
  f32x4 ys[2];
#pragma unroll
  for (int g = 0; g < 5; ++g) {
    f32x4 v[4];
#pragma unroll
    for (int k = 0; k < 4; ++k) v[k] = f32x4{bflo(y[k][g][0]), bfhi(y[k][g][0]), bflo(y[k][g][1]), bfhi(y[k][g][1])};
    const f32x4 sum = (v[0] + v[1]) + (v[2] + v[3]);
    if (g < 3) *(LAS f32x4*)(L + BRING + 61440 + (t * 5 + g) * 1024 + lane * 16) = sum;
    else ys[g - 3] = sum;
  }
  const int r = lane & 15;
  LAS char* xp = L + X_TILE + ((t >> 1) * 32 + 8 * (r >> 2) + 4 * (t & 1) + (r & 3)) * 160 + (lane >> 4) * 8 + 96;
  LAS char* dq = L + BRING + t * 5120 + 3072 + lane * 16;
  PIN(xp); PIN(dp); PIN(dq); PIN(ys[0]); PIN(ys[1]);
  wait_vm<0>();
  bar();
  if (t == 3) { STAMP(50); }
  f32x4 dk[2], own[2];
#pragma unroll
  for (int g = 0; g < 2; ++g) { dk[g] = *(LAS f32x4*)(dq + g * 1024); own[g] = *(LAS f32x4*)(dp + g * 1024); }
  finish<2>(dk, own, ys, xp);
}

__device__ __forceinline__ void st_flag(unsigned char* ws, unsigned off, u64 v, int lane) {
  if (lane == 0) __hip_atomic_store((GAS u64*)(ws + off), v, __ATOMIC_RELAXED, __HIP_MEMORY_SCOPE_AGENT);
}
__device__ __forceinline__ void wait_flag(const unsigned char* ws, unsigned off, u64 tag, u64 seen) {
  if (!__all(seen == tag)) {
#pragma nounroll
    for (int spin = 0; spin < (1 << 16); ++spin)
      if (__all(ld_flag(ws, off) == tag)) break;
  }
  __builtin_amdgcn_fence(__ATOMIC_ACQUIRE, "wavefront");
}
template <int N, class Fn>
__device__ __forceinline__ void wait_tags(u32x4 (&g)[N], unsigned tlo, unsigned thi, Fn&& reload) {
  auto good = [&]() {
    unsigned bad = 0;
#pragma unroll
    for (int k = 0; k < N; ++k) bad |= (g[k][1] ^ tlo) | (g[k][3] ^ thi);
    return __all(bad == 0);
  };
  if (!good()) {
#pragma nounroll
    for (int spin = 0; spin < (1 << 16); ++spin) {
      reload();
      if (good()) break;
    }
  }
}
struct First { cptr base; unsigned v0, v1, v2, v3; int kc; };
#define SPIN(x) asm volatile("" : "+s"(x))
__device__ __forceinline__ void p1_tile(First& f, unsigned lb, int ld, int j, bool barrier) {
  cptr a = f.base + f.kc * 128, a2 = a + 32 * 1280;
  unsigned l = ld < 2 ? lb + ARING + (j & 3) * 16384 + ld * 8192 : lb + BRING + ((j & 3) ? (j & 3) * 20480 : 10240);
  SPIN(a); SPIN(a2); SPIN(l);
  if (barrier) bar();
  dma4(a, f.v0, f.v1, f.v2, f.v3, l);
  dma4(a2, f.v0, f.v1, f.v2, f.v3, l + 4096);
  if (ld == 2) dma2(a + 64 * 1280, f.v0, f.v1, l + 8192);
  f.kc = f.kc == 9 ? 0 : f.kc + 1;
}
__device__ __forceinline__ void loader(const Params& p, LAS char* L, unsigned lb, const Ids& id, int ld, int lane, First& f) {
  const int b = blockIdx.x;
  const unsigned vl = lane * 16;
  const unsigned c0 = ((lane & 7) ^ (lane >> 4)) * 16, c1 = c0 ^ 64;
  const int rl = 8 * (lane >> 5) + ((lane >> 3) & 3);
  const int rot = ROT;
#pragma nounroll
  for (int j = 2; j < 10; ++j) {
    if (j < 4) { if (ld == 2) wait_vm<10>(); else wait_vm<8>(); }
    if (j > 4) { if (ld == 2) wait_vm<20>(); else wait_vm<16>(); }
    p1_tile(f, lb, ld, j, j != 4);
  }
  const unsigned vw0 = rl * 4096 + c0, vw1 = (rl + 16) * 4096 + c1 - 1024, vw2 = (rl + 4) * 4096 + c0 - 2048, vw3 = (rl + 20) * 4096 + c1 - 3072;
  const unsigned u0 = ld ? vl : vw0, u1 = ld ? vl : vw1, u2 = ld ? vl : vw2, u3 = ld ? vl : vw3;
  const cptr bw = (cptr)(id.kq < 2 ? p.wsr : p.wsi) + (id.tt * 128 * 4096 + (id.kq & 1) * 2048);
  const int g0 = ld ? 2 * ld - 2 : 4;
  const unsigned l0 = lb + BRING + g0 * 4096, l1 = ld ? l0 + 4096 : lb + ARING, ls1 = ld ? 20480 : 16384, l2 = lb + ARING + (ld + 1) * 4096;
  auto yk = [&](int y) { return (y & ~3) | ((y + id.et) & 3); };
  {
    const cptr bg = p.ws + W_GU + (unsigned)id.cb * 245760u;
    const int ka[2] = {((g0 + rot) % 10) * 4096, ((g0 + rot + 5) % 10) * 4096}, kb[2] = {((g0 + 1 + rot) % 10) * 4096, ((g0 + rot + 6) % 10) * 4096};
    if (ld == 2) wait_vm<20>(); else wait_vm<16>();
    bar();
#pragma nounroll
    for (int j = 10; j < 22; ++j) {
      const cptr so = bg + ((j - 10) >> 1) * 40960;
      const int n = (j - 16) * 3 + ld;
      cptr a0 = so + ((j & 1) ? ka[1] : ka[0]), a1 = so + ((j & 1) ? kb[1] : kb[0]), a2 = bw + (yk(n >> 2) * 128 + (n & 3) * 32 * 4096);
      unsigned la = l0 + (j & 3) * 20480, lw = lb + ARING + (((n >> 2) + 2) & 3) * 16384 + (n & 3) * 4096;
      SPIN(a0); SPIN(a1); SPIN(a2); SPIN(la); SPIN(lw);
      if (j != 11) {
        if (j == 10) wait_vm<8>();
        else if (j < 18) { if (ld == 0) wait_vm<8>(); else wait_vm<16>(); }
        else { if (ld == 0) wait_vm<16>(); else wait_vm<24>(); }
        bar();
      }
      dma4c(a0, vl, la);
      if (ld) dma4c(a1, vl, la + 4096);
      if (n >= 0 && n < 16) dma4(a2, vw0, vw1, vw2, vw3, lw);
    }
  }
  const int c80 = id.et * 2 + (id.kq & 1);
  const cptr bh = p.ws + W_H + (unsigned)(id.tt * 2 + (id.kq >> 1)) * 196608u;
  const cptr bab = p.ws + W_ABT + ((unsigned)(((id.kq >> 1) * 4 + id.et) * 64 + (id.kq & 1) * 32) * 10240u + g0 * 4096);
  const cptr bwd = p.ws + W_WDT + (unsigned)c80 * 245760u;
  const cptr y1 = ld ? bab + 4096 : bw, y2 = bw + (ld + 1) * 32 * 4096;
  const int ym1 = ld ? 20480 : 128;
  const cptr d0 = bwd + g0 * 4096, d1 = ld ? d0 + 4096 : bh, d2 = bh + (ld + 1) * 4096;
#pragma nounroll
  for (int i = 19; i < 35; ++i) {
    const int k = yk(i - 19), sl = (i + 3) & 3;
    cptr a0 = bab + k * 20480, a1 = y1 + k * ym1, a2 = y2 + k * 128;
    unsigned la = l0 + sl * 20480, lc = l1 + sl * ls1, ld2 = l2 + sl * 16384;
    SPIN(a0); SPIN(a1); SPIN(a2); SPIN(la); SPIN(lc); SPIN(ld2);
    if (i >= 25) wait_vm<24>(); else if (i == 24) wait_vm<16>(); else if (ld == 0) wait_vm<8>(); else wait_vm<16>();
    bar();
    dma4c(a0, vl, la);
    if (i >= 23 || ld) dma4s(a1, u0, u1, u2, u3, lc);
    if (i >= 23) dma4(a2, vw0, vw1, vw2, vw3, ld2);
  }
#pragma nounroll
  for (int i = 35; i < 47; ++i) {
    const int sl = (i + 3) & 3;
    cptr a0 = d0 + (i - 35) * 20480, a1 = d1 + (i - 35) * ls1, a2 = d2 + (i - 35) * 16384;
    unsigned la = l0 + sl * 20480, lc = l1 + sl * ls1, ld2 = l2 + sl * 16384;
    SPIN(a0); SPIN(a1); SPIN(a2); SPIN(la); SPIN(lc); SPIN(ld2);
    wait_vm<24>();
    bar();
    dma4c(a0, vl, la);
    dma4c(a1, vl, lc);
    dma4c(a2, vl, ld2);
  }
  cptr bp = p.ws + W_P + ((unsigned)(((id.tt * 4 + id.et) * 4 + id.kq) * 4 * 10) * 1024u + ((id.kq + 1 + ld) & 3) * 10240);
  unsigned l = lb + (ld == 0 ? ARING + 2 * 16384 : BRING + 2 * 20480 + (ld - 1) * 10240);
  SPIN(bp); SPIN(l);
  wait_vm<24>(); bar();
  dma4c(bp, vl, l); dma4c(bp + 4096, vl, l + 4096); dma2c(bp + 8192, vl, l + 8192);
}

__device__ __forceinline__ void helper(const Params& p, LAS char* L, unsigned lb, const Ids& id, int lane, u64 pc) {
  const int b = blockIdx.x;
  const __amdgpu_buffer_rsrc_t rws = __builtin_amdgcn_make_buffer_rsrc((void*)p.ws, 0, (int)W_END, 0x00020000);
  STAMP(60);
  __builtin_amdgcn_s_setprio(3);
  const u64 prev = ld_flag(p.ws, W_EPOCH);
  const int r8 = lane >> 3;
  u32x4 dv[10];
  const GAS char* ds = (const GAS char*)(b < 128 ? p.wdr : p.wdi) + (((b & 127) * 5 + (r8 < 5 ? r8 : 4)) * 1280 + (lane & 7) * 16);
#pragma unroll
  for (int i = 0; i < 10; ++i) dv[i] = *(const GAS u32x4*)(ds + i * 128);
  auto code_touch = [&]() {
    const unsigned off = (((b >> 3) % ((CODE_BYTES + 8191) / 8192)) * 64 + lane) * 128;
    touch((cptr)(pc & ~255ull), off < CODE_BYTES ? off : 0, lb + T_PAD);
  };
  const u64 tag = next_tag(prev);
  const unsigned tlo = (unsigned)tag, thi = (unsigned)(tag >> 32);
  {
    float t4[4] = {0.f, 0.f, 0.f, 0.f};
#pragma unroll
    for (int i = 0; i < 10; ++i)
#pragma unroll
      for (int e = 0; e < 4; ++e) t4[e] = dot_ones(dv[i][e], t4[e]);
    const unsigned sm = __builtin_bit_cast(unsigned, row8_sum((t4[0] + t4[1]) + (t4[2] + t4[3])));
    const u32x4 o = {sm, tlo, sm, thi};
    if ((lane & 7) == 0 && r8 < 5) __builtin_amdgcn_raw_buffer_store_b128(o, rws, W_CSD + (b * 5 + r8) * 16, 0, AUX_SC1);
  }
  const int pcl = (id.cb * 3) >> 1, tile = id.tt * 4 + id.et, c80 = id.et * 2 + (id.kq & 1);
  const int wk = b / 5, wc = b % 5;
  u64 f0 = 0, f1 = 0, f2 = 0;
  unsigned fo0 = 0, fo1 = 0, fo2 = 0;
  u32x4 gr[2], cq[10];
  const int go = lane < 40 ? lane : 0;
  auto ld_gr = [&]() {
    gr[0] = __builtin_amdgcn_raw_buffer_load_b128(rws, W_CSD + (id.cb * 80 + go) * 16, 0, AUX_SC1);
    gr[1] = __builtin_amdgcn_raw_buffer_load_b128(rws, W_CSD + (id.cb * 80 + 40 + go) * 16, 0, AUX_SC1);
  };
  auto ld_cq = [&]() {
#pragma unroll
    for (int k = 0; k < 10; ++k) cq[k] = __builtin_amdgcn_raw_buffer_load_b128(rws, W_CSP + ((k * 24 + pcl) * 32 + lane) * 16, 0, AUX_SC1);
  };
  const cptr wsb = (cptr)(id.kq < 2 ? p.wsr : p.wsi) + (id.tt * 128 * 4096 + (id.kq & 1) * 2048 + id.et * 128);
  sfor<0, 48>([&](auto ic) __attribute__((always_inline)) {
    constexpr int i = decltype(ic)::v;
    if constexpr (i == 8) wait_flag(p.ws, fo0, tag, f0);
    if constexpr (i == 35) { wait_flag(p.ws, fo1, tag, f1); wait_flag(p.ws, fo2, tag, f2); }
    if constexpr (i == 19 || i == 35 || i == 47) wait_flag(p.ws, fo0, tag, f0);
    bar();
    STAMP(i);
    if constexpr (i == 0) {
      if (b < 240 && lane < 32) {
        LAS float* cp = (LAS float*)(L + T_CP + lane * 4);
        const float u0 = (cp[0] + cp[64]) + (cp[128] + cp[192]), u1 = (cp[32] + cp[96]) + (cp[160] + cp[224]);
        const u32x4 o = {__builtin_bit_cast(unsigned, u0), tlo, __builtin_bit_cast(unsigned, u1), thi};
        __builtin_amdgcn_raw_buffer_store_b128(o, rws, W_CSP + (b * 32 + lane) * 16, 0, AUX_SC1);
      }
    }
    if constexpr (i == DRAIN_G) st_flag(p.ws, W_FG + b * 8, tag, lane);
    if constexpr (i == 5) ld_gr();
    if constexpr (i == 6) {
      const int l20 = lane < 20 ? lane : 0;
      fo0 = W_FG + ((l20 >> 1) * 24 + pcl + (l20 & 1)) * 8;
      f0 = ld_flag(p.ws, fo0);
    }
    if constexpr (i == 8) {
      wait_tags<2>(gr, tlo, thi, ld_gr);
      *(LAS unsigned*)(L + T_CS + go * 4) = gr[0][0];
      *(LAS unsigned*)(L + T_CS + (40 + go) * 4) = gr[1][0];
    }
    if constexpr (i == 8) ld_cq();
    if constexpr (i == 12) {
      if (b < 240) {
        const cptr sn = (cptr)p.wdn + (wk * 32 * 1280 + wc * 256);
        const unsigned vn = (lane >> 4) * 1280 + (lane & 15) * 16;
        SELNT(NT_W, dma4)(sn, vn, vn + 4096, vn + 2 * 4096, vn + 3 * 4096, lb + T_BIG);
        SELNT(NT_W, dma4)(sn + 16 * 1280, vn, vn + 4096, vn + 2 * 4096, vn + 3 * 4096, lb + T_BIG + 4096);
      }
    }
    if constexpr (i == 10) wait_tags<10>(cq, tlo, thi, ld_cq);
    if constexpr (i == 11) {
#pragma unroll
      for (int m = 0; m < 2; ++m)
#pragma unroll
        for (int h = 0; h < 2; ++h) {
          const unsigned col = (lane >> 5) * 64 + h * 32 + (lane & 31) - (id.cb & 1) * 32;
          const float v = ((asf(cq[m][2 * h]) + asf(cq[2 + m][2 * h])) + (asf(cq[4 + m][2 * h]) + asf(cq[6 + m][2 * h]))) + asf(cq[8 + m][2 * h]);
          *(LAS float*)(col < 96 ? L + T_CG + (m * 96 + col) * 4 : L + T_PAD) = v;
        }
    }
    if constexpr (i >= 19 && i < 23) {
      if (b < 240) {
        LAS char* a = L + T_BIG + (4 * (lane >> 4) + ((lane & 15) >> 2)) * 256 + 8 * (lane & 3);
#pragma unroll
        for (int c = (i - 19) * 2; c < (i - 19) * 2 + 2; ++c) {
          const bf16x8 f = cat(ldstr(a + c * 32), ldstr(a + 4096 + c * 32));
          const int eg = wc * 8 + c, cc = eg / 5, g5 = eg - cc * 5;
          __builtin_amdgcn_raw_buffer_store_b128(__builtin_bit_cast(u32x4, f), rws, lane * 16 + W_WDT + ((cc * 48 + wk) * 5 + g5) * 1024, 0, AUX_SC1);
        }
      }
    }
    if constexpr (i == 14) st_flag(p.ws, W_FB + (id.rb * 16 + id.cb) * 8, tag, lane);
    if constexpr (i == 16) code_touch();
    if constexpr (i == 26) { wait_vm<0>(); st_flag(p.ws, W_FA + b * 8, tag, lane); }
    if constexpr (i == 17) {
      fo0 = W_FB + ((8 * (id.kq & 1) + (lane & 7)) * 16 + 8 * (id.kq >> 1) + 2 * id.et + ((lane >> 3) & 1)) * 8;
      f0 = ld_flag(p.ws, fo0);
    }
    if constexpr (i == 31) {
      fo1 = W_FA + ((lane < 48 ? lane : 0) * 5 + (c80 * 5) / 8) * 8;
      fo2 = W_FA + ((lane < 48 ? lane : 0) * 5 + (c80 * 5 + 4) / 8) * 8;
      f1 = ld_flag(p.ws, fo1); f2 = ld_flag(p.ws, fo2);
    }
    if constexpr (i == 27) st_flag(p.ws, W_FH + (id.rb * 16 + id.cb) * 8, tag, lane);
    if constexpr (i == 30) code_touch();
    if constexpr (i == 32) { fo0 = W_FH + (id.tt * 16 + (lane & 15)) * 8; f0 = ld_flag(p.ws, fo0); }
    if constexpr (i == 35) {
      gr[0] = __builtin_amdgcn_raw_buffer_load_b128(rws, W_FA + lane * 32, 0, AUX_SC1);
      gr[1] = __builtin_amdgcn_raw_buffer_load_b128(rws, W_FA + lane * 32 + 16, 0, AUX_SC1);
    }
    if constexpr (i == 37) {
      const bool all = __all(((gr[0][0] ^ tlo) | (gr[0][1] ^ thi) | (gr[0][2] ^ tlo) | (gr[0][3] ^ thi) | (gr[1][0] ^ tlo) | (gr[1][1] ^ thi) | (gr[1][2] ^ tlo) | (gr[1][3] ^ thi)) == 0);
      if (b == 0) {
        if (!all) {
#pragma nounroll
          for (int k = 0; k < 4; ++k) wait_flag(p.ws, W_FA + (k * 64 + lane) * 8, tag, ~tag);
        }
        st_flag(p.ws, W_EPOCH, tag, lane);
      }
    }
    if constexpr (i == DRAIN_P) st_flag(p.ws, W_FP + (tile * 4 + id.kq) * 8, tag, lane);
    if constexpr (i == 45) { fo0 = W_FP + (tile * 4 + (lane & 3)) * 8; f0 = ld_flag(p.ws, fo0); }
    if constexpr (WT0 >= 0 && i >= WT0 && i < WT0 + 8 * WTS && (i - WT0) % WTS == 0) {
      constexpr int n = (i - WT0) / WTS;
      touch(wsb + ((n & 1) * 64 * 4096 + (n >> 1) * 512), lane * 4096, lb + T_PAD);
    }
  });
  __builtin_amdgcn_s_waitcnt(0x0F70);
}

__global__ __launch_bounds__(NTHR, 2) void fused_kernel(Params p) {
  extern __shared__ __attribute__((aligned(16))) char smem[];
  u64 pc;
  asm volatile("s_getpc_b64 %0" : "=s"(pc));
  LAS char* L = (LAS char*)smem;
  const unsigned lb = (unsigned)(size_t)L;
  const int lane = threadIdx.x & 63, w = __builtin_amdgcn_readfirstlane(threadIdx.x >> 6);
  const int b = blockIdx.x, g = b & 7, rr = b >> 3;
  if (w == 7) {
    const unsigned off = ((rr % ((CODE_BYTES + 8191) / 8192)) * 64 + lane) * 128;
    touch((cptr)(pc & ~255ull), off < CODE_BYTES ? off : 0, lb + T_PAD);
  }
  Ids id;
  id.rb = (g >> 1) * 4 + (rr >> 3);
  id.cb = (g & 1) * 8 + (rr & 7);
  id.tt = (g & 1) * 8 + (rr & 7);
  id.et = rr >> 3;
  id.kq = g >> 1;
  First f;
  {
    const int pc48 = b % 48, pr = b / 48;
    const bool isx = w == 4 || w == 5;
    const unsigned row = isx ? 8 * (lane >> 5) + ((lane >> 3) & 3) : lane >> 3, stride = w < 4 ? 3072 : 1280;
    const unsigned c0 = ((lane & 7) ^ (w < 4 ? 0 : lane >> 4)) * 16, c1 = c0 ^ (w < 4 ? 0 : 64);
    f.v0 = row * stride + c0;
    f.v1 = row * stride + c1 + ((isx ? 16 : 8) * stride - 1024);
    f.v2 = f.v0 + ((isx ? 4 : 16) * stride - 2048);
    f.v3 = f.v1 + ((isx ? 4 : 16) * stride - 2048);
    f.base = w < 4 ? (cptr)(pc48 < 24 ? p.wg : p.wu) + ((pr * 128 + w * 32) * 3072 + (pc48 % 24) * 128)
                   : isx ? (cptr)p.x + (id.rb * 128 + (w - 4) * 64) * 1280 : (cptr)(id.cb < 8 ? p.wdr : p.wdi) + (id.cb & 7) * 80 * 1280;
    f.kc = ROT % 10;
    if (w < 4) {
      if (b < 240) SELNT(NT_G, dma4)(f.base, f.v0, f.v1, f.v2, f.v3, lb + BRING + (w ? w * 20480 + 10240 : 0));
    } else if (w < 7) {
      p1_tile(f, lb, w - 4, 0, false);
      p1_tile(f, lb, w - 4, 1, false);
    }
  }
  if (w >= 4) {
    const int ld = w - 4;
    if (ld < 3) loader(p, L, lb, id, ld, lane, f);
    else helper(p, L, lb, id, lane, pc);
    const int n0 = ld * 64 + lane, n1 = n0 + 256, n2 = ld < 2 ? n0 + 512 : n0;
    unsigned x0 = (n0 / 10) * 1120 + n0 * 16, x1 = (n1 / 10) * 1120 + n1 * 16, x2 = (n2 / 10) * 1120 + n2 * 16;
    PIN(x0); PIN(x1); PIN(x2);
    const cptr so = (cptr)p.x + ((id.tt * 128 + (id.kq >> 1) * 64) * 1280 + (id.et * 2 + (id.kq & 1)) * 160);
    wait_vm<22>();
    bar();
    if (w == 7) { STAMP(48); }
    dma1(so, x0, lb + X_TILE + ld * 1024); dma1(so, x1, lb + X_TILE + (ld + 4) * 1024); dma1(so, x2, lb + X_TILE + (ld < 2 ? ld + 8 : ld) * 1024);
    wait_vm<3>();
    bar();
    if (w == 7) { STAMP(49); }
    aux_tail(p, L, ld, lane);
  } else compute(p, L, id, w);
  const int tid = w * 64 + fresh_lane();
  const __amdgpu_buffer_rsrc_t ro = __builtin_amdgcn_make_buffer_rsrc((char*)p.out + ((id.tt * 128 + (id.kq >> 1) * 64) * 1280 + (id.et * 160 + (id.kq & 1) * 80) * 2), 0, 63 * 1280 + 160, 0x00020000);
  unsigned o0 = (tid / 10) * 1120 + tid * 16, o1 = ((tid + 512) / 10) * 1120 + (tid + 512) * 16;
  PIN(o0); PIN(o1);
  bar();
  const u32x4 v0 = *(LAS u32x4*)(L + X_TILE + tid * 16);
  if (w < 2) {
    const u32x4 v1 = *(LAS u32x4*)(L + X_TILE + 8192 + tid * 16);
    __builtin_amdgcn_raw_buffer_store_b128(v1, ro, o1, 0, AUX_SC1);
  }
  __builtin_amdgcn_raw_buffer_store_b128(v0, ro, o0, 0, AUX_SC1);
}
}

extern "C" void kernel_launch(void* const* d_in, const int* in_sizes, int n_in, void* d_out, int out_size, void* d_ws, size_t ws_size,
                              hipStream_t stream) {
  Params p;
  p.x = (const u16*)d_in[0];
  p.wsr = (const u16*)d_in[1];
  p.wsi = (const u16*)d_in[2];
  p.wdr = (const u16*)d_in[3];
  p.wdi = (const u16*)d_in[4];
  p.wg = (const u16*)d_in[6];
  p.wu = (const u16*)d_in[7];
  p.wdn = (const u16*)d_in[8];
  p.out = (u16*)d_out;
  p.ws = (unsigned char*)d_ws;
  (void)hipFuncSetAttribute((const void*)fused_kernel, hipFuncAttributeMaxDynamicSharedMemorySize, LDS_BYTES);
  fused_kernel<<<256, NTHR, LDS_BYTES, stream>>>(p);
}
```

```cpp
#include <hip/hip_runtime.h>
#include <hip/hip_bf16.h>

typedef unsigned short u16;
typedef unsigned long long u64;
typedef short bf16x8 __attribute__((ext_vector_type(8)));
typedef short bf16x4 __attribute__((ext_vector_type(4)));
typedef float f32x4 __attribute__((ext_vector_type(4)));
typedef float f32x2 __attribute__((ext_vector_type(2)));
typedef unsigned u32x4 __attribute__((ext_vector_type(4)));
typedef unsigned u32x2 __attribute__((ext_vector_type(2)));
typedef int i32x4 __attribute__((ext_vector_type(4)));
typedef __bf16 bf2v __attribute__((ext_vector_type(2)));
#define LAS __attribute__((address_space(3)))
#define GAS __attribute__((address_space(1)))

namespace {
constexpr int S = 2048, D = 640, F = 1536;
constexpr int NTHR = 512;
constexpr int ARING = 0;
constexpr int BRING = 65536;
constexpr int TAB = 147456;
constexpr int T_ST = TAB;
constexpr int T_CS = TAB + 1024;
constexpr int T_CP = TAB + 1344;
constexpr int T_CG = TAB + 2368;
constexpr int T_PAD = TAB + 3328;
constexpr int T_EK = TAB + 12288;
constexpr int T_BIG = TAB + 4096;
constexpr int X_TILE = ARING + 3 * 16384;
constexpr int LDS_BYTES = 163840;
constexpr unsigned W_EPOCH = 0;
constexpr unsigned W_FA = 4096, W_FB = W_FA + 2048, W_FH = W_FB + 2048, W_FP = W_FH + 2048, W_FG = W_FP + 2048;
constexpr unsigned W_CSP = 139264;
constexpr unsigned W_ST = 81920;
constexpr unsigned W_CSD = W_ST + 32768;
constexpr unsigned W_ABT = 262144;
constexpr unsigned W_H = W_ABT + 2u * 4 * 64 * 10 * 1024;
constexpr unsigned W_WDT = W_H + 32u * 96 * 2 * 1024;
constexpr unsigned W_GU = W_WDT + 8u * 48 * 5 * 1024;
constexpr unsigned W_P = W_GU + 16u * 20 * 12 * 1024;
constexpr unsigned W_END = W_P + 256u * 4 * 10 * 1024;
constexpr unsigned W_STAMP = W_END;
constexpr int AUX_SC1 = 16;
#define DRAIN_G 3
#define PRO 2
#define DRAIN_P 41
#define CODE_BYTES 33536
#define WT0 -1
#define WTS 2
#define MAP 1
#define ROT (3 * (b >> 6) + ((b >> 3) & 7))
#define NT_W 0
#define NT_G 0
#define SELNT(c, f) ((c) ? f##nt : f)
#define GB 8

struct Params {
  const u16 *x, *wsr, *wsi, *wdr, *wdi, *wg, *wu, *wdn;
  u16* out;
  unsigned char* ws;
};

typedef const unsigned char* cptr;
#define DMA_BODY(POL) \
__device__ __forceinline__ void dma2##POL(cptr base, unsigned v0, unsigned v1, unsigned lds) { \
  asm volatile("s_mov_b32 m0, %3\n\ts_nop 0\n\tglobal_load_lds_dwordx4 %0, %2 " #POL "\n\tglobal_load_lds_dwordx4 %1, %2 offset:1024 " #POL \
               ::"v"(v0), "v"(v1), "s"(base), "s"(lds) : "memory"); \
} \
__device__ __forceinline__ void dma3##POL(cptr base, unsigned v0, unsigned v1, unsigned v2, unsigned lds) { \
  asm volatile("s_mov_b32 m0, %4\n\ts_nop 0\n\tglobal_load_lds_dwordx4 %0, %3 " #POL "\n\tglobal_load_lds_dwordx4 %1, %3 offset:1024 " #POL "\n\t" \
               "global_load_lds_dwordx4 %2, %3 offset:2048 " #POL ::"v"(v0), "v"(v1), "v"(v2), "s"(base), "s"(lds) : "memory"); \
} \
__device__ __forceinline__ void dma4##POL(cptr base, unsigned v0, unsigned v1, unsigned v2, unsigned v3, unsigned lds) { \
  asm volatile("s_mov_b32 m0, %5\n\ts_nop 0\n\tglobal_load_lds_dwordx4 %0, %4 " #POL "\n\tglobal_load_lds_dwordx4 %1, %4 offset:1024 " #POL "\n\t" \
               "global_load_lds_dwordx4 %2, %4 offset:2048 " #POL "\n\tglobal_load_lds_dwordx4 %3, %4 offset:3072 " #POL \
               ::"v"(v0), "v"(v1), "v"(v2), "v"(v3), "s"(base), "s"(lds) : "memory"); \
}
DMA_BODY()
DMA_BODY(nt)
DMA_BODY(sc1)
#define dma4s dma4sc1
__device__ __forceinline__ void dma1(cptr base, unsigned v, unsigned lds) {
  asm volatile("s_mov_b32 m0, %2\n\ts_nop 0\n\tglobal_load_lds_dwordx4 %0, %1" ::"v"(v), "s"(base), "s"(lds) : "memory");
}
__device__ __forceinline__ void dma4c(cptr base, unsigned v, unsigned lds) {
  asm volatile("s_mov_b32 m0, %2\n\ts_nop 0\n\tglobal_load_lds_dwordx4 %0, %1 sc1\n\tglobal_load_lds_dwordx4 %0, %1 offset:1024 sc1\n\t"
               "global_load_lds_dwordx4 %0, %1 offset:2048 sc1\n\tglobal_load_lds_dwordx4 %0, %1 offset:3072 sc1"
               ::"v"(v), "s"(base), "s"(lds) : "memory");
}
__device__ __forceinline__ void dma2c(cptr base, unsigned v, unsigned lds) {
  asm volatile("s_mov_b32 m0, %2\n\ts_nop 0\n\tglobal_load_lds_dwordx4 %0, %1 sc1\n\tglobal_load_lds_dwordx4 %0, %1 offset:1024 sc1"
               ::"v"(v), "s"(base), "s"(lds) : "memory");
}
__device__ __forceinline__ void touch(cptr base, unsigned voff, unsigned pad) {
  asm volatile("s_mov_b32 m0, %2\n\ts_nop 0\n\tglobal_load_lds_dword %0, %1" ::"v"(voff), "s"(base), "s"(pad) : "memory");
}
template <int N> __device__ __forceinline__ void wait_vm() { asm volatile("s_waitcnt vmcnt(%0)" ::"i"(N) : "memory"); }
#define SCHED0() __builtin_amdgcn_sched_barrier(0)
__device__ __forceinline__ void bar() {
  SCHED0();
  __builtin_amdgcn_s_waitcnt(0xC07F);
  asm volatile("s_barrier" ::: "memory");
  SCHED0();
}
__device__ __forceinline__ f32x4 mfma(bf16x8 a, bf16x8 b, f32x4 c) { return __builtin_amdgcn_mfma_f32_16x16x32_bf16(a, b, c, 0, 0, 0); }
__device__ __forceinline__ bf16x8 ldsr(LAS char* p) { return *(LAS bf16x8*)p; }
__device__ __forceinline__ bf16x4 ldstr(LAS char* p) { return __builtin_amdgcn_ds_read_tr16_b64_v4i16((LAS bf16x4*)p); }
__device__ __forceinline__ bf16x8 cat(bf16x4 a, bf16x4 b) { return __builtin_shufflevector(a, b, 0, 1, 2, 3, 4, 5, 6, 7); }
__device__ __forceinline__ unsigned pack2(float a, float b) {
  f32x2 v = {a, b};
  bf2v r = __builtin_convertvector(v, bf2v);
  return __builtin_bit_cast(unsigned, r);
}
__device__ __forceinline__ float asf(unsigned v) { return __builtin_bit_cast(float, v); }
__device__ __forceinline__ float bflo(unsigned v) { return __builtin_bit_cast(float, v << 16); }
__device__ __forceinline__ float bfhi(unsigned v) { return __builtin_bit_cast(float, v & 0xffff0000u); }
template <int CTRL> __device__ __forceinline__ float dpp_add(float v) {
  return v + __builtin_bit_cast(float, __builtin_amdgcn_update_dpp(0, __builtin_bit_cast(int, v), CTRL, 0xf, 0xf, true));
}
__device__ __forceinline__ float row8_sum(float v) {
  v = dpp_add<0xB1>(v); v = dpp_add<0x4E>(v); v = dpp_add<0x141>(v);
  return v;
}
__device__ __forceinline__ float dot_ones(unsigned v, float acc) {
  return __builtin_amdgcn_fdot2_f32_bf16(__builtin_bit_cast(bf2v, v), __builtin_bit_cast(bf2v, 0x3f803f80u), acc, false);
}
__device__ __forceinline__ u64 ld_flag(const unsigned char* ws, unsigned off) {
  return __hip_atomic_load((GAS u64*)(ws + off), __ATOMIC_RELAXED, __HIP_MEMORY_SCOPE_AGENT);
}
__device__ __forceinline__ u64 next_tag(u64 prev) { return prev * 6364136223846793005ull + 1442695040888963407ull; }

#define STAMP(i) do { } while (0)

struct Ids {
  int rb, cb, tt, et, kq;
};

template <int I> struct IC { static constexpr int v = I; };
template <int I, int N, class Fn> __device__ __forceinline__ void sfor(Fn&& f) {
  if constexpr (I < N) { f(IC<I>{}); sfor<I + 1, N>(f); }
}
__device__ __forceinline__ int fresh_lane() {
  int l;
  asm volatile("v_mbcnt_lo_u32_b32 %0, -1, 0\n\tv_mbcnt_hi_u32_b32 %0, -1, %0" : "=v"(l));
  return l;
}
__device__ __forceinline__ float sel(float a, float b, u64 m) {
  float o;
  asm("v_cndmask_b32_e64 %0, %1, %2, %3" : "=v"(o) : "v"(a), "v"(b), "s"(m));
  return o;
}
__device__ __forceinline__ float pick4(f32x4 v) {
  float a = v[0], b = v[1], c = v[2], d = v[3];
  asm volatile("s_nop 7" : "+v"(a), "+v"(b), "+v"(c), "+v"(d));
  return sel(sel(a, b, 0xAAAAAAAAAAAAAAAAull), sel(c, d, 0xAAAAAAAAAAAAAAAAull), 0xCCCCCCCCCCCCCCCCull);
}
#define PIN(x) asm volatile("" : "+v"(x))
template <int N>
__device__ __forceinline__ void finish(f32x4 (&dk)[N], const f32x4 (&d1)[N], f32x4 (&ys)[N], LAS char* xp) {
  f32x4 ex[N];
  u32x2 xv[N];
#pragma unroll
  for (int g = 0; g < N; ++g) xv[g] = *(volatile LAS u32x2*)(xp + g * 32);
  SCHED0();
#pragma unroll
  for (int g = 0; g < N; ++g) { dk[g] += d1[g]; ex[g] = dk[g] * -1.4426950409f; }
  SCHED0();
#pragma unroll
  for (int g = 0; g < N; ++g) {
#pragma unroll
    for (int e = 0; e < 4; ++e) ex[g][e] = __builtin_amdgcn_exp2f(ex[g][e]);
    dk[g] *= ys[g];
  }
  SCHED0();
#pragma unroll
  for (int g = 0; g < N; ++g) ex[g] += 1.f;
  SCHED0();
#pragma unroll
  for (int g = 0; g < N; ++g) {
#pragma unroll
    for (int e = 0; e < 4; ++e) ex[g][e] = __builtin_amdgcn_rcpf(ex[g][e]);
    ys[g] = f32x4{bflo(xv[g][0]), bfhi(xv[g][0]), bflo(xv[g][1]), bfhi(xv[g][1])};
  }
  SCHED0();
#pragma unroll
  for (int g = 0; g < N; ++g) {
    const f32x4 o = dk[g] * ex[g] + ys[g];
    *(volatile LAS u32x2*)(xp + g * 32) = u32x2{pack2(o[0], o[1]), pack2(o[2], o[3])};
  }
}

__device__ __forceinline__ void compute(const Params& p, LAS char* L, const Ids& id, int w) {
  int lane = fresh_lane(), r = lane & 15, q = lane >> 4;
  const __amdgpu_buffer_rsrc_t rws = __builtin_amdgcn_make_buffer_rsrc((void*)p.ws, 0, (int)W_END, 0x00020000);
  const f32x4 z4 = {0.f, 0.f, 0.f, 0.f};

  const bf16x8 ones = {0x3f80, 0x3f80, 0x3f80, 0x3f80, 0x3f80, 0x3f80, 0x3f80, 0x3f80};
  if (blockIdx.x < 240) {
    const int pc = blockIdx.x % 48, pr = blockIdx.x / 48;
    LAS char* a = L + BRING + (w ? w * 20480 + 10240 : 0) + (8 * q + (r >> 2)) * 128 + 8 * (r & 3);
    const unsigned go = lane * 16 + W_GU + (((pc % 24) * 4 * 20 + pr * 4 + w) * 2 + pc / 24) * 1024;
    wait_vm<0>();
    bf16x8 f[4];
#pragma unroll
    for (int c = 0; c < 4; ++c) f[c] = cat(ldstr(a + c * 32), ldstr(a + 512 + c * 32));
    f32x4 acc[4];
#pragma unroll
    for (int c = 0; c < 4; ++c) acc[c] = mfma(ones, f[c], z4);
    SCHED0();
#pragma unroll
    for (int c = 0; c < 4; ++c) __builtin_amdgcn_raw_buffer_store_b128(__builtin_bit_cast(u32x4, f[c]), rws, go + c * 40960, 0, AUX_SC1);
    SCHED0();
    float v01 = (q & 1) ? acc[1][0] : acc[0][0], v23 = (q & 1) ? acc[3][0] : acc[2][0];
    PIN(v01); PIN(v23);
    *(LAS float*)(L + T_CP + (w * 64 + lane) * 4) = (q & 2) ? v23 : v01;
  }

  bf16x8 xr[20][2];
  f32x4 accP[5][2], st[4];
  float cs[5];
  {
    f32x4 accS[2], accQ[2];
    bf16x8 pb[2][5];
    const int oK0 = r * 128 + ((q ^ (r >> 1)) & 7) * 16, oK1 = oK0 ^ 64;
    LAS char* xA[2] = {L + ARING + w * 4096 + oK0, L + ARING + w * 4096 + oK1};
    LAS char* pB[2] = {L + BRING + 10240 + oK0, L + BRING + 10240 + oK1};
    PIN(xA[0]); PIN(xA[1]); PIN(pB[0]); PIN(pB[1]);
    auto rdx = [&](int i, int ks, int t) { xr[2 * i + ks][t] = ldsr(xA[ks] + (i & 3) * 16384 + t * 2048); };
    auto rdw = [&](int i, int ks, int g) { pb[ks][g] = ldsr(pB[ks] + ((i & 3) ? (i & 3) * 20480 - 10240 : 0) + g * 2048); };
    auto stat = [&](int s, int k) {
      if (k < 2) accS[k] = mfma(xr[s][k], ones, s == 0 ? z4 : accS[k]);
      else accQ[k - 2] = mfma(xr[s][k - 2], xr[s][k - 2], s == 0 ? z4 : accQ[k - 2]);
    };
#pragma unroll
    for (int i = 0; i < 10; ++i) {
      if (i == DRAIN_G) wait_vm<0>();
      bar();
#pragma unroll
      for (int k = 0; k < 10; ++k) {
        if (i > 0) accP[k >> 1][k & 1] = mfma(xr[2 * i - 1][k & 1], pb[1][k >> 1], accP[k >> 1][k & 1]);
        if (k & 1) rdw(i, 0, k >> 1);
        else if (k < 8) rdx(i, k >> 2, (k >> 1) & 1);
        SCHED0();
      }
#pragma unroll
      for (int k = 0; k < 18; ++k) {
        if (k < 2) stat(2 * i, k);
        else if (k < 12) {
          accP[(k - 2) >> 1][k & 1] = mfma(xr[2 * i][k & 1], pb[0][(k - 2) >> 1], i == 0 ? z4 : accP[(k - 2) >> 1][k & 1]);
          if (k & 1) rdw(i, 1, (k - 2) >> 1);
        } else if (k < 14) stat(2 * i, k - 10);
        else stat(2 * i + 1, k - 14);
        SCHED0();
      }
    }
    lane = fresh_lane(); r = lane & 15; q = lane >> 4;
    const bool diag = (r >> 2) == q;
    LAS char* const tw = diag ? L + T_ST + (w * 32 + r) * 4 : L + T_PAD;
    LAS char* const te = diag ? L + T_EK + (w * 32 + r) * 16 : L + T_PAD;
    const float sg = id.cb >= 8 ? -1.f : 1.f;
    float sm[2], sq[2], rs[2];
#pragma unroll
    for (int k = 0; k < 10; ++k) {
      accP[k >> 1][k & 1] = mfma(xr[19][k & 1], pb[1][k >> 1], accP[k >> 1][k & 1]);
      if (k < 2) {
#pragma unroll
        for (int g = 3 * k; g < 3 * k + 3 && g < 5; ++g) cs[g] = *(LAS float*)(L + T_CS + (g * 16 + r) * 4);
      }
      if (k == 1 || k == 2) {
        const int t = k - 1;
        sm[t] = pick4(accS[t]) * (-1.0f / D); sq[t] = pick4(accQ[t]) * (1.0f / D);
        PIN(sm[t]); PIN(sq[t]);
      }
      if (k == 3) {
#pragma unroll
        for (int t = 0; t < 2; ++t) { sq[t] = fmaxf(sq[t] - sm[t] * sm[t], 0.f) + 1e-5f; PIN(sq[t]); }
#pragma unroll
        for (int t = 0; t < 2; ++t) { rs[t] = __builtin_amdgcn_rsqf(sq[t]); PIN(rs[t]); }
      }
      if (k == 4) {
#pragma unroll
        for (int t = 0; t < 2; ++t) {
          const float v = rs[t] * sg;
          *(LAS float*)(tw + t * 64) = v; *(LAS float*)(tw + 512 + t * 64) = v * sm[t];
        }
      }
      if (k == 5) {
#pragma unroll
        for (int n = 0; n < 4; ++n) st[n] = *(LAS f32x4*)(L + T_ST + (n >> 1) * 512 + (w * 32 + (n & 1) * 16 + 4 * q) * 4);
      }
      if (k == 6 || k == 7) {
        const int t = k - 6;
        *(LAS f32x4*)(te + t * 256) = f32x4{sm[t], -2.302208198f * rs[t], -0.1029432397f * rs[t] * rs[t] * rs[t], sq[t]};
      }
      SCHED0();
    }
  }
  LAS char* const fB0 = L + BRING + lane * 16;
  LAS char* const fB3 = L + BRING + 61440 + lane * 16;
  auto fragB = [&](int stage, int n) -> bf16x8 { return (stage & 3) == 3 ? ldsr(fB3 + n * 1024) : ldsr(fB0 + (stage & 3) * 20480 + n * 1024); };
  LAS char* const xB0 = L + ARING + w * 4096 + r * 128 + ((q ^ (r >> 1)) & 7) * 16;
  LAS char* const xB1 = L + ARING + w * 4096 + ((r * 128 + ((q ^ (r >> 1)) & 7) * 16) ^ 64);
  const unsigned abBase = W_ABT + (unsigned)((((id.cb >> 3) * 4 + ((id.cb & 7) >> 1)) * 64 + id.rb * 4 + w) * 10 + (id.cb & 1) * 5) * 1024u + lane * 16;

  u32x4 abo;
  f32x4 accH[2][4];
  bf16x8 wb[4][2];
  const unsigned hbase = W_H + (unsigned)(((id.rb * 2 + (w >> 1)) * 96 + id.cb * 6) * 2 + (w & 1)) * 1024u + lane * 16;
  float gv[8], uv[8], wv[8];
  f32x4 cgv, cuv, ek[2];
  auto h_tab = [&](int c) {
    cgv = *(LAS f32x4*)(L + T_CG + (c * 16 + 4 * q) * 4);
    cuv = *(LAS f32x4*)(L + T_CG + 384 + (c * 16 + 4 * q) * 4);
  };
  auto h_op = [&](int c, int n) {
    const int ph = n >> 3, x = n & 7, t = x >> 2, e = x & 3;
    if (ph == 0) { gv[x] = accH[c & 1][t][e] + ek[t][0] * cgv[e]; PIN(gv[x]); uv[x] = accH[c & 1][2 + t][e] + ek[t][0] * cuv[e]; PIN(uv[x]); }
    if (ph == 1) { wv[x] = gv[x] * (ek[t][1] + ek[t][2] * (gv[x] * gv[x])); PIN(wv[x]); }
    if (ph == 2) { wv[x] = __builtin_amdgcn_exp2f(wv[x]); PIN(wv[x]); uv[x] *= gv[x]; PIN(uv[x]); }
    if (ph == 3) { wv[x] = __builtin_amdgcn_rcpf(wv[x] * ek[t][3] + ek[t][3]); PIN(wv[x]); }
    if (ph == 4) {
      uv[x] *= wv[x]; PIN(uv[x]);
      if (x == 7) {
        u32x4 o = {pack2(uv[0], uv[1]), pack2(uv[2], uv[3]), pack2(uv[4], uv[5]), pack2(uv[6], uv[7])};
        __builtin_amdgcn_raw_buffer_store_b128(o, rws, hbase + c * 2048, 0, AUX_SC1);
      }
    }
  };
  const int wm = w >> 1, wn = w & 1;
  LAS char *aBase0, *aBase1, *yBase;
  bf16x8 xa0[4], xa1[4], yb0[5], yb1[5];
  auto rdg = [&](int g, int m) { wb[g & 3][m] = fragB(10 + g / 10, (g % 10) * 2 + m); };
  bar();
#pragma unroll
  for (int g = 0; g < 3; ++g) { rdg(g, 0); rdg(g, 1); }
  sfor<0, 120>([&](auto gi) __attribute__((always_inline)) {
    constexpr int g = decltype(gi)::v, c = g / 20, s = g % 20;
    if (g == 110) {
      const int l2 = fresh_lane(), r2 = l2 & 15, oK0 = r2 * 128 + (((l2 >> 4) ^ (r2 >> 1)) & 7) * 16;
      aBase0 = L + ARING + wm * 8192 + oK0; aBase1 = L + ARING + wm * 8192 + (oK0 ^ 64); yBase = L + BRING + wn * 5120 + l2 * 16;
    }
    if (g % 10 == 8) {
      if (g == 38) wait_vm<0>();
      bar();
    }
#pragma unroll
    for (int k = 0; k < 4; ++k) {
      accH[c & 1][k] = mfma(wb[g & 3][k >> 1], xr[s][k & 1], s == 0 ? z4 : accH[c & 1][k]);
      bool rd = true;
      if (g >= 118) {
        const int n = (g - 118) * 4 + k;
        if (n < 4) xa0[n] = ldsr(aBase0 + 2 * 16384 + n * 2048); else yb0[n - 4] = ldsr(yBase + 2 * 20480 + (n - 4) * 1024);
      } else if (g % 10 == 8) rdg(g + 2 + (k >> 1), k & 1);
      else if (g % 10 != 7 && k < 2) rdg(g + 3, k);
      else rd = false;
      if (!rd) {
        const int n = s % 10 == 7 ? 2 * s + k : 2 * s + k - 2;
        if (c == 0) {
          if (n < 25) {
            const int gg = n / 5, ph = n % 5;
            if (ph < 4) {
              const int t = ph >> 1, e = (ph & 1) * 2;
              unsigned pk = pack2(accP[gg][t][e] * st[t][e] + st[2 + t][e] * cs[gg], accP[gg][t][e + 1] * st[t][e + 1] + st[2 + t][e + 1] * cs[gg]);
              PIN(pk);
              abo[ph] = pk;
            } else __builtin_amdgcn_raw_buffer_store_b128(abo, rws, abBase + gg * 1024, 0, AUX_SC1);
          }
          if (n >= 10 && n < 26 && !(n & 1)) {
            const int m = (n - 10) >> 1;
            xr[14 + (m >> 1)][m & 1] = ldsr(((m & 2) ? xB1 : xB0) + ((7 + (m >> 2)) & 3) * 16384 + (m & 1) * 2048);
          }
          if (n == 34) ek[0] = *(LAS f32x4*)(L + T_EK + (w * 32 + r) * 16);
          if (n == 35) ek[1] = *(LAS f32x4*)(L + T_EK + (w * 32 + 16 + r) * 16);
        } else if (c < 5) h_op(c - 1, n);
        else if (n < 36) { h_op(4, n); if (n >= 32) h_op(4, n + 4); }
        if (n == (c == 0 ? 38 : 36)) h_tab(c);
      }
      SCHED0();
    }
  });

  f32x4 accY[5][4];
  auto y_stage = [&](int i, int h0) {
    LAS char* a0 = aBase0 + ((i + 2) & 3) * 16384; LAS char* a1 = aBase1 + ((i + 2) & 3) * 16384;
    LAS char* b = yBase + ((i + 2) & 3) * 20480;
    PIN(a0); PIN(a1); PIN(b);
    bar();
    int n = h0;
#pragma unroll
    for (int k = 0; k < 20; ++k) {
      accY[k >> 2][k & 3] = mfma(yb1[k >> 2], xa1[k & 3], accY[k >> 2][k & 3]);
      if (k < 4) xa0[k] = ldsr(a0 + k * 2048);
      else if (k < 9) yb0[k - 4] = ldsr(b + (k - 4) * 1024);
      else if (k < 11) yb1[k - 9] = ldsr(b + 10240 + (k - 9) * 1024);
      else if (k == 12) yb1[2] = ldsr(b + 10240 + 2048);
      else if (k == 16) yb1[3] = ldsr(b + 10240 + 3072);
      else if (n < 40) h_op(5, n++);
      SCHED0();
    }
#pragma unroll
    for (int k = 0; k < 20; ++k) {
      accY[k >> 2][k & 3] = mfma(yb0[k >> 2], xa0[k & 3], accY[k >> 2][k & 3]);
      if (k == 0) yb1[4] = ldsr(b + 10240 + 4096);
      else if (k < 5) xa1[k - 1] = ldsr(a1 + (k - 1) * 2048);
      else if (n < 40) h_op(5, n++);
      SCHED0();
    }
  };
#pragma unroll
  for (int k = 0; k < 20; ++k) {
    if (k == 0) yb0[4] = ldsr(yBase + 2 * 20480 + 4096);
    accY[k >> 2][k & 3] = mfma(yb0[k >> 2], xa0[k & 3], z4);
    if (k < 4) xa1[k] = ldsr(aBase1 + 2 * 16384 + k * 2048);
    else if (k < 9) yb1[k - 4] = ldsr(yBase + 2 * 20480 + 10240 + (k - 4) * 1024);
    else h_op(5, k - 9);
    SCHED0();
  }
  y_stage(1, 11); y_stage(2, 33);
#pragma nounroll
  for (int i = 3; i < 16; ++i) {
    if (i == 5) wait_vm<0>();
    y_stage(i, 40);
  }

  lane = fresh_lane(); r = lane & 15; q = lane >> 4;
  const int tile = id.tt * 4 + id.et;
  f32x4 accD[5][2];
#pragma unroll
  for (int g = 0; g < 5; ++g) { accD[g][0] = z4; accD[g][1] = z4; }
  LAS char* const hB = L + ARING + wm * 1024 + wn * 8192 + lane * 16 + wn * 8;
  LAS char* const dB = fB0 + wn * 10240;
  bf16x8 hh[2][2], dw[2][5];
  auto d_rd = [&](LAS char* a, LAS char* a2, LAS char* b, int ks, int m) {
    if (m < 2) hh[ks][m] = cat(*(volatile LAS bf16x4*)((m ? a2 : a) + ks * 4096), *(volatile LAS bf16x4*)((m ? a2 : a) + ks * 4096 + 2048));
    else dw[ks][m - 2] = ldsr(b + ks * 5120 + (m - 2) * 1024);
  };
  auto d_mm = [&](int ks, int k) { accD[k >> 1][k & 1] = mfma(dw[ks][k >> 1], hh[ks][k & 1], accD[k >> 1][k & 1]); };
  const unsigned pbase = W_P + (unsigned)(((tile * 4 + w) * 4 + id.kq) * 10) * 1024u + lane * 16;
  auto p_store = [&](int u) {
    u32x4 o[5];
#pragma unroll
    for (int g = 0; g < 5; ++g) {
      const f32x4 a = accY[g][2 * u], b = accY[g][2 * u + 1];
      o[g] = u32x4{pack2(a[0], a[1]), pack2(a[2], a[3]), pack2(b[0], b[1]), pack2(b[2], b[3])};
    }
    if (w != id.kq) {
#pragma unroll
      for (int g = 0; g < 5; ++g) __builtin_amdgcn_raw_buffer_store_b128(o[g], rws, pbase + (u * 5 + g) * 1024, 0, AUX_SC1);
    } else {
#pragma unroll
      for (int g = 0; g < 5; ++g) *(LAS u32x4*)(L + T_BIG + (u * 5 + g) * 1024 + lane * 16) = o[g];
    }
  };
  auto d_second = [&](LAS char* a, LAS char* a2, LAS char* b) {
#pragma unroll
    for (int k = 0; k < 10; ++k) {
      d_mm(0, k);
      if (k < 2) d_rd(a, a2, b, 1, k); else if (k < 4) d_rd(a, a2, b, 1, k + 3);
      SCHED0();
    }
  };
  auto d_stage = [&](int i) {
    LAS char* a = hB + ((i + 2) & 3) * 16384; LAS char* a2 = a + (8 - 16 * wn); LAS char* b = dB + ((i + 2) & 3) * 20480;
    PIN(a); PIN(a2); PIN(b);
    bar();
#pragma unroll
    for (int k = 0; k < 10; ++k) {
      d_mm(1, k);
      if (k < 7) d_rd(a, a2, b, 0, k); else d_rd(a, a2, b, 1, k - 5);
      SCHED0();
    }
    d_second(a, a2, b);
  };
  {
    LAS char* a = hB + 2 * 16384; LAS char* a2 = a + (8 - 16 * wn); LAS char* b = dB + 2 * 20480;
    PIN(a); PIN(a2); PIN(b);
    bar();
#pragma unroll
    for (int k = 0; k < 20; ++k) {
      accY[k >> 2][k & 3] = mfma(yb1[k >> 2], xa1[k & 3], accY[k >> 2][k & 3]);
      if (k < 7) d_rd(a, a2, b, 0, k); else if (k < 10) d_rd(a, a2, b, 1, k - 5);
      SCHED0();
    }
    d_second(a, a2, b);
  }
  p_store(0);
  p_store(1);
#pragma nounroll
  for (int i = 1; i < 12; ++i) {
    if (i == DRAIN_P - 38) wait_vm<0>();
    d_stage(i);
  }
#pragma unroll
  for (int k = 0; k < 10; ++k) {
    d_mm(1, k < 5 ? 2 * k + 1 : 2 * (9 - k));
    if (k >= 5) *(LAS f32x4*)(fB0 + ((w ^ 1) * 5 + k - 5) * 1024) = accD[k - 5][1];
    SCHED0();
  }
  LAS char* dp = fB0 + w * 5120;
  *(LAS f32x4*)(L + ARING + w * 2048 + 1024 + lane * 16) = accD[4][0];
  *(LAS f32x4*)(L + ARING + w * 2048 + lane * 16) = accD[3][0];
  LAS char* xp = L + X_TILE + (wm * 32 + 8 * (r >> 2) + 4 * wn + (r & 3)) * 160 + q * 8;
  PIN(xp); PIN(dp);
  bar();
  f32x4 dk[3], ys[3];
#pragma unroll
  for (int g = 0; g < 3; ++g) dk[g] = *(LAS f32x4*)(dp + g * 1024);
#pragma unroll
  for (int g = 0; g < 3; ++g) ys[g] = *(LAS f32x4*)(dp + 61440 + g * 1024);
  const f32x4 own[3] = {accD[0][0], accD[1][0], accD[2][0]};
  finish<3>(dk, own, ys, xp);
}

__device__ __forceinline__ void aux_tail(const Params& p, LAS char* L, int t, int lane) {
  const int po = (t >> 1) * 5120 + lane * 16 + (t & 1) * 8;
  u32x2 y[4][5];
#pragma unroll
  for (int g = 0; g < 5; ++g) {
    y[0][g] = *(volatile LAS u32x2*)(L + T_BIG + po + g * 1024);
    y[1][g] = *(volatile LAS u32x2*)(L + BRING + 2 * 20480 + po + g * 1024);
    y[2][g] = *(volatile LAS u32x2*)(L + BRING + 2 * 20480 + 10240 + po + g * 1024);
    y[3][g] = *(volatile LAS u32x2*)(L + ARING + 2 * 16384 + po + g * 1024);
  }
  SCHED0();
  LAS char* dp = L + ARING + t * 2048 + lane * 16;
  f32x4 ys[2];
#pragma unroll
  for (int g = 0; g < 5; ++g) {
    f32x4 v[4];
#pragma unroll
    for (int k = 0; k < 4; ++k) v[k] = f32x4{bflo(y[k][g][0]), bfhi(y[k][g][0]), bflo(y[k][g][1]), bfhi(y[k][g][1])};
    const f32x4 sum = (v[0] + v[1]) + (v[2] + v[3]);
    if (g < 3) *(LAS f32x4*)(L + BRING + 61440 + (t * 5 + g) * 1024 + lane * 16) = sum;
    else ys[g - 3] = sum;
  }
  const int r = lane & 15;
  LAS char* xp = L + X_TILE + ((t >> 1) * 32 + 8 * (r >> 2) + 4 * (t & 1) + (r & 3)) * 160 + (lane >> 4) * 8 + 96;
  LAS char* dq = L + BRING + t * 5120 + 3072 + lane * 16;
  PIN(xp); PIN(dp); PIN(dq); PIN(ys[0]); PIN(ys[1]);
  wait_vm<0>();
  bar();
  if (t == 3) { STAMP(50); }
  f32x4 dk[2], own[2];
#pragma unroll
  for (int g = 0; g < 2; ++g) { dk[g] = *(LAS f32x4*)(dq + g * 1024); own[g] = *(LAS f32x4*)(dp + g * 1024); }
  finish<2>(dk, own, ys, xp);
}

__device__ __forceinline__ void st_flag(unsigned char* ws, unsigned off, u64 v, int lane) {
  if (lane == 0) __hip_atomic_store((GAS u64*)(ws + off), v, __ATOMIC_RELAXED, __HIP_MEMORY_SCOPE_AGENT);
}
__device__ __forceinline__ void wait_flag(const unsigned char* ws, unsigned off, u64 tag, u64 seen) {
  if (!__all(seen == tag)) {
#pragma nounroll
    for (int spin = 0; spin < (1 << 16); ++spin)
      if (__all(ld_flag(ws, off) == tag)) break;
  }
  __builtin_amdgcn_fence(__ATOMIC_ACQUIRE, "wavefront");
}
template <int N, class Fn>
__device__ __forceinline__ void wait_tags(u32x4 (&g)[N], unsigned tlo, unsigned thi, Fn&& reload) {
  auto good = [&]() {
    unsigned bad = 0;
#pragma unroll
    for (int k = 0; k < N; ++k) bad |= (g[k][1] ^ tlo) | (g[k][3] ^ thi);
    return __all(bad == 0);
  };
  if (!good()) {
#pragma nounroll
    for (int spin = 0; spin < (1 << 16); ++spin) {
      reload();
      if (good()) break;
    }
  }
}
struct First { cptr base; unsigned v0, v1, v2, v3; int kc; };
#define SPIN(x) asm volatile("" : "+s"(x))
__device__ __forceinline__ void p1_tile(First& f, unsigned lb, int ld, int j, bool barrier) {
  cptr a = f.base + f.kc * 128, a2 = a + 32 * 1280;
  unsigned l = ld < 2 ? lb + ARING + (j & 3) * 16384 + ld * 8192 : lb + BRING + ((j & 3) ? (j & 3) * 20480 : 10240);
  SPIN(a); SPIN(a2); SPIN(l);
  if (barrier) bar();
  dma4(a, f.v0, f.v1, f.v2, f.v3, l);
  dma4(a2, f.v0, f.v1, f.v2, f.v3, l + 4096);
  if (ld == 2) dma2(a + 64 * 1280, f.v0, f.v1, l + 8192);
  f.kc = f.kc == 9 ? 0 : f.kc + 1;
}
__device__ __forceinline__ void loader(const Params& p, LAS char* L, unsigned lb, const Ids& id, int ld, int lane, First& f) {
  const int b = blockIdx.x;
  const unsigned vl = lane * 16;
  const unsigned c0 = ((lane & 7) ^ (lane >> 4)) * 16, c1 = c0 ^ 64;
  const int rl = 8 * (lane >> 5) + ((lane >> 3) & 3);
  const int rot = ROT;
#pragma nounroll
  for (int j = 2; j < 10; ++j) {
    if (j < 4) { if (ld == 2) wait_vm<10>(); else wait_vm<8>(); }
    if (j > 4) { if (ld == 2) wait_vm<20>(); else wait_vm<16>(); }
    p1_tile(f, lb, ld, j, j != 4);
  }
  const unsigned vw0 = rl * 4096 + c0, vw1 = (rl + 16) * 4096 + c1 - 1024, vw2 = (rl + 4) * 4096 + c0 - 2048, vw3 = (rl + 20) * 4096 + c1 - 3072;
  const unsigned u0 = ld ? vl : vw0, u1 = ld ? vl : vw1, u2 = ld ? vl : vw2, u3 = ld ? vl : vw3;
  const cptr bw = (cptr)(id.kq < 2 ? p.wsr : p.wsi) + (id.tt * 128 * 4096 + (id.kq & 1) * 2048);
  const int g0 = ld ? 2 * ld - 2 : 4;
  const unsigned l0 = lb + BRING + g0 * 4096, l1 = ld ? l0 + 4096 : lb + ARING, ls1 = ld ? 20480 : 16384, l2 = lb + ARING + (ld + 1) * 4096;
  auto yk = [&](int y) { return (y & ~3) | ((y + id.et) & 3); };
  {
    const cptr bg = p.ws + W_GU + (unsigned)id.cb * 245760u;
    const int ka[2] = {((g0 + rot) % 10) * 4096, ((g0 + rot + 5) % 10) * 4096}, kb[2] = {((g0 + 1 + rot) % 10) * 4096, ((g0 + rot + 6) % 10) * 4096};
    if (ld == 2) wait_vm<20>(); else wait_vm<16>();
    bar();
#pragma nounroll
    for (int j = 10; j < 22; ++j) {
      const cptr so = bg + ((j - 10) >> 1) * 40960;
      const int n = (j - 16) * 3 + ld;
      cptr a0 = so + ((j & 1) ? ka[1] : ka[0]), a1 = so + ((j & 1) ? kb[1] : kb[0]), a2 = bw + (yk(n >> 2) * 128 + (n & 3) * 32 * 4096);
      unsigned la = l0 + (j & 3) * 20480, lw = lb + ARING + (((n >> 2) + 2) & 3) * 16384 + (n & 3) * 4096;
      SPIN(a0); SPIN(a1); SPIN(a2); SPIN(la); SPIN(lw);
      if (j != 11) {
        if (j == 10) wait_vm<8>();
        else if (j < 18) { if (ld == 0) wait_vm<8>(); else wait_vm<16>(); }
        else { if (ld == 0) wait_vm<16>(); else wait_vm<24>(); }
        bar();
      }
      dma4c(a0, vl, la);
      if (ld) dma4c(a1, vl, la + 4096);
      if (n >= 0 && n < 16) dma4(a2, vw0, vw1, vw2, vw3, lw);
    }
  }
  const int c80 = id.et * 2 + (id.kq & 1);
  const cptr bh = p.ws + W_H + (unsigned)(id.tt * 2 + (id.kq >> 1)) * 196608u;
  const cptr bab = p.ws + W_ABT + ((unsigned)(((id.kq >> 1) * 4 + id.et) * 64 + (id.kq & 1) * 32) * 10240u + g0 * 4096);
  const cptr bwd = p.ws + W_WDT + (unsigned)c80 * 245760u;
  const cptr y1 = ld ? bab + 4096 : bw, y2 = bw + (ld + 1) * 32 * 4096;
  const int ym1 = ld ? 20480 : 128;
  const cptr d0 = bwd + g0 * 4096, d1 = ld ? d0 + 4096 : bh, d2 = bh + (ld + 1) * 4096;
#pragma nounroll
  for (int i = 19; i < 35; ++i) {
    const int k = yk(i - 19), sl = (i + 3) & 3;
    cptr a0 = bab + k * 20480, a1 = y1 + k * ym1, a2 = y2 + k * 128;
    unsigned la = l0 + sl * 20480, lc = l1 + sl * ls1, ld2 = l2 + sl * 16384;
    SPIN(a0); SPIN(a1); SPIN(a2); SPIN(la); SPIN(lc); SPIN(ld2);
    if (i >= 25) wait_vm<24>(); else if (i == 24) wait_vm<16>(); else if (ld == 0) wait_vm<8>(); else wait_vm<16>();
    bar();
    dma4c(a0, vl, la);
    if (i >= 23 || ld) dma4s(a1, u0, u1, u2, u3, lc);
    if (i >= 23) dma4(a2, vw0, vw1, vw2, vw3, ld2);
  }
#pragma nounroll
  for (int i = 35; i < 47; ++i) {
    const int sl = (i + 3) & 3;
    cptr a0 = d0 + (i - 35) * 20480, a1 = d1 + (i - 35) * ls1, a2 = d2 + (i - 35) * 16384;
    unsigned la = l0 + sl * 20480, lc = l1 + sl * ls1, ld2 = l2 + sl * 16384;
    SPIN(a0); SPIN(a1); SPIN(a2); SPIN(la); SPIN(lc); SPIN(ld2);
    wait_vm<24>();
    bar();
    dma4c(a0, vl, la);
    dma4c(a1, vl, lc);
    dma4c(a2, vl, ld2);
  }
  cptr bp = p.ws + W_P + ((unsigned)(((id.tt * 4 + id.et) * 4 + id.kq) * 4 * 10) * 1024u + ((id.kq + 1 + ld) & 3) * 10240);
  unsigned l = lb + (ld == 0 ? ARING + 2 * 16384 : BRING + 2 * 20480 + (ld - 1) * 10240);
  SPIN(bp); SPIN(l);
  wait_vm<24>(); bar();
  dma4c(bp, vl, l); dma4c(bp + 4096, vl, l + 4096); dma2c(bp + 8192, vl, l + 8192);
}

__device__ __forceinline__ void helper(const Params& p, LAS char* L, unsigned lb, const Ids& id, int lane, u64 pc, u64 te) {
  const int b = blockIdx.x;
  const __amdgpu_buffer_rsrc_t rws = __builtin_amdgcn_make_buffer_rsrc((void*)p.ws, 0, (int)W_END, 0x00020000);
  STAMP(60);
  __builtin_amdgcn_s_setprio(3);
  const u64 prev = ld_flag(p.ws, W_EPOCH);
  const int r8 = lane >> 3;
  u32x4 dv[10];
  const GAS char* ds = (const GAS char*)(b < 128 ? p.wdr : p.wdi) + (((b & 127) * 5 + (r8 < 5 ? r8 : 4)) * 1280 + (lane & 7) * 16);
#pragma unroll
  for (int i = 0; i < 10; ++i) dv[i] = *(const GAS u32x4*)(ds + i * 128);
  auto code_touch = [&]() {
    const unsigned off = (((b >> 3) % ((CODE_BYTES + 8191) / 8192)) * 64 + lane) * 128;
    touch((cptr)(pc & ~255ull), off < CODE_BYTES ? off : 0, lb + T_PAD);
  };
  const u64 tag = next_tag(prev);
  const unsigned tlo = (unsigned)tag, thi = (unsigned)(tag >> 32);
  {
    float t4[4] = {0.f, 0.f, 0.f, 0.f};
#pragma unroll
    for (int i = 0; i < 10; ++i)
#pragma unroll
      for (int e = 0; e < 4; ++e) t4[e] = dot_ones(dv[i][e], t4[e]);
    const unsigned sm = __builtin_bit_cast(unsigned, row8_sum((t4[0] + t4[1]) + (t4[2] + t4[3])));
    const u32x4 o = {sm, tlo, sm, thi};
    if ((lane & 7) == 0 && r8 < 5) __builtin_amdgcn_raw_buffer_store_b128(o, rws, W_CSD + (b * 5 + r8) * 16, 0, AUX_SC1);
  }
  const int pcl = (id.cb * 3) >> 1, tile = id.tt * 4 + id.et, c80 = id.et * 2 + (id.kq & 1);
  const int wk = b / 5, wc = b % 5;
  u64 f0 = 0, f1 = 0, f2 = 0;
  unsigned fo0 = 0, fo1 = 0, fo2 = 0;
  u32x4 gr[2], cq[10];
  const int go = lane < 40 ? lane : 0;
  auto ld_gr = [&]() {
    gr[0] = __builtin_amdgcn_raw_buffer_load_b128(rws, W_CSD + (id.cb * 80 + go) * 16, 0, AUX_SC1);
    gr[1] = __builtin_amdgcn_raw_buffer_load_b128(rws, W_CSD + (id.cb * 80 + 40 + go) * 16, 0, AUX_SC1);
  };
  auto ld_cq = [&]() {
#pragma unroll
    for (int k = 0; k < 10; ++k) cq[k] = __builtin_amdgcn_raw_buffer_load_b128(rws, W_CSP + ((k * 24 + pcl) * 32 + lane) * 16, 0, AUX_SC1);
  };
  const cptr wsb = (cptr)(id.kq < 2 ? p.wsr : p.wsi) + (id.tt * 128 * 4096 + (id.kq & 1) * 2048 + id.et * 128);
  sfor<0, 48>([&](auto ic) __attribute__((always_inline)) {
    constexpr int i = decltype(ic)::v;
    if constexpr (i == 8) wait_flag(p.ws, fo0, tag, f0);
    if constexpr (i == 35) { wait_flag(p.ws, fo1, tag, f1); wait_flag(p.ws, fo2, tag, f2); }
    if constexpr (i == 19 || i == 35 || i == 47) wait_flag(p.ws, fo0, tag, f0);
    bar();
    STAMP(i);
    if constexpr (i == 0) {
      if (b < 240 && lane < 32) {
        LAS float* cp = (LAS float*)(L + T_CP + lane * 4);
        const float u0 = (cp[0] + cp[64]) + (cp[128] + cp[192]), u1 = (cp[32] + cp[96]) + (cp[160] + cp[224]);
        const u32x4 o = {__builtin_bit_cast(unsigned, u0), tlo, __builtin_bit_cast(unsigned, u1), thi};
        __builtin_amdgcn_raw_buffer_store_b128(o, rws, W_CSP + (b * 32 + lane) * 16, 0, AUX_SC1);
      }
    }
    if constexpr (i == DRAIN_G) st_flag(p.ws, W_FG + b * 8, tag, lane);
    if constexpr (i == 5) ld_gr();
    if constexpr (i == 6) {
      const int l20 = lane < 20 ? lane : 0;
      fo0 = W_FG + ((l20 >> 1) * 24 + pcl + (l20 & 1)) * 8;
      f0 = ld_flag(p.ws, fo0);
    }
    if constexpr (i == 8) {
      wait_tags<2>(gr, tlo, thi, ld_gr);
      *(LAS unsigned*)(L + T_CS + go * 4) = gr[0][0];
      *(LAS unsigned*)(L + T_CS + (40 + go) * 4) = gr[1][0];
    }
    if constexpr (i == 8) ld_cq();
    if constexpr (i == 12) {
      if (b < 240) {
        const cptr sn = (cptr)p.wdn + (wk * 32 * 1280 + wc * 256);
        const unsigned vn = (lane >> 4) * 1280 + (lane & 15) * 16;
        SELNT(NT_W, dma4)(sn, vn, vn + 4096, vn + 2 * 4096, vn + 3 * 4096, lb + T_BIG);
        SELNT(NT_W, dma4)(sn + 16 * 1280, vn, vn + 4096, vn + 2 * 4096, vn + 3 * 4096, lb + T_BIG + 4096);
      }
    }
    if constexpr (i == 10) wait_tags<10>(cq, tlo, thi, ld_cq);
    if constexpr (i == 11) {
#pragma unroll
      for (int m = 0; m < 2; ++m)
#pragma unroll
        for (int h = 0; h < 2; ++h) {
          const unsigned col = (lane >> 5) * 64 + h * 32 + (lane & 31) - (id.cb & 1) * 32;
          const float v = ((asf(cq[m][2 * h]) + asf(cq[2 + m][2 * h])) + (asf(cq[4 + m][2 * h]) + asf(cq[6 + m][2 * h]))) + asf(cq[8 + m][2 * h]);
          *(LAS float*)(col < 96 ? L + T_CG + (m * 96 + col) * 4 : L + T_PAD) = v;
        }
    }
    if constexpr (i >= 19 && i < 23) {
      if (b < 240) {
        LAS char* a = L + T_BIG + (4 * (lane >> 4) + ((lane & 15) >> 2)) * 256 + 8 * (lane & 3);
#pragma unroll
        for (int c = (i - 19) * 2; c < (i - 19) * 2 + 2; ++c) {
          const bf16x8 f = cat(ldstr(a + c * 32), ldstr(a + 4096 + c * 32));
          const int eg = wc * 8 + c, cc = eg / 5, g5 = eg - cc * 5;
          __builtin_amdgcn_raw_buffer_store_b128(__builtin_bit_cast(u32x4, f), rws, lane * 16 + W_WDT + ((cc * 48 + wk) * 5 + g5) * 1024, 0, AUX_SC1);
        }
      }
    }
    if constexpr (i == 14) st_flag(p.ws, W_FB + (id.rb * 16 + id.cb) * 8, tag, lane);
    if constexpr (i == 16) code_touch();
    if constexpr (i == 26) { wait_vm<0>(); st_flag(p.ws, W_FA + b * 8, tag, lane); }
    if constexpr (i == 17) {
      fo0 = W_FB + ((8 * (id.kq & 1) + (lane & 7)) * 16 + 8 * (id.kq >> 1) + 2 * id.et + ((lane >> 3) & 1)) * 8;
      f0 = ld_flag(p.ws, fo0);
    }
    if constexpr (i == 31) {
      fo1 = W_FA + ((lane < 48 ? lane : 0) * 5 + (c80 * 5) / 8) * 8;
      fo2 = W_FA + ((lane < 48 ? lane : 0) * 5 + (c80 * 5 + 4) / 8) * 8;
      f1 = ld_flag(p.ws, fo1); f2 = ld_flag(p.ws, fo2);
    }
    if constexpr (i == 27) st_flag(p.ws, W_FH + (id.rb * 16 + id.cb) * 8, tag, lane);
    if constexpr (i == 30) code_touch();
    if constexpr (i == 32) { fo0 = W_FH + (id.tt * 16 + (lane & 15)) * 8; f0 = ld_flag(p.ws, fo0); }
    if constexpr (i == 35) {
      gr[0] = __builtin_amdgcn_raw_buffer_load_b128(rws, W_FA + lane * 32, 0, AUX_SC1);
      gr[1] = __builtin_amdgcn_raw_buffer_load_b128(rws, W_FA + lane * 32 + 16, 0, AUX_SC1);
    }
    if constexpr (i == 37) {
      const bool all = __all(((gr[0][0] ^ tlo) | (gr[0][1] ^ thi) | (gr[0][2] ^ tlo) | (gr[0][3] ^ thi) | (gr[1][0] ^ tlo) | (gr[1][1] ^ thi) | (gr[1][2] ^ tlo) | (gr[1][3] ^ thi)) == 0);
      if (b == 0) {
        if (!all) {
#pragma nounroll
          for (int k = 0; k < 4; ++k) wait_flag(p.ws, W_FA + (k * 64 + lane) * 8, tag, ~tag);
        }
        st_flag(p.ws, W_EPOCH, tag, lane);
      }
    }
    if constexpr (i == DRAIN_P) st_flag(p.ws, W_FP + (tile * 4 + id.kq) * 8, tag, lane);
    if constexpr (i == 45) { fo0 = W_FP + (tile * 4 + (lane & 3)) * 8; f0 = ld_flag(p.ws, fo0); }
    if constexpr (WT0 >= 0 && i >= WT0 && i < WT0 + 8 * WTS && (i - WT0) % WTS == 0) {
      constexpr int n = (i - WT0) / WTS;
      touch(wsb + ((n & 1) * 64 * 4096 + (n >> 1) * 512), lane * 4096, lb + T_PAD);
    }
  });
  __builtin_amdgcn_s_waitcnt(0x0F70);
}

__global__ __launch_bounds__(NTHR, 2) void fused_kernel(Params p) {
  extern __shared__ __attribute__((aligned(16))) char smem[];
  u64 te;
  asm volatile("s_memrealtime %0\n\ts_waitcnt lgkmcnt(0)" : "=s"(te));
  u64 pc;
  asm volatile("s_getpc_b64 %0" : "=s"(pc));
  LAS char* L = (LAS char*)smem;
  const unsigned lb = (unsigned)(size_t)L;
  const int lane = threadIdx.x & 63, w = __builtin_amdgcn_readfirstlane(threadIdx.x >> 6);
  const int b = blockIdx.x, g = b & 7, rr = b >> 3;
  if (w == 7) {
    const unsigned off = ((rr % ((CODE_BYTES + 8191) / 8192)) * 64 + lane) * 128;
    touch((cptr)(pc & ~255ull), off < CODE_BYTES ? off : 0, lb + T_PAD);
  }
  Ids id;
  id.rb = (g >> 1) * 4 + (rr >> 3);
  id.cb = (g & 1) * 8 + (rr & 7);
  id.tt = (g & 1) * 8 + (rr & 7);
  id.et = rr >> 3;
  id.kq = g >> 1;
  First f;
  {
    const int pc48 = b % 48, pr = b / 48;
    const bool isx = w == 4 || w == 5;
    const unsigned row = isx ? 8 * (lane >> 5) + ((lane >> 3) & 3) : lane >> 3, stride = w < 4 ? 3072 : 1280;
    const unsigned c0 = ((lane & 7) ^ (w < 4 ? 0 : lane >> 4)) * 16, c1 = c0 ^ (w < 4 ? 0 : 64);
    f.v0 = row * stride + c0;
    f.v1 = row * stride + c1 + ((isx ? 16 : 8) * stride - 1024);
    f.v2 = f.v0 + ((isx ? 4 : 16) * stride - 2048);
    f.v3 = f.v1 + ((isx ? 4 : 16) * stride - 2048);
    f.base = w < 4 ? (cptr)(pc48 < 24 ? p.wg : p.wu) + ((pr * 128 + w * 32) * 3072 + (pc48 % 24) * 128)
                   : isx ? (cptr)p.x + (id.rb * 128 + (w - 4) * 64) * 1280 : (cptr)(id.cb < 8 ? p.wdr : p.wdi) + (id.cb & 7) * 80 * 1280;
    f.kc = ROT % 10;
    if (w < 4) {
      if (b < 240) SELNT(NT_G, dma4)(f.base, f.v0, f.v1, f.v2, f.v3, lb + BRING + (w ? w * 20480 + 10240 : 0));
    } else if (w < 7) {
      p1_tile(f, lb, w - 4, 0, false);
      if (w == 4) { const u64 t1 = __builtin_amdgcn_s_memrealtime(); *(LAS u64*)(L + TAB + 14336) = b == 5 ? (t1 - te) * 5 : 0; }
      p1_tile(f, lb, w - 4, 1, false);
    }
  }
  if (w >= 4) {
    const int ld = w - 4;
    if (ld < 3) loader(p, L, lb, id, ld, lane, f);
    else helper(p, L, lb, id, lane, pc, te);
    const int n0 = ld * 64 + lane, n1 = n0 + 256, n2 = ld < 2 ? n0 + 512 : n0;
    unsigned x0 = (n0 / 10) * 1120 + n0 * 16, x1 = (n1 / 10) * 1120 + n1 * 16, x2 = (n2 / 10) * 1120 + n2 * 16;
    PIN(x0); PIN(x1); PIN(x2);
    const cptr so = (cptr)p.x + ((id.tt * 128 + (id.kq >> 1) * 64) * 1280 + (id.et * 2 + (id.kq & 1)) * 160);
    wait_vm<22>();
    bar();
    if (w == 7) { STAMP(48); }
    dma1(so, x0, lb + X_TILE + ld * 1024); dma1(so, x1, lb + X_TILE + (ld + 4) * 1024); dma1(so, x2, lb + X_TILE + (ld < 2 ? ld + 8 : ld) * 1024);
    wait_vm<3>();
    bar();
    if (w == 7) { STAMP(49); }
    aux_tail(p, L, ld, lane);
  } else compute(p, L, id, w);
  const int tid = w * 64 + fresh_lane();
  const __amdgpu_buffer_rsrc_t ro = __builtin_amdgcn_make_buffer_rsrc((char*)p.out + ((id.tt * 128 + (id.kq >> 1) * 64) * 1280 + (id.et * 160 + (id.kq & 1) * 80) * 2), 0, 63 * 1280 + 160, 0x00020000);
  unsigned o0 = (tid / 10) * 1120 + tid * 16, o1 = ((tid + 512) / 10) * 1120 + (tid + 512) * 16;
  PIN(o0); PIN(o1);
  bar();
  const u32x4 v0 = *(LAS u32x4*)(L + X_TILE + tid * 16);
  if (w < 2) {
    const u32x4 v1 = *(LAS u32x4*)(L + X_TILE + 8192 + tid * 16);
    __builtin_amdgcn_raw_buffer_store_b128(v1, ro, o1, 0, AUX_SC1);
  }
  __builtin_amdgcn_raw_buffer_store_b128(v0, ro, o0, 0, AUX_SC1);
  if (w == 7) { const u64 d = *(LAS u64*)(L + TAB + 14336); const u64 t0 = __builtin_amdgcn_s_memrealtime(); while (__builtin_amdgcn_s_memrealtime() - t0 < d) __builtin_amdgcn_s_sleep(1); }
}
}

extern "C" void kernel_launch(void* const* d_in, const int* in_sizes, int n_in, void* d_out, int out_size, void* d_ws, size_t ws_size,
                              hipStream_t stream) {
  Params p;
  p.x = (const u16*)d_in[0];
  p.wsr = (const u16*)d_in[1];
  p.wsi = (const u16*)d_in[2];
  p.wdr = (const u16*)d_in[3];
  p.wdi = (const u16*)d_in[4];
  p.wg = (const u16*)d_in[6];
  p.wu = (const u16*)d_in[7];
  p.wdn = (const u16*)d_in[8];
  p.out = (u16*)d_out;
  p.ws = (unsigned char*)d_ws;
  (void)hipFuncSetAttribute((const void*)fused_kernel, hipFuncAttributeMaxDynamicSharedMemorySize, LDS_BYTES);
  fused_kernel<<<256, NTHR, LDS_BYTES, stream>>>(p);
}
```

```cpp
#include <hip/hip_runtime.h>
#include <hip/hip_bf16.h>

typedef unsigned short u16;
typedef unsigned long long u64;
typedef short bf16x8 __attribute__((ext_vector_type(8)));
typedef short bf16x4 __attribute__((ext_vector_type(4)));
typedef float f32x4 __attribute__((ext_vector_type(4)));
typedef float f32x2 __attribute__((ext_vector_type(2)));
typedef unsigned u32x4 __attribute__((ext_vector_type(4)));
typedef unsigned u32x2 __attribute__((ext_vector_type(2)));
typedef int i32x4 __attribute__((ext_vector_type(4)));
typedef __bf16 bf2v __attribute__((ext_vector_type(2)));
#define LAS __attribute__((address_space(3)))
#define GAS __attribute__((address_space(1)))

namespace {
constexpr int S = 2048, D = 640, F = 1536;
constexpr int NTHR = 512;
constexpr int ARING = 0;
constexpr int BRING = 65536;
constexpr int TAB = 147456;
constexpr int T_ST = TAB;
constexpr int T_CS = TAB + 1024;
constexpr int T_CP = TAB + 1344;
constexpr int T_CG = TAB + 2368;
constexpr int T_PAD = TAB + 3328;
constexpr int T_EK = TAB + 12288;
constexpr int T_BIG = TAB + 4096;
constexpr int X_TILE = ARING + 3 * 16384;
constexpr int LDS_BYTES = 163840;
constexpr unsigned W_EPOCH = 0;
constexpr unsigned FS = 128;
constexpr unsigned W_FA = 4096, W_FB = W_FA + 256 * FS, W_FH = W_FB + 256 * FS, W_FP = W_FH + 256 * FS, W_FG = W_FP + 256 * FS;
constexpr unsigned W_CSD = W_FG + 256 * FS;
constexpr unsigned W_CSP = W_CSD + 256 * 128;
constexpr unsigned W_ABT = 524288;
static_assert(W_CSP + 240 * 512 <= W_ABT, "workspace map");
constexpr unsigned W_H = W_ABT + 2u * 4 * 64 * 10 * 1024;
constexpr unsigned W_WDT = W_H + 32u * 96 * 2 * 1024;
constexpr unsigned W_GU = W_WDT + 8u * 48 * 5 * 1024;
constexpr unsigned W_P = W_GU + 16u * 20 * 12 * 1024;
constexpr unsigned W_END = W_P + 256u * 4 * 10 * 1024;
constexpr unsigned W_STAMP = W_END;
constexpr int AUX_SC1 = 16;
#define DRAIN_G 3
#define PRO 3
#define DRAIN_P 41
#define CODE_BYTES 32000
#define WT0 -1
#define WTS 2
#define YROT 1
#define DROT 1
#define MAP 1
#define ROT (3 * (b >> 6) + ((b >> 3) & 7))
#define NT_W 0
#define NT_G 0
#define SELNT(c, f) ((c) ? f##nt : f)
#define GB 7

struct Params {
  const u16 *x, *wsr, *wsi, *wdr, *wdi, *wg, *wu, *wdn;
  u16* out;
  unsigned char* ws;
};

typedef const unsigned char* cptr;
__device__ __forceinline__ cptr uni(cptr a) {
  const u64 v = (u64)a;
  return (cptr)(((u64)(unsigned)__builtin_amdgcn_readfirstlane((int)(v >> 32)) << 32) | (unsigned)__builtin_amdgcn_readfirstlane((int)v));
}
#define DMA_BODY(POL) \
__device__ __forceinline__ void dma2##POL(cptr base, unsigned v0, unsigned v1, unsigned lds) { \
  asm volatile("s_mov_b32 m0, %3\n\ts_nop 0\n\tglobal_load_lds_dwordx4 %0, %2 " #POL "\n\tglobal_load_lds_dwordx4 %1, %2 offset:1024 " #POL \
               ::"v"(v0), "v"(v1), "s"(base), "s"(lds) : "memory"); \
} \
__device__ __forceinline__ void dma3##POL(cptr base, unsigned v0, unsigned v1, unsigned v2, unsigned lds) { \
  asm volatile("s_mov_b32 m0, %4\n\ts_nop 0\n\tglobal_load_lds_dwordx4 %0, %3 " #POL "\n\tglobal_load_lds_dwordx4 %1, %3 offset:1024 " #POL "\n\t" \
               "global_load_lds_dwordx4 %2, %3 offset:2048 " #POL ::"v"(v0), "v"(v1), "v"(v2), "s"(base), "s"(lds) : "memory"); \
} \
__device__ __forceinline__ void dma4##POL(cptr base, unsigned v0, unsigned v1, unsigned v2, unsigned v3, unsigned lds) { \
  asm volatile("s_mov_b32 m0, %5\n\ts_nop 0\n\tglobal_load_lds_dwordx4 %0, %4 " #POL "\n\tglobal_load_lds_dwordx4 %1, %4 offset:1024 " #POL "\n\t" \
               "global_load_lds_dwordx4 %2, %4 offset:2048 " #POL "\n\tglobal_load_lds_dwordx4 %3, %4 offset:3072 " #POL \
               ::"v"(v0), "v"(v1), "v"(v2), "v"(v3), "s"(base), "s"(lds) : "memory"); \
}
DMA_BODY()
DMA_BODY(nt)
DMA_BODY(sc1)
#define dma4s dma4sc1
__device__ __forceinline__ void dma1(cptr base, unsigned v, unsigned lds) {
  asm volatile("s_mov_b32 m0, %2\n\ts_nop 0\n\tglobal_load_lds_dwordx4 %0, %1" ::"v"(v), "s"(base), "s"(lds) : "memory");
}
__device__ __forceinline__ void dma4c(cptr base, unsigned v, unsigned lds) {
  asm volatile("s_mov_b32 m0, %2\n\ts_nop 0\n\tglobal_load_lds_dwordx4 %0, %1 sc1\n\tglobal_load_lds_dwordx4 %0, %1 offset:1024 sc1\n\t"
               "global_load_lds_dwordx4 %0, %1 offset:2048 sc1\n\tglobal_load_lds_dwordx4 %0, %1 offset:3072 sc1"
               ::"v"(v), "s"(base), "s"(lds) : "memory");
}
__device__ __forceinline__ void dma2c(cptr base, unsigned v, unsigned lds) {
  asm volatile("s_mov_b32 m0, %2\n\ts_nop 0\n\tglobal_load_lds_dwordx4 %0, %1 sc1\n\tglobal_load_lds_dwordx4 %0, %1 offset:1024 sc1"
               ::"v"(v), "s"(base), "s"(lds) : "memory");
}
__device__ __forceinline__ void touch(cptr base, unsigned voff, unsigned pad) {
  asm volatile("s_mov_b32 m0, %2\n\ts_nop 0\n\tglobal_load_lds_dword %0, %1" ::"v"(voff), "s"(base), "s"(pad) : "memory");
}
template <int N> __device__ __forceinline__ void wait_vm() { asm volatile("s_waitcnt vmcnt(%0)" ::"i"(N) : "memory"); }
#define SCHED0() __builtin_amdgcn_sched_barrier(0)
__device__ __forceinline__ void bar() {
  SCHED0();
  __builtin_amdgcn_s_waitcnt(0xC07F);
  asm volatile("s_barrier" ::: "memory");
  SCHED0();
}
__device__ __forceinline__ f32x4 mfma(bf16x8 a, bf16x8 b, f32x4 c) { return __builtin_amdgcn_mfma_f32_16x16x32_bf16(a, b, c, 0, 0, 0); }
__device__ __forceinline__ bf16x8 ldsr(LAS char* p) { return *(LAS bf16x8*)p; }
__device__ __forceinline__ bf16x4 ldstr(LAS char* p) { return __builtin_amdgcn_ds_read_tr16_b64_v4i16((LAS bf16x4*)p); }
__device__ __forceinline__ bf16x8 cat(bf16x4 a, bf16x4 b) { return __builtin_shufflevector(a, b, 0, 1, 2, 3, 4, 5, 6, 7); }
__device__ __forceinline__ unsigned pack2(float a, float b) {
  f32x2 v = {a, b};
  bf2v r = __builtin_convertvector(v, bf2v);
  return __builtin_bit_cast(unsigned, r);
}
__device__ __forceinline__ float asf(unsigned v) { return __builtin_bit_cast(float, v); }
__device__ __forceinline__ float bflo(unsigned v) { return __builtin_bit_cast(float, v << 16); }
__device__ __forceinline__ float bfhi(unsigned v) { return __builtin_bit_cast(float, v & 0xffff0000u); }
template <int CTRL> __device__ __forceinline__ float dpp_add(float v) {
  return v + __builtin_bit_cast(float, __builtin_amdgcn_update_dpp(0, __builtin_bit_cast(int, v), CTRL, 0xf, 0xf, true));
}
__device__ __forceinline__ float row8_sum(float v) {
  v = dpp_add<0xB1>(v); v = dpp_add<0x4E>(v); v = dpp_add<0x141>(v);
  return v;
}
__device__ __forceinline__ float dot_ones(unsigned v, float acc) {
  return __builtin_amdgcn_fdot2_f32_bf16(__builtin_bit_cast(bf2v, v), __builtin_bit_cast(bf2v, 0x3f803f80u), acc, false);
}
__device__ __forceinline__ u64 ld_flag(const unsigned char* ws, unsigned off) {
  return __hip_atomic_load((GAS u64*)(ws + off), __ATOMIC_RELAXED, __HIP_MEMORY_SCOPE_AGENT);
}
__device__ __forceinline__ u64 next_tag(u64 prev) { return prev * 6364136223846793005ull + 1442695040888963407ull; }

#define STAMP(i) do { } while (0)

struct Ids {
  int rb, cb, tt, et, kq;
};

template <int I> struct IC { static constexpr int v = I; };
template <int I, int N, class Fn> __device__ __forceinline__ void sfor(Fn&& f) {
  if constexpr (I < N) { f(IC<I>{}); sfor<I + 1, N>(f); }
}
__device__ __forceinline__ int fresh_lane() {
  int l;
  asm volatile("v_mbcnt_lo_u32_b32 %0, -1, 0\n\tv_mbcnt_hi_u32_b32 %0, -1, %0" : "=v"(l));
  return l;
}
__device__ __forceinline__ float pick4(f32x4 v) {
  float o, t;
  asm volatile("v_cndmask_b32_e64 %0, %2, %3, %6\n\tv_cndmask_b32_e64 %1, %4, %5, %6\n\tv_cndmask_b32_e64 %0, %0, %1, %7"
               : "=&v"(o), "=&v"(t) : "v"(v[0]), "v"(v[1]), "v"(v[2]), "v"(v[3]), "s"(0xAAAAAAAAAAAAAAAAull), "s"(0xCCCCCCCCCCCCCCCCull));
  return o;
}
#define PIN(x) asm volatile("" : "+v"(x))
template <int N>
__device__ __forceinline__ void finish(f32x4 (&dk)[N], const f32x4 (&d1)[N], f32x4 (&ys)[N], LAS char* xp) {
  f32x4 ex[N];
  u32x2 xv[N];
#pragma unroll
  for (int g = 0; g < N; ++g) xv[g] = *(volatile LAS u32x2*)(xp + g * 32);
  SCHED0();
#pragma unroll
  for (int g = 0; g < N; ++g) { dk[g] += d1[g]; ex[g] = dk[g] * -1.4426950409f; }
  SCHED0();
#pragma unroll
  for (int g = 0; g < N; ++g) {
#pragma unroll
    for (int e = 0; e < 4; ++e) ex[g][e] = __builtin_amdgcn_exp2f(ex[g][e]);
    dk[g] *= ys[g];
  }
  SCHED0();
#pragma unroll
  for (int g = 0; g < N; ++g) ex[g] += 1.f;
  SCHED0();
#pragma unroll
  for (int g = 0; g < N; ++g) {
#pragma unroll
    for (int e = 0; e < 4; ++e) ex[g][e] = __builtin_amdgcn_rcpf(ex[g][e]);
    ys[g] = f32x4{bflo(xv[g][0]), bfhi(xv[g][0]), bflo(xv[g][1]), bfhi(xv[g][1])};
  }
  SCHED0();
#pragma unroll
  for (int g = 0; g < N; ++g) {
    const f32x4 o = dk[g] * ex[g] + ys[g];
    *(volatile LAS u32x2*)(xp + g * 32) = u32x2{pack2(o[0], o[1]), pack2(o[2], o[3])};
  }
}

__device__ __forceinline__ void compute(const Params& p, LAS char* L, const Ids& id, int w) {
  int lane = fresh_lane(), r = lane & 15, q = lane >> 4;
  const __amdgpu_buffer_rsrc_t rws = __builtin_amdgcn_make_buffer_rsrc((void*)p.ws, 0, (int)W_END, 0x00020000);
  const f32x4 z4 = {0.f, 0.f, 0.f, 0.f};

  const bf16x8 ones = {0x3f80, 0x3f80, 0x3f80, 0x3f80, 0x3f80, 0x3f80, 0x3f80, 0x3f80};
  if (blockIdx.x < 240) {
    const int pc = blockIdx.x % 48, pr = blockIdx.x / 48;
    LAS char* a = L + BRING + (w ? w * 20480 + 10240 : 0) + (8 * q + (r >> 2)) * 128 + 8 * (r & 3);
    const unsigned go = lane * 16 + W_GU + (((pc % 24) * 4 * 20 + pr * 4 + w) * 2 + pc / 24) * 1024;
    wait_vm<0>();
    bf16x8 f[4];
#pragma unroll
    for (int c = 0; c < 4; ++c) f[c] = cat(ldstr(a + c * 32), ldstr(a + 512 + c * 32));
    f32x4 acc[4];
#pragma unroll
    for (int c = 0; c < 4; ++c) acc[c] = mfma(ones, f[c], z4);
    SCHED0();
#pragma unroll
    for (int c = 0; c < 4; ++c) __builtin_amdgcn_raw_buffer_store_b128(__builtin_bit_cast(u32x4, f[c]), rws, go + c * 40960, 0, AUX_SC1);
    SCHED0();
    float v01 = (q & 1) ? acc[1][0] : acc[0][0], v23 = (q & 1) ? acc[3][0] : acc[2][0];
    PIN(v01); PIN(v23);
    *(LAS float*)(L + T_CP + (w * 64 + lane) * 4) = (q & 2) ? v23 : v01;
  }

  bf16x8 xr[20][2];
  LAS char *fB0, *fB3;
  bf16x8 wb[4][2];
  auto rdg = [&](int g, int m) {
    const int sl = (10 + g / 10) & 3, n = (g % 10) * 2 + m;
    wb[g & 3][m] = sl == 3 ? ldsr(fB3 + n * 1024) : ldsr(fB0 + sl * 20480 + n * 1024);
  };
  f32x4 accP[5][2], st[4];
  float cs[5];
  {
    f32x4 accS[2], accQ[2];
    bf16x8 pb[2][5];
    const int oK0 = r * 128 + ((q ^ (r >> 1)) & 7) * 16, oK1 = oK0 ^ 64;
    LAS char* xA[2] = {L + ARING + w * 4096 + oK0, L + ARING + w * 4096 + oK1};
    LAS char* pB[2] = {L + BRING + 10240 + oK0, L + BRING + 10240 + oK1};
    PIN(xA[0]); PIN(xA[1]); PIN(pB[0]); PIN(pB[1]);
    auto rdx = [&](int i, int ks, int t) { xr[2 * i + ks][t] = ldsr(xA[ks] + (i & 3) * 16384 + t * 2048); };
    auto rdw = [&](int i, int ks, int g) { pb[ks][g] = ldsr(pB[ks] + ((i & 3) ? (i & 3) * 20480 - 10240 : 0) + g * 2048); };
    auto stat = [&](int s, int k) {
      if (k < 2) accS[k] = mfma(ones, xr[s][k], s == 0 ? z4 : accS[k]);
      else accQ[k - 2] = mfma(xr[s][k - 2], xr[s][k - 2], s == 0 ? z4 : accQ[k - 2]);
    };
#pragma unroll
    for (int i = 0; i < 10; ++i) {
      if (i == DRAIN_G) wait_vm<0>();
      bar();
#pragma unroll
      for (int k = 0; k < 10; ++k) {
        if (i > 0) accP[k >> 1][k & 1] = mfma(xr[2 * i - 1][k & 1], pb[1][k >> 1], accP[k >> 1][k & 1]);
        if (k & 1) rdw(i, 0, k >> 1);
        else if (k < 8) rdx(i, k >> 2, (k >> 1) & 1);
        SCHED0();
      }
      if (i < 9) {
#pragma unroll
        for (int k = 0; k < 18; ++k) {
          if (k < 2) stat(2 * i, k);
          else if (k < 12) {
            accP[(k - 2) >> 1][k & 1] = mfma(xr[2 * i][k & 1], pb[0][(k - 2) >> 1], i == 0 ? z4 : accP[(k - 2) >> 1][k & 1]);
            if (k & 1) rdw(i, 1, (k - 2) >> 1);
          } else if (k < 14) stat(2 * i, k - 10);
          else stat(2 * i + 1, k - 14);
          SCHED0();
        }
      }
    }
    LAS char *tw, *te, *tr, *tc;
    float sm[2], sq[2], rs[2];
    const float sg = id.cb >= 8 ? -1.f : 1.f;
#pragma unroll
    for (int k = 0; k < 28; ++k) {
      if (k == 18) bar();
      if (k < 8) stat(18 + (k >> 2), k & 3);
      else if (k < 18) accP[(k - 8) >> 1][k & 1] = mfma(xr[18][k & 1], pb[0][(k - 8) >> 1], accP[(k - 8) >> 1][k & 1]);
      else accP[(k - 18) >> 1][k & 1] = mfma(xr[19][k & 1], pb[1][(k - 18) >> 1], accP[(k - 18) >> 1][k & 1]);
      if (k < 5) rdw(9, 1, k);
      if (k == 5) { lane = fresh_lane(); r = lane & 15; q = lane >> 4; PIN(r); PIN(q); }
      if (k == 6) { tw = L + T_ST + (w * 32 + r) * 4; te = L + T_EK + (w * 32 + r) * 16; PIN(tw); PIN(te); }
      if (k == 7) { const bool diag = (r >> 2) == q; tw = diag ? tw : L + T_PAD; te = diag ? te : L + T_PAD; PIN(tw); PIN(te); }
      if (k == 8) { tr = L + T_ST + (w * 32 + 4 * q) * 4; tc = L + T_CS + r * 4; PIN(tr); PIN(tc); }
      if (k == 9) { fB0 = L + BRING + lane * 16; fB3 = fB0 + 61440; PIN(fB0); PIN(fB3); }
      if (k == 11) { sm[0] = accS[0][0] * (-1.0f / D); sm[1] = accS[1][0] * (-1.0f / D); PIN(sm[0]); PIN(sm[1]); }
      if (k == 12 || k == 13) { sq[k - 12] = pick4(accQ[k - 12]) * (1.0f / D); PIN(sq[k - 12]); }
      if (k == 14 || k == 15) { const int t = k - 14; sq[t] = fmaxf(sq[t] - sm[t] * sm[t], 0.f) + 1e-5f; rs[t] = __builtin_amdgcn_rsqf(sq[t]); PIN(sq[t]); PIN(rs[t]); }
      if (k == 16 || k == 17) {
        const int t = k - 16;
        const float v = rs[t] * sg;
        *(LAS float*)(tw + t * 64) = v; *(LAS float*)(tw + 512 + t * 64) = v * sm[t];
      }
      if (k >= 18 && k < 24) rdg((k - 18) >> 1, k & 1);
      if (k == 24 || k == 25) {
        st[2 * k - 48] = *(LAS f32x4*)(tr + (k - 24) * 512); st[2 * k - 47] = *(LAS f32x4*)(tr + (k - 24) * 512 + 64);
      }
      if (k == 23) {
#pragma unroll
        for (int g = 0; g < 5; ++g) cs[g] = *(LAS float*)(tc + g * 64);
      }
      if (k == 26 || k == 27) {
        const int t = k - 26;
        *(LAS f32x4*)(te + t * 256) = f32x4{sm[t], -2.302208198f * rs[t], -0.1029432397f * rs[t] * rs[t] * rs[t], sq[t]};
      }
      SCHED0();
    }
  }
  LAS char* const xB0 = L + ARING + w * 4096 + r * 128 + ((q ^ (r >> 1)) & 7) * 16;
  LAS char* const xB1 = L + ARING + w * 4096 + ((r * 128 + ((q ^ (r >> 1)) & 7) * 16) ^ 64);
  const unsigned abBase = W_ABT + (unsigned)((((id.cb >> 3) * 4 + ((id.cb & 7) >> 1)) * 64 + id.rb * 4 + w) * 10 + (id.cb & 1) * 5) * 1024u + lane * 16;

  u32x4 abo;
  f32x4 accH[2][4];
  const unsigned hbase = W_H + (unsigned)(((id.rb * 2 + (w >> 1)) * 96 + id.cb * 6) * 2 + (w & 1)) * 1024u + lane * 16;
  float gv[8], uv[8], wv[8];
  f32x4 cgv, cuv, ek[2];
  auto h_tab = [&](int c) {
    cgv = *(LAS f32x4*)(L + T_CG + (c * 16 + 4 * q) * 4);
    cuv = *(LAS f32x4*)(L + T_CG + 384 + (c * 16 + 4 * q) * 4);
  };
  auto h_op = [&](int c, int n) {
    const int ph = n >> 3, x = n & 7, t = x >> 2, e = x & 3;
    if (ph == 0) { gv[x] = accH[c & 1][t][e] + ek[t][0] * cgv[e]; PIN(gv[x]); uv[x] = accH[c & 1][2 + t][e] + ek[t][0] * cuv[e]; PIN(uv[x]); }
    if (ph == 1) { wv[x] = gv[x] * (ek[t][1] + ek[t][2] * (gv[x] * gv[x])); PIN(wv[x]); }
    if (ph == 2) { wv[x] = __builtin_amdgcn_exp2f(wv[x]); PIN(wv[x]); uv[x] *= gv[x]; PIN(uv[x]); }
    if (ph == 3) { wv[x] = __builtin_amdgcn_rcpf(wv[x] * ek[t][3] + ek[t][3]); PIN(wv[x]); }
    if (ph == 4) {
      uv[x] *= wv[x]; PIN(uv[x]);
      if (x == 7) {
        u32x4 o = {pack2(uv[0], uv[1]), pack2(uv[2], uv[3]), pack2(uv[4], uv[5]), pack2(uv[6], uv[7])};
        __builtin_amdgcn_raw_buffer_store_b128(o, rws, hbase + c * 2048, 0, AUX_SC1);
      }
    }
  };
  const int wm = w >> 1, wn = w & 1;
  LAS char *aBase0, *aBase1, *yBase;
  bf16x8 xa0[4], xa1[4], yb0[5], yb1[5];
  sfor<0, 120>([&](auto gi) __attribute__((always_inline)) {
    constexpr int g = decltype(gi)::v, c = g / 20, s = g % 20;
    if (g == 110) {
      const int l2 = fresh_lane(), r2 = l2 & 15, oK0 = r2 * 128 + (((l2 >> 4) ^ (r2 >> 1)) & 7) * 16;
      aBase0 = L + ARING + wm * 8192 + oK0; aBase1 = L + ARING + wm * 8192 + (oK0 ^ 64); yBase = L + BRING + wn * 5120 + l2 * 16;
    }
    if (g % 10 == 8) {
      if (g == 38) wait_vm<0>();
      bar();
    }
#pragma unroll
    for (int k = 0; k < 4; ++k) {
      accH[c & 1][k] = mfma(wb[g & 3][k >> 1], xr[s][k & 1], s == 0 ? z4 : accH[c & 1][k]);
      bool rd = true;
      if (g >= 118) {
        const int n = (g - 118) * 4 + k;
        if (n < 4) xa0[n] = ldsr(aBase0 + 2 * 16384 + n * 2048); else yb0[n - 4] = ldsr(yBase + 2 * 20480 + (n - 4) * 1024);
      } else if (g % 10 == 8) rdg(g + 2 + (k >> 1), k & 1);
      else if (g % 10 != 7 && k < 2) rdg(g + 3, k);
      else rd = false;
      if (!rd) {
        const int n = s % 10 == 7 ? 2 * s + k : 2 * s + k - 2;
        if (c == 0) {
          if (n >= 2 && n < 27) {
            const int gg = (n - 2) / 5, ph = (n - 2) % 5;
            if (ph < 4) {
              const int t = ph >> 1, e = (ph & 1) * 2;
              unsigned pk = pack2(accP[gg][t][e] * st[t][e] + st[2 + t][e] * cs[gg], accP[gg][t][e + 1] * st[t][e + 1] + st[2 + t][e + 1] * cs[gg]);
              PIN(pk);
              abo[ph] = pk;
            } else __builtin_amdgcn_raw_buffer_store_b128(abo, rws, abBase + gg * 1024, 0, AUX_SC1);
          }
          if (n >= 8 && n < 32 && !(n & 1)) {
            const int m = (n - 8) >> 1;
            xr[12 + (m >> 1)][m & 1] = ldsr(((m & 2) ? xB1 : xB0) + ((6 + (m >> 2)) & 3) * 16384 + (m & 1) * 2048);
          }
          if (n == 34) ek[0] = *(LAS f32x4*)(L + T_EK + (w * 32 + r) * 16);
          if (n == 35) ek[1] = *(LAS f32x4*)(L + T_EK + (w * 32 + 16 + r) * 16);
        } else if (c < 5) h_op(c - 1, n);
        else if (n < 36) { h_op(4, n); if (n >= 32) h_op(4, n + 4); }
        if (n == (c == 0 ? 38 : 36)) h_tab(c);
      }
      SCHED0();
    }
  });

  f32x4 accY[5][4];
  auto y_stage = [&](int i, int h0) {
    LAS char* a0 = aBase0 + ((i + 2) & 3) * 16384; LAS char* a1 = aBase1 + ((i + 2) & 3) * 16384;
    LAS char* b = yBase + ((i + 2) & 3) * 20480;
    PIN(a0); PIN(a1); PIN(b);
    bar();
    int n = h0;
#pragma unroll
    for (int k = 0; k < 20; ++k) {
      accY[k >> 2][k & 3] = mfma(yb1[k >> 2], xa1[k & 3], accY[k >> 2][k & 3]);
      if (k < 4) xa0[k] = ldsr(a0 + k * 2048);
      else if (k < 9) yb0[k - 4] = ldsr(b + (k - 4) * 1024);
      else if (k < 11) yb1[k - 9] = ldsr(b + 10240 + (k - 9) * 1024);
      else if (k == 12) yb1[2] = ldsr(b + 10240 + 2048);
      else if (k == 16) yb1[3] = ldsr(b + 10240 + 3072);
      else if (n < 40) h_op(5, n++);
      SCHED0();
    }
#pragma unroll
    for (int k = 0; k < 20; ++k) {
      accY[k >> 2][k & 3] = mfma(yb0[k >> 2], xa0[k & 3], accY[k >> 2][k & 3]);
      if (k == 0) yb1[4] = ldsr(b + 10240 + 4096);
      else if (k < 5) xa1[k - 1] = ldsr(a1 + (k - 1) * 2048);
      else if (n < 40) h_op(5, n++);
      SCHED0();
    }
  };
#pragma unroll
  for (int k = 0; k < 20; ++k) {
    if (k == 0) yb0[4] = ldsr(yBase + 2 * 20480 + 4096);
    accY[k >> 2][k & 3] = mfma(yb0[k >> 2], xa0[k & 3], z4);
    if (k < 4) xa1[k] = ldsr(aBase1 + 2 * 16384 + k * 2048);
    else if (k < 9) yb1[k - 4] = ldsr(yBase + 2 * 20480 + 10240 + (k - 4) * 1024);
    else h_op(5, k - 9);
    SCHED0();
  }
  y_stage(1, 11); y_stage(2, 33);
#pragma nounroll
  for (int i = 3; i < 16; ++i) {
    if (i == 5) wait_vm<0>();
    y_stage(i, 40);
  }

  lane = fresh_lane(); r = lane & 15; q = lane >> 4;
  const int tile = id.tt * 4 + id.et;
  f32x4 accD[5][2];
#pragma unroll
  for (int g = 0; g < 5; ++g) { accD[g][0] = z4; accD[g][1] = z4; }
  LAS char* const hB = L + ARING + wm * 1024 + wn * 8192 + lane * 16 + wn * 8;
  LAS char* const dB = fB0 + wn * 10240;
  bf16x8 hh[2][2], dw[2][5];
  auto d_rd = [&](LAS char* a, LAS char* a2, LAS char* b, int ks, int m) {
    if (m < 2) hh[ks][m] = cat(*(volatile LAS bf16x4*)((m ? a2 : a) + ks * 4096), *(volatile LAS bf16x4*)((m ? a2 : a) + ks * 4096 + 2048));
    else dw[ks][m - 2] = ldsr(b + ks * 5120 + (m - 2) * 1024);
  };
  auto d_mm = [&](int ks, int k) { accD[k >> 1][k & 1] = mfma(dw[ks][k >> 1], hh[ks][k & 1], accD[k >> 1][k & 1]); };
  const unsigned pbase = W_P + (unsigned)(((tile * 4 + w) * 4 + id.kq) * 10) * 1024u + lane * 16;
  auto p_store = [&](int u) {
    u32x4 o[5];
#pragma unroll
    for (int g = 0; g < 5; ++g) {
      const f32x4 a = accY[g][2 * u], b = accY[g][2 * u + 1];
      o[g] = u32x4{pack2(a[0], a[1]), pack2(a[2], a[3]), pack2(b[0], b[1]), pack2(b[2], b[3])};
    }
    if (w != id.kq) {
#pragma unroll
      for (int g = 0; g < 5; ++g) __builtin_amdgcn_raw_buffer_store_b128(o[g], rws, pbase + (u * 5 + g) * 1024, 0, AUX_SC1);
    } else {
#pragma unroll
      for (int g = 0; g < 5; ++g) *(LAS u32x4*)(L + T_BIG + (u * 5 + g) * 1024 + lane * 16) = o[g];
    }
  };
  auto d_second = [&](LAS char* a, LAS char* a2, LAS char* b) {
#pragma unroll
    for (int k = 0; k < 10; ++k) {
      d_mm(0, k);
      if (k < 2) d_rd(a, a2, b, 1, k); else if (k < 4) d_rd(a, a2, b, 1, k + 3);
      SCHED0();
    }
  };
  auto d_stage = [&](int i) {
    LAS char* a = hB + ((i + 2) & 3) * 16384; LAS char* a2 = a + (8 - 16 * wn); LAS char* b = dB + ((i + 2) & 3) * 20480;
    PIN(a); PIN(a2); PIN(b);
    bar();
#pragma unroll
    for (int k = 0; k < 10; ++k) {
      d_mm(1, k);
      if (k < 7) d_rd(a, a2, b, 0, k); else d_rd(a, a2, b, 1, k - 5);
      SCHED0();
    }
    d_second(a, a2, b);
  };
  {
    LAS char* a = hB + 2 * 16384; LAS char* a2 = a + (8 - 16 * wn); LAS char* b = dB + 2 * 20480;
    PIN(a); PIN(a2); PIN(b);
    bar();
#pragma unroll
    for (int k = 0; k < 20; ++k) {
      accY[k >> 2][k & 3] = mfma(yb1[k >> 2], xa1[k & 3], accY[k >> 2][k & 3]);
      if (k < 7) d_rd(a, a2, b, 0, k); else if (k < 10) d_rd(a, a2, b, 1, k - 5);
      SCHED0();
    }
    d_second(a, a2, b);
  }
  p_store(0);
  p_store(1);
#pragma nounroll
  for (int i = 1; i < 12; ++i) {
    if (i == DRAIN_P - 38) wait_vm<0>();
    d_stage(i);
  }
#pragma unroll
  for (int k = 0; k < 10; ++k) {
    d_mm(1, k < 5 ? 2 * k + 1 : 2 * (9 - k));
    if (k >= 5) *(LAS f32x4*)(fB0 + ((w ^ 1) * 5 + k - 5) * 1024) = accD[k - 5][1];
    SCHED0();
  }
  LAS char* dp = fB0 + w * 5120;
  *(LAS f32x4*)(L + ARING + w * 2048 + 1024 + lane * 16) = accD[4][0];
  *(LAS f32x4*)(L + ARING + w * 2048 + lane * 16) = accD[3][0];
  LAS char* xp = L + X_TILE + (wm * 32 + 8 * (r >> 2) + 4 * wn + (r & 3)) * 160 + q * 8;
  PIN(xp); PIN(dp);
  bar();
  f32x4 dk[3], ys[3];
#pragma unroll
  for (int g = 0; g < 3; ++g) dk[g] = *(LAS f32x4*)(dp + g * 1024);
#pragma unroll
  for (int g = 0; g < 3; ++g) ys[g] = *(LAS f32x4*)(dp + 61440 + g * 1024);
  const f32x4 own[3] = {accD[0][0], accD[1][0], accD[2][0]};
  finish<3>(dk, own, ys, xp);
}

__device__ __forceinline__ void aux_tail(const Params& p, LAS char* L, int t, int lane, bf16x8 pickA) {
  const int po = (t >> 1) * 5120 + lane * 16;
  bf16x8 y[4][5];
#pragma unroll
  for (int g = 0; g < 5; ++g) {
    y[0][g] = ldsr(L + T_BIG + po + g * 1024);
    y[1][g] = ldsr(L + BRING + 2 * 20480 + po + g * 1024);
    y[2][g] = ldsr(L + BRING + 2 * 20480 + 10240 + po + g * 1024);
    y[3][g] = ldsr(L + ARING + 2 * 16384 + po + g * 1024);
  }
  SCHED0();
  LAS char* dp = L + ARING + t * 2048 + lane * 16;
  f32x4 ys[2];
#pragma unroll
  for (int g = 0; g < 5; ++g) {
    f32x4 sum = {0.f, 0.f, 0.f, 0.f};
#pragma unroll
    for (int k = 0; k < 4; ++k) sum = mfma(pickA, y[k][g], sum);
    if (g < 3) *(LAS f32x4*)(L + BRING + 61440 + (t * 5 + g) * 1024 + lane * 16) = sum;
    else ys[g - 3] = sum;
  }
  const int r = lane & 15;
  LAS char* xp = L + X_TILE + ((t >> 1) * 32 + 8 * (r >> 2) + 4 * (t & 1) + (r & 3)) * 160 + (lane >> 4) * 8 + 96;
  LAS char* dq = L + BRING + t * 5120 + 3072 + lane * 16;
  PIN(xp); PIN(dp); PIN(dq); PIN(ys[0]); PIN(ys[1]);
  wait_vm<0>();
  bar();
  if (t == 3) { STAMP(50); }
  f32x4 dk[2], own[2];
#pragma unroll
  for (int g = 0; g < 2; ++g) { dk[g] = *(LAS f32x4*)(dq + g * 1024); own[g] = *(LAS f32x4*)(dp + g * 1024); }
  finish<2>(dk, own, ys, xp);
}

__device__ __forceinline__ void st_flag(unsigned char* ws, unsigned off, u64 v, int lane) {
  if (lane == 0) __hip_atomic_store((GAS u64*)(ws + off), v, __ATOMIC_RELAXED, __HIP_MEMORY_SCOPE_AGENT);
}
__device__ __forceinline__ void wait_flag(const unsigned char* ws, unsigned off, u64 tag, u64 seen) {
  if (!__all(seen == tag)) {
#pragma nounroll
    for (int spin = 0; spin < (1 << 16); ++spin)
      if (__all(ld_flag(ws, off) == tag)) break;
  }
  __builtin_amdgcn_fence(__ATOMIC_ACQUIRE, "wavefront");
}
template <int N, class Fn>
__device__ __forceinline__ void wait_tags(u32x4 (&g)[N], unsigned tlo, unsigned thi, Fn&& reload) {
  auto good = [&]() {
    unsigned bad = 0;
#pragma unroll
    for (int k = 0; k < N; ++k) bad |= (g[k][1] ^ tlo) | (g[k][3] ^ thi);
    return __all(bad == 0);
  };
  if (!good()) {
#pragma nounroll
    for (int spin = 0; spin < (1 << 16); ++spin) {
      asm volatile("" ::: "memory");
      reload();
      if (good()) break;
    }
  }
}
struct First { cptr base; unsigned v0, v1, v2, v3; int kc; };
__device__ __forceinline__ void first_offsets(First& f, int w, int lane) {
  const bool isx = w == 4 || w == 5;
  const unsigned row = isx ? 8 * (lane >> 5) + ((lane >> 3) & 3) : lane >> 3, stride = w < 4 ? 3072 : 1280;
  const unsigned c0 = ((lane & 7) ^ (w < 4 ? 0 : lane >> 4)) * 16, c1 = c0 ^ (w < 4 ? 0 : 64);
  f.v0 = row * stride + c0;
  f.v1 = row * stride + c1 + ((isx ? 16 : 8) * stride - 1024);
  f.v2 = f.v0 + ((isx ? 4 : 16) * stride - 2048);
  f.v3 = f.v1 + ((isx ? 4 : 16) * stride - 2048);
}
#define SPIN(x) asm volatile("" : "+s"(x))
__device__ __forceinline__ void p1_tile(First& f, unsigned lb, int ld, int j, bool barrier) {
  cptr a = f.base + f.kc * 128, a2 = a + 32 * 1280;
  unsigned l = ld < 2 ? lb + ARING + (j & 3) * 16384 + ld * 8192 : lb + BRING + ((j & 3) ? (j & 3) * 20480 : 10240);
  SPIN(a); SPIN(a2); SPIN(l);
  if (barrier) bar();
  dma4(a, f.v0, f.v1, f.v2, f.v3, l);
  dma4(a2, f.v0, f.v1, f.v2, f.v3, l + 4096);
  if (ld == 2) dma2(a + 64 * 1280, f.v0, f.v1, l + 8192);
  f.kc = f.kc == 9 ? 0 : f.kc + 1;
}
__device__ __forceinline__ void loader(const Params& p, LAS char* L, unsigned lb, const Ids& id, int ld, int lane, First& f) {
  const int b = blockIdx.x;
  const unsigned vl = lane * 16;
  const unsigned c0 = ((lane & 7) ^ (lane >> 4)) * 16, c1 = c0 ^ 64;
  const int rl = 8 * (lane >> 5) + ((lane >> 3) & 3);
  const int rot = ROT;
  auto w1 = [&]() { if (ld == 2) wait_vm<10>(); else wait_vm<8>(); };
  auto w2 = [&]() { if (ld == 2) wait_vm<20>(); else wait_vm<16>(); };
  p1_tile(f, lb, ld, 2, false);
  constexpr int J0 = 3;
#pragma nounroll
  for (int j = J0; j < 10; ++j) {
    w2();
    p1_tile(f, lb, ld, j, true);
  }
  const unsigned vw0 = rl * 4096 + c0, vw1 = (rl + 16) * 4096 + c1 - 1024, vw2 = (rl + 4) * 4096 + c0 - 2048, vw3 = (rl + 20) * 4096 + c1 - 3072;
  const unsigned u0 = ld ? vl : vw0, u1 = ld ? vl : vw1, u2 = ld ? vl : vw2, u3 = ld ? vl : vw3;
  const cptr bw = (cptr)(id.kq < 2 ? p.wsr : p.wsi) + (id.tt * 128 * 4096 + (id.kq & 1) * 2048);
  const int g0 = ld ? 2 * ld - 2 : 4;
  const unsigned l0 = lb + BRING + g0 * 4096, l1 = ld ? l0 + 4096 : lb + ARING, ls1 = ld ? 20480 : 16384, l2 = lb + ARING + (ld + 1) * 4096;
  const int ro4 = id.et + (YROT ? id.tt : 0);
  auto yk = [&](int y) { return (y & ~3) | ((y + ro4) & 3); };
  {
    const cptr bg = p.ws + W_GU + (unsigned)id.cb * 245760u;
    const int ka[2] = {((g0 + rot) % 10) * 4096, ((g0 + rot + 5) % 10) * 4096}, kb[2] = {((g0 + 1 + rot) % 10) * 4096, ((g0 + rot + 6) % 10) * 4096};
    if (GB == 8) { w2(); bar(); }
#pragma nounroll
    for (int j = 10; j < 22; ++j) {
      const cptr so = bg + ((j - 10) >> 1) * 40960;
      const int n = (j - 16) * 3 + ld;
      cptr a0 = so + ((j & 1) ? ka[1] : ka[0]), a1 = so + ((j & 1) ? kb[1] : kb[0]), a2 = bw + (yk(n >> 2) * 128 + (n & 3) * 32 * 4096);
      unsigned la = l0 + (j & 3) * 20480, lw = lb + ARING + (((n >> 2) + 2) & 3) * 16384 + (n & 3) * 4096;
      SPIN(a0); SPIN(a1); SPIN(a2); SPIN(la); SPIN(lw);
      if (GB == 7 || j != 11) {
        if (j == 10) { if (GB == 8) wait_vm<8>(); else wait_vm<16>(); }
        else if (j == 11) wait_vm<12>();
        else if (j < 18) { if (ld == 0) wait_vm<8>(); else wait_vm<16>(); }
        else { if (ld == 0) wait_vm<16>(); else wait_vm<24>(); }
        bar();
      }
      dma4c(a0, vl, la);
      if (ld) dma4c(a1, vl, la + 4096);
      if (n >= 0 && n < 16) dma4(a2, vw0, vw1, vw2, vw3, lw);
    }
  }
  const int c80 = id.et * 2 + (id.kq & 1);
  const cptr bh = p.ws + W_H + (unsigned)(id.tt * 2 + (id.kq >> 1)) * 196608u;
  const cptr bab = p.ws + W_ABT + ((unsigned)(((id.kq >> 1) * 4 + id.et) * 64 + (id.kq & 1) * 32) * 10240u + g0 * 4096);
  const cptr bwd = p.ws + W_WDT + (unsigned)c80 * 245760u;
  const cptr y1 = ld ? bab + 4096 : bw, y2 = bw + (ld + 1) * 32 * 4096;
  const int ym1 = ld ? 20480 : 128;
  const cptr d0 = bwd + g0 * 4096, d1 = ld ? d0 + 4096 : bh, d2 = bh + (ld + 1) * 4096;
#pragma nounroll
  for (int i = 19; i < 35; ++i) {
    const int k = yk(i - 19), sl = (i + 3) & 3;
    cptr a0 = bab + k * 20480, a1 = y1 + k * ym1, a2 = y2 + k * 128;
    unsigned la = l0 + sl * 20480, lc = l1 + sl * ls1, ld2 = l2 + sl * 16384;
    SPIN(a0); SPIN(a1); SPIN(a2); SPIN(la); SPIN(lc); SPIN(ld2);
    if (i >= 25) wait_vm<24>(); else if (i == 24) wait_vm<16>(); else if (ld == 0) wait_vm<8>(); else wait_vm<16>();
    bar();
    dma4c(a0, vl, la);
    if (i >= 23 || ld) dma4s(a1, u0, u1, u2, u3, lc);
    if (i >= 23) dma4(a2, vw0, vw1, vw2, vw3, ld2);
  }
#pragma nounroll
  for (int i = 35; i < 47; ++i) {
    const int sl = (i + 3) & 3;
    const int k = DROT ? ((i - 35) & ~3) | ((i - 35 + id.et + id.tt) & 3) : i - 35;
    cptr a0 = d0 + k * 20480, a1 = d1 + k * ls1, a2 = d2 + k * 16384;
    unsigned la = l0 + sl * 20480, lc = l1 + sl * ls1, ld2 = l2 + sl * 16384;
    SPIN(a0); SPIN(a1); SPIN(a2); SPIN(la); SPIN(lc); SPIN(ld2);
    wait_vm<24>();
    bar();
    dma4c(a0, vl, la);
    dma4c(a1, vl, lc);
    dma4c(a2, vl, ld2);
  }
  cptr bp = p.ws + W_P + ((unsigned)(((id.tt * 4 + id.et) * 4 + id.kq) * 4 * 10) * 1024u + ((id.kq + 1 + ld) & 3) * 10240);
  unsigned l = lb + (ld == 0 ? ARING + 2 * 16384 : BRING + 2 * 20480 + (ld - 1) * 10240);
  SPIN(bp); SPIN(l);
  wait_vm<24>(); bar();
  dma4c(bp, vl, l); dma4c(bp + 4096, vl, l + 4096); dma2c(bp + 8192, vl, l + 8192);
}

__device__ __forceinline__ void helper(const Params& p, LAS char* L, unsigned lb, const Ids& id, int lane, u64 pc) {
  const int b = blockIdx.x;
  const __amdgpu_buffer_rsrc_t rws = __builtin_amdgcn_make_buffer_rsrc((void*)p.ws, 0, (int)W_END, 0x00020000);
  STAMP(60);
  __builtin_amdgcn_s_setprio(3);
  const u64 prev = ld_flag(p.ws, W_EPOCH);
  const int r8 = lane >> 3;
  u32x4 dv[10];
  const GAS char* ds = (const GAS char*)(b < 128 ? p.wdr : p.wdi) + (((b & 127) * 5 + (r8 < 5 ? r8 : 4)) * 1280 + (lane & 7) * 16);
#pragma unroll
  for (int i = 0; i < 10; ++i) dv[i] = *(const GAS u32x4*)(ds + i * 128);
  auto code_touch = [&]() {
    const unsigned off = (((b >> 3) % ((CODE_BYTES + 8191) / 8192)) * 64 + lane) * 128;
    touch((cptr)(pc & ~255ull), off < CODE_BYTES ? off : 0, lb + T_PAD);
  };
  const u64 tag = next_tag(prev);
  const unsigned tlo = (unsigned)tag, thi = (unsigned)(tag >> 32);
  {
    float t4[4] = {0.f, 0.f, 0.f, 0.f};
#pragma unroll
    for (int i = 0; i < 10; ++i)
#pragma unroll
      for (int e = 0; e < 4; ++e) t4[e] = dot_ones(dv[i][e], t4[e]);
    const unsigned sm = __builtin_bit_cast(unsigned, row8_sum((t4[0] + t4[1]) + (t4[2] + t4[3])));
    const u32x4 o = {sm, tlo, sm, thi};
    if ((lane & 7) == 0 && r8 < 5) __builtin_amdgcn_raw_buffer_store_b128(o, rws, W_CSD + (b * 8 + r8) * 16, 0, AUX_SC1);
  }
  const int pcl = (id.cb * 3) >> 1, tile = id.tt * 4 + id.et, c80 = id.et * 2 + (id.kq & 1);
  const int wk = b / 5, wc = b % 5;
  u64 f0 = 0, f1 = 0, f2 = 0, fe[4] = {0, 0, 0, 0};
  unsigned fo0 = 0, fo1 = 0, fo2 = 0;
  u32x4 gr[2], cq[10];
  const int l5 = (lane & 7) < 5 ? lane & 7 : 0, go = r8 * 5 + l5;
  const unsigned gro = W_CSD + ((id.cb * 16 + r8) * 8 + l5) * 16;
  auto ld_gr = [&]() {
    gr[0] = __builtin_amdgcn_raw_buffer_load_b128(rws, gro, 0, AUX_SC1);
    gr[1] = __builtin_amdgcn_raw_buffer_load_b128(rws, gro + 1024, 0, AUX_SC1);
  };
  auto ld_cq = [&]() {
#pragma unroll
    for (int k = 0; k < 10; ++k) cq[k] = __builtin_amdgcn_raw_buffer_load_b128(rws, W_CSP + ((k * 24 + pcl) * 32 + lane) * 16, 0, AUX_SC1);
  };
  sfor<0, 48>([&](auto ic) __attribute__((always_inline)) {
    constexpr int i = decltype(ic)::v;
    if constexpr (i == GB) wait_flag(p.ws, fo0, tag, f0);
    if constexpr (i == 35) { wait_flag(p.ws, fo1, tag, f1); wait_flag(p.ws, fo2, tag, f2); }
    if constexpr (i == 19 || i == 35 || i == 47) wait_flag(p.ws, fo0, tag, f0);
    bar();
    STAMP(i);
    if constexpr (i == 0) {
      if (b < 240 && lane < 32) {
        LAS float* cp = (LAS float*)(L + T_CP + lane * 4);
        const float u0 = (cp[0] + cp[64]) + (cp[128] + cp[192]), u1 = (cp[32] + cp[96]) + (cp[160] + cp[224]);
        const u32x4 o = {__builtin_bit_cast(unsigned, u0), tlo, __builtin_bit_cast(unsigned, u1), thi};
        __builtin_amdgcn_raw_buffer_store_b128(o, rws, W_CSP + (b * 32 + lane) * 16, 0, AUX_SC1);
      }
    }
    if constexpr (i == DRAIN_G) st_flag(p.ws, W_FG + b * FS, tag, lane);
    if constexpr (i == 4) ld_gr();
    if constexpr (i == GB - 2) {
      const int l20 = lane < 20 ? lane : 0;
      fo0 = W_FG + ((l20 >> 1) * 24 + pcl + (l20 & 1)) * FS;
      f0 = ld_flag(p.ws, fo0);
    }
    if constexpr (i == 8) {
      wait_tags<2>(gr, tlo, thi, ld_gr);
      *(LAS unsigned*)(L + T_CS + go * 4) = gr[0][0];
      *(LAS unsigned*)(L + T_CS + (40 + go) * 4) = gr[1][0];
    }
    if constexpr (i == 8) ld_cq();
    if constexpr (i == 12) {
      if (b < 240) {
        const cptr sn = (cptr)p.wdn + (wk * 32 * 1280 + wc * 256);
        const unsigned vn = (lane >> 4) * 1280 + (lane & 15) * 16;
        SELNT(NT_W, dma4)(sn, vn, vn + 4096, vn + 2 * 4096, vn + 3 * 4096, lb + T_BIG);
        SELNT(NT_W, dma4)(sn + 16 * 1280, vn, vn + 4096, vn + 2 * 4096, vn + 3 * 4096, lb + T_BIG + 4096);
      }
    }
    if constexpr (i == 10) wait_tags<10>(cq, tlo, thi, ld_cq);
    if constexpr (i == 11) {
#pragma unroll
      for (int m = 0; m < 2; ++m)
#pragma unroll
        for (int h = 0; h < 2; ++h) {
          const unsigned col = (lane >> 5) * 64 + h * 32 + (lane & 31) - (id.cb & 1) * 32;
          const float v = ((asf(cq[m][2 * h]) + asf(cq[2 + m][2 * h])) + (asf(cq[4 + m][2 * h]) + asf(cq[6 + m][2 * h]))) + asf(cq[8 + m][2 * h]);
          *(LAS float*)(col < 96 ? L + T_CG + (m * 96 + col) * 4 : L + T_PAD) = v;
        }
    }
    if constexpr (i >= 19 && i < 23) {
      if (b < 240) {
        LAS char* a = L + T_BIG + (4 * (lane >> 4) + ((lane & 15) >> 2)) * 256 + 8 * (lane & 3);
#pragma unroll
        for (int c = (i - 19) * 2; c < (i - 19) * 2 + 2; ++c) {
          const bf16x8 f = cat(ldstr(a + c * 32), ldstr(a + 4096 + c * 32));
          const int eg = wc * 8 + c, cc = eg / 5, g5 = eg - cc * 5;
          __builtin_amdgcn_raw_buffer_store_b128(__builtin_bit_cast(u32x4, f), rws, lane * 16 + W_WDT + ((cc * 48 + wk) * 5 + g5) * 1024, 0, AUX_SC1);
        }
      }
    }
    if constexpr (i == 14) st_flag(p.ws, W_FB + (id.rb * 16 + id.cb) * FS, tag, lane);
    if constexpr (i == 16) code_touch();
    if constexpr (i == 25) { wait_vm<0>(); st_flag(p.ws, W_FA + b * FS, tag, lane); }
    if constexpr (i == 17) {
      fo0 = W_FB + ((8 * (id.kq & 1) + (lane & 7)) * 16 + 8 * (id.kq >> 1) + 2 * id.et + ((lane >> 3) & 1)) * FS;
      f0 = ld_flag(p.ws, fo0);
    }
    if constexpr (i == 30) {
      fo1 = W_FA + ((lane < 48 ? lane : 0) * 5 + (c80 * 5) / 8) * FS;
      fo2 = W_FA + ((lane < 48 ? lane : 0) * 5 + (c80 * 5 + 4) / 8) * FS;
      f1 = ld_flag(p.ws, fo1); f2 = ld_flag(p.ws, fo2);
    }
    if constexpr (i == 27) st_flag(p.ws, W_FH + (id.rb * 16 + id.cb) * FS, tag, lane);
    if constexpr (i == 28) code_touch();
    if constexpr (i == 32) { fo0 = W_FH + (id.tt * 16 + (lane & 15)) * FS; f0 = ld_flag(p.ws, fo0); }
    if constexpr (i == 21) {
      if (b == 0) {
#pragma unroll
        for (int k = 0; k < 4; ++k) fe[k] = ld_flag(p.ws, W_FG + (k * 64 + lane) * FS);
      }
    }
    if constexpr (i == 24) {
      if (b == 0) {
#pragma unroll
        for (int k = 0; k < 4; ++k) wait_flag(p.ws, W_FG + (k * 64 + lane) * FS, tag, fe[k]);
        st_flag(p.ws, W_EPOCH, tag, lane);
      }
    }
    if constexpr (i == DRAIN_P) st_flag(p.ws, W_FP + (tile * 4 + id.kq) * FS, tag, lane);
    if constexpr (i == 45) { fo0 = W_FP + (tile * 4 + (lane & 3)) * FS; f0 = ld_flag(p.ws, fo0); }
  });
  __builtin_amdgcn_s_waitcnt(0x0F70);
}

__global__ __launch_bounds__(NTHR, 2) void fused_kernel(Params p) {
  extern __shared__ __attribute__((aligned(16))) char smem[];
  u64 pc;
  asm volatile("s_getpc_b64 %0" : "=s"(pc));
  LAS char* L = (LAS char*)smem;
  const unsigned lb = (unsigned)(size_t)L;
  int lane = threadIdx.x & 63;
  const int w = __builtin_amdgcn_readfirstlane(threadIdx.x >> 6);
  const int b = blockIdx.x, g = b & 7, rr = b >> 3;
  if (w == 7) {
    const unsigned off = ((rr % ((CODE_BYTES + 8191) / 8192)) * 64 + lane) * 128;
    touch((cptr)(pc & ~255ull), off < CODE_BYTES ? off : 0, lb + T_PAD);
  }
  Ids id;
  id.rb = (g >> 1) * 4 + (rr >> 3);
  id.cb = (g & 1) * 8 + (rr & 7);
  id.tt = (g & 1) * 8 + (rr & 7);
  id.et = rr >> 3;
  id.kq = g >> 1;
  First f;
  {
    const int pc48 = b % 48, pr = b / 48;
    const bool isx = w == 4 || w == 5;
    first_offsets(f, w, lane);
    f.base = w < 4 ? (cptr)(pc48 < 24 ? p.wg : p.wu) + ((pr * 128 + w * 32) * 3072 + (pc48 % 24) * 128)
                   : isx ? (cptr)p.x + (id.rb * 128 + (w - 4) * 64) * 1280 : (cptr)(id.cb < 8 ? p.wdr : p.wdi) + (id.cb & 7) * 80 * 1280;
    f.kc = ROT % 10;
    if (w < 4) {
      if (b < 240) SELNT(NT_G, dma4)(f.base, f.v0, f.v1, f.v2, f.v3, lb + BRING + (w ? w * 20480 + 10240 : 0));
    } else if (w < 7) {
      p1_tile(f, lb, w - 4, 0, false);
      if (PRO != 5 || w == 6) p1_tile(f, lb, w - 4, 1, false);
    }
  }
  if (w >= 4) {
    const int ld = w - 4;
    lane = fresh_lane();
    first_offsets(f, w, lane);
    if (ld < 3) loader(p, L, lb, id, ld, lane, f);
    else helper(p, L, lb, id, lane, pc);
    const cptr so = uni((cptr)p.x + ((id.tt * 128 + (id.kq >> 1) * 64) * 1280 + (id.et * 2 + (id.kq & 1)) * 160));
    const int n0 = ld * 64 + lane, n1 = n0 + 256, n2 = ld < 2 ? n0 + 512 : n0;
    unsigned x0 = (n0 / 10) * 1120 + n0 * 16, x1 = (n1 / 10) * 1120 + n1 * 16, x2 = (n2 / 10) * 1120 + n2 * 16;
    const unsigned one = (0x3f80u << ((lane & 1) * 16)) & -(unsigned)(((lane & 15) >> 2) == (lane >> 4));
    const int od = (ld & 1) * 2 + ((lane >> 1) & 1);
    u32x4 pa = {one & -(unsigned)(od == 0), one & -(unsigned)(od == 1), one & -(unsigned)(od == 2), one & -(unsigned)(od == 3)};
    PIN(x0); PIN(x1); PIN(x2); PIN(pa);
    wait_vm<22>();
    bar();
    if (w == 7) { STAMP(48); }
    dma1(so, x0, lb + X_TILE + ld * 1024); dma1(so, x1, lb + X_TILE + (ld + 4) * 1024); dma1(so, x2, lb + X_TILE + (ld < 2 ? ld + 8 : ld) * 1024);
    wait_vm<3>();
    bar();
    if (w == 7) { STAMP(49); }
    aux_tail(p, L, ld, lane, __builtin_bit_cast(bf16x8, pa));
  } else compute(p, L, id, w);
  const int tid = w * 64 + fresh_lane();
  const __amdgpu_buffer_rsrc_t ro = __builtin_amdgcn_make_buffer_rsrc((char*)p.out + ((id.tt * 128 + (id.kq >> 1) * 64) * 1280 + (id.et * 160 + (id.kq & 1) * 80) * 2), 0, 63 * 1280 + 160, 0x00020000);
  unsigned o0 = (tid / 10) * 1120 + tid * 16, o1 = ((tid + 512) / 10) * 1120 + (tid + 512) * 16;
  PIN(o0); PIN(o1);
  bar();
  const u32x4 v0 = *(LAS u32x4*)(L + X_TILE + tid * 16);
  if (w < 2) {
    const u32x4 v1 = *(LAS u32x4*)(L + X_TILE + 8192 + tid * 16);
    __builtin_amdgcn_raw_buffer_store_b128(v1, ro, o1, 0, AUX_SC1);
  }
  __builtin_amdgcn_raw_buffer_store_b128(v0, ro, o0, 0, AUX_SC1);
}
}

extern "C" void kernel_launch(void* const* d_in, const int* in_sizes, int n_in, void* d_out, int out_size, void* d_ws, size_t ws_size,
                              hipStream_t stream) {
  Params p;
  p.x = (const u16*)d_in[0];
  p.wsr = (const u16*)d_in[1];
  p.wsi = (const u16*)d_in[2];
  p.wdr = (const u16*)d_in[3];
  p.wdi = (const u16*)d_in[4];
  p.wg = (const u16*)d_in[6];
  p.wu = (const u16*)d_in[7];
  p.wdn = (const u16*)d_in[8];
  p.out = (u16*)d_out;
  p.ws = (unsigned char*)d_ws;
  (void)hipFuncSetAttribute((const void*)fused_kernel, hipFuncAttributeMaxDynamicSharedMemorySize, LDS_BYTES);
  fused_kernel<<<256, NTHR, LDS_BYTES, stream>>>(p);
}
```

```cpp
#include <hip/hip_runtime.h>
#include <hip/hip_bf16.h>

typedef unsigned short u16;
typedef unsigned long long u64;
typedef short bf16x8 __attribute__((ext_vector_type(8)));
typedef short bf16x4 __attribute__((ext_vector_type(4)));
typedef float f32x4 __attribute__((ext_vector_type(4)));
typedef float f32x2 __attribute__((ext_vector_type(2)));
typedef unsigned u32x4 __attribute__((ext_vector_type(4)));
typedef unsigned u32x2 __attribute__((ext_vector_type(2)));
typedef int i32x4 __attribute__((ext_vector_type(4)));
typedef __bf16 bf2v __attribute__((ext_vector_type(2)));
#define LAS __attribute__((address_space(3)))
#define GAS __attribute__((address_space(1)))

namespace {
constexpr int S = 2048, D = 640, F = 1536;
constexpr int NTHR = 512;
constexpr int ARING = 0;
constexpr int BRING = 65536;
constexpr int TAB = 147456;
constexpr int T_ST = TAB;
constexpr int T_CS = TAB + 1024;
constexpr int T_CP = TAB + 1344;
constexpr int T_CG = TAB + 2368;
constexpr int T_PAD = TAB + 3328;
constexpr int T_EK = TAB + 12288;
constexpr int T_BIG = TAB + 4096;
constexpr int X_TILE = ARING + 3 * 16384;
constexpr int LDS_BYTES = 163840;
constexpr unsigned W_EPOCH = 0;
constexpr unsigned FS = 128;
constexpr unsigned W_FA = 4096, W_FB = W_FA + 256 * FS, W_FH = W_FB + 256 * FS, W_FP = W_FH + 256 * FS, W_FG = W_FP + 256 * FS;
constexpr unsigned W_CSD = W_FG + 256 * FS;
constexpr unsigned W_CSP = W_CSD + 256 * 128;
constexpr unsigned W_ABT = 524288;
static_assert(W_CSP + 240 * 512 <= W_ABT, "workspace map");
constexpr unsigned W_H = W_ABT + 2u * 4 * 64 * 10 * 1024;
constexpr unsigned W_WDT = W_H + 32u * 96 * 2 * 1024;
constexpr unsigned W_GU = W_WDT + 8u * 48 * 5 * 1024;
constexpr unsigned W_P = W_GU + 16u * 20 * 12 * 1024;
constexpr unsigned W_END = W_P + 256u * 4 * 10 * 1024;
constexpr unsigned W_STAMP = W_END;
constexpr int AUX_SC1 = 16;
#define DRAIN_G 3
#define PRO 3
#define DRAIN_P 41
#define CODE_BYTES 32000
#define WT0 -1
#define WTS 2
#define YROT 1
#define DROT 1
#define YSPLIT 2
#define MAP 1
#define ROT (3 * (b >> 6) + ((b >> 3) & 7))
#define NT_W 0
#define NT_G 0
#define SELNT(c, f) ((c) ? f##nt : f)
#define GB 7

struct Params {
  const u16 *x, *wsr, *wsi, *wdr, *wdi, *wg, *wu, *wdn;
  u16* out;
  unsigned char* ws;
};

typedef const unsigned char* cptr;
__device__ __forceinline__ cptr uni(cptr a) {
  const u64 v = (u64)a;
  return (cptr)(((u64)(unsigned)__builtin_amdgcn_readfirstlane((int)(v >> 32)) << 32) | (unsigned)__builtin_amdgcn_readfirstlane((int)v));
}
#define DMA_BODY(POL) \
__device__ __forceinline__ void dma2##POL(cptr base, unsigned v0, unsigned v1, unsigned lds) { \
  asm volatile("s_mov_b32 m0, %3\n\ts_nop 0\n\tglobal_load_lds_dwordx4 %0, %2 " #POL "\n\tglobal_load_lds_dwordx4 %1, %2 offset:1024 " #POL \
               ::"v"(v0), "v"(v1), "s"(base), "s"(lds) : "memory"); \
} \
__device__ __forceinline__ void dma3##POL(cptr base, unsigned v0, unsigned v1, unsigned v2, unsigned lds) { \
  asm volatile("s_mov_b32 m0, %4\n\ts_nop 0\n\tglobal_load_lds_dwordx4 %0, %3 " #POL "\n\tglobal_load_lds_dwordx4 %1, %3 offset:1024 " #POL "\n\t" \
               "global_load_lds_dwordx4 %2, %3 offset:2048 " #POL ::"v"(v0), "v"(v1), "v"(v2), "s"(base), "s"(lds) : "memory"); \
} \
__device__ __forceinline__ void dma4##POL(cptr base, unsigned v0, unsigned v1, unsigned v2, unsigned v3, unsigned lds) { \
  asm volatile("s_mov_b32 m0, %5\n\ts_nop 0\n\tglobal_load_lds_dwordx4 %0, %4 " #POL "\n\tglobal_load_lds_dwordx4 %1, %4 offset:1024 " #POL "\n\t" \
               "global_load_lds_dwordx4 %2, %4 offset:2048 " #POL "\n\tglobal_load_lds_dwordx4 %3, %4 offset:3072 " #POL \
               ::"v"(v0), "v"(v1), "v"(v2), "v"(v3), "s"(base), "s"(lds) : "memory"); \
}
DMA_BODY()
DMA_BODY(nt)
DMA_BODY(sc1)
#define dma4s dma4sc1
__device__ __forceinline__ void dma1(cptr base, unsigned v, unsigned lds) {
  asm volatile("s_mov_b32 m0, %2\n\ts_nop 0\n\tglobal_load_lds_dwordx4 %0, %1" ::"v"(v), "s"(base), "s"(lds) : "memory");
}
__device__ __forceinline__ void dma4c(cptr base, unsigned v, unsigned lds) {
  asm volatile("s_mov_b32 m0, %2\n\ts_nop 0\n\tglobal_load_lds_dwordx4 %0, %1 sc1\n\tglobal_load_lds_dwordx4 %0, %1 offset:1024 sc1\n\t"
               "global_load_lds_dwordx4 %0, %1 offset:2048 sc1\n\tglobal_load_lds_dwordx4 %0, %1 offset:3072 sc1"
               ::"v"(v), "s"(base), "s"(lds) : "memory");
}
__device__ __forceinline__ void dma2c(cptr base, unsigned v, unsigned lds) {
  asm volatile("s_mov_b32 m0, %2\n\ts_nop 0\n\tglobal_load_lds_dwordx4 %0, %1 sc1\n\tglobal_load_lds_dwordx4 %0, %1 offset:1024 sc1"
               ::"v"(v), "s"(base), "s"(lds) : "memory");
}
__device__ __forceinline__ void touch(cptr base, unsigned voff, unsigned pad) {
  asm volatile("s_mov_b32 m0, %2\n\ts_nop 0\n\tglobal_load_lds_dword %0, %1" ::"v"(voff), "s"(base), "s"(pad) : "memory");
}
template <int N> __device__ __forceinline__ void wait_vm() { asm volatile("s_waitcnt vmcnt(%0)" ::"i"(N) : "memory"); }
#define SCHED0() __builtin_amdgcn_sched_barrier(0)
__device__ __forceinline__ void bar() {
  SCHED0();
  __builtin_amdgcn_s_waitcnt(0xC07F);
  asm volatile("s_barrier" ::: "memory");
  SCHED0();
}
__device__ __forceinline__ f32x4 mfma(bf16x8 a, bf16x8 b, f32x4 c) { return __builtin_amdgcn_mfma_f32_16x16x32_bf16(a, b, c, 0, 0, 0); }
__device__ __forceinline__ bf16x8 ldsr(LAS char* p) { return *(LAS bf16x8*)p; }
__device__ __forceinline__ bf16x4 ldstr(LAS char* p) { return __builtin_amdgcn_ds_read_tr16_b64_v4i16((LAS bf16x4*)p); }
__device__ __forceinline__ bf16x8 cat(bf16x4 a, bf16x4 b) { return __builtin_shufflevector(a, b, 0, 1, 2, 3, 4, 5, 6, 7); }
__device__ __forceinline__ unsigned pack2(float a, float b) {
  f32x2 v = {a, b};
  bf2v r = __builtin_convertvector(v, bf2v);
  return __builtin_bit_cast(unsigned, r);
}
__device__ __forceinline__ float asf(unsigned v) { return __builtin_bit_cast(float, v); }
__device__ __forceinline__ float bflo(unsigned v) { return __builtin_bit_cast(float, v << 16); }
__device__ __forceinline__ float bfhi(unsigned v) { return __builtin_bit_cast(float, v & 0xffff0000u); }
template <int CTRL> __device__ __forceinline__ float dpp_add(float v) {
  return v + __builtin_bit_cast(float, __builtin_amdgcn_update_dpp(0, __builtin_bit_cast(int, v), CTRL, 0xf, 0xf, true));
}
__device__ __forceinline__ float row8_sum(float v) {
  v = dpp_add<0xB1>(v); v = dpp_add<0x4E>(v); v = dpp_add<0x141>(v);
  return v;
}
__device__ __forceinline__ float dot_ones(unsigned v, float acc) {
  return __builtin_amdgcn_fdot2_f32_bf16(__builtin_bit_cast(bf2v, v), __builtin_bit_cast(bf2v, 0x3f803f80u), acc, false);
}
__device__ __forceinline__ u64 ld_flag(const unsigned char* ws, unsigned off) {
  return __hip_atomic_load((GAS u64*)(ws + off), __ATOMIC_RELAXED, __HIP_MEMORY_SCOPE_AGENT);
}
__device__ __forceinline__ u64 next_tag(u64 prev) { return prev * 6364136223846793005ull + 1442695040888963407ull; }

#define STAMP(i) do { } while (0)

struct Ids {
  int rb, cb, tt, et, kq;
};

template <int I> struct IC { static constexpr int v = I; };
template <int I, int N, class Fn> __device__ __forceinline__ void sfor(Fn&& f) {
  if constexpr (I < N) { f(IC<I>{}); sfor<I + 1, N>(f); }
}
__device__ __forceinline__ int fresh_lane() {
  int l;
  asm volatile("v_mbcnt_lo_u32_b32 %0, -1, 0\n\tv_mbcnt_hi_u32_b32 %0, -1, %0" : "=v"(l));
  return l;
}
__device__ __forceinline__ float pick4(f32x4 v) {
  float o, t;
  asm volatile("v_cndmask_b32_e64 %0, %2, %3, %6\n\tv_cndmask_b32_e64 %1, %4, %5, %6\n\tv_cndmask_b32_e64 %0, %0, %1, %7"
               : "=&v"(o), "=&v"(t) : "v"(v[0]), "v"(v[1]), "v"(v[2]), "v"(v[3]), "s"(0xAAAAAAAAAAAAAAAAull), "s"(0xCCCCCCCCCCCCCCCCull));
  return o;
}
#define PIN(x) asm volatile("" : "+v"(x))
template <int N>
__device__ __forceinline__ void finish(f32x4 (&dk)[N], const f32x4 (&d1)[N], f32x4 (&ys)[N], LAS char* xp) {
  f32x4 ex[N];
  u32x2 xv[N];
#pragma unroll
  for (int g = 0; g < N; ++g) xv[g] = *(volatile LAS u32x2*)(xp + g * 32);
  SCHED0();
#pragma unroll
  for (int g = 0; g < N; ++g) { dk[g] += d1[g]; ex[g] = dk[g] * -1.4426950409f; }
  SCHED0();
#pragma unroll
  for (int g = 0; g < N; ++g) {
#pragma unroll
    for (int e = 0; e < 4; ++e) ex[g][e] = __builtin_amdgcn_exp2f(ex[g][e]);
    dk[g] *= ys[g];
  }
  SCHED0();
#pragma unroll
  for (int g = 0; g < N; ++g) ex[g] += 1.f;
  SCHED0();
#pragma unroll
  for (int g = 0; g < N; ++g) {
#pragma unroll
    for (int e = 0; e < 4; ++e) ex[g][e] = __builtin_amdgcn_rcpf(ex[g][e]);
    ys[g] = f32x4{bflo(xv[g][0]), bfhi(xv[g][0]), bflo(xv[g][1]), bfhi(xv[g][1])};
  }
  SCHED0();
#pragma unroll
  for (int g = 0; g < N; ++g) {
    const f32x4 o = dk[g] * ex[g] + ys[g];
    *(volatile LAS u32x2*)(xp + g * 32) = u32x2{pack2(o[0], o[1]), pack2(o[2], o[3])};
  }
}

__device__ __forceinline__ unsigned out_off(int n) { unsigned o = (n / 10) * 1120 + n * 16; PIN(o); return o; }
__device__ __forceinline__ void compute(const Params& p, LAS char* L, const Ids& id, int w, unsigned& o0, unsigned& o1) {
  int lane = fresh_lane(), r = lane & 15, q = lane >> 4;
  const __amdgpu_buffer_rsrc_t rws = __builtin_amdgcn_make_buffer_rsrc((void*)p.ws, 0, (int)W_END, 0x00020000);
  const f32x4 z4 = {0.f, 0.f, 0.f, 0.f};

  unsigned sAb = W_ABT + (unsigned)((((id.cb >> 3) * 4 + ((id.cb & 7) >> 1)) * 64 + id.rb * 4 + w) * 10 + (id.cb & 1) * 5) * 1024u;
  unsigned sH = W_H + (unsigned)(((id.rb * 2 + (w >> 1)) * 96 + id.cb * 6) * 2 + (w & 1)) * 1024u;
  unsigned sP = W_P + (unsigned)((((id.tt * 4 + id.et) * 4 + w) * 4 + id.kq) * 10) * 1024u;
  asm volatile("" : "+s"(sAb), "+s"(sH), "+s"(sP));
  const bf16x8 ones = {0x3f80, 0x3f80, 0x3f80, 0x3f80, 0x3f80, 0x3f80, 0x3f80, 0x3f80};
  if (blockIdx.x < 240) {
    const int pc = blockIdx.x % 48, pr = blockIdx.x / 48;
    LAS char* a = L + BRING + (w ? w * 20480 + 10240 : 0) + (8 * q + (r >> 2)) * 128 + 8 * (r & 3);
    const unsigned go = lane * 16 + W_GU + (((pc % 24) * 4 * 20 + pr * 4 + w) * 2 + pc / 24) * 1024;
    wait_vm<0>();
    bf16x8 f[4];
#pragma unroll
    for (int c = 0; c < 4; ++c) f[c] = cat(ldstr(a + c * 32), ldstr(a + 512 + c * 32));
    f32x4 acc[4];
#pragma unroll
    for (int c = 0; c < 4; ++c) acc[c] = mfma(ones, f[c], z4);
    SCHED0();
#pragma unroll
    for (int c = 0; c < 4; ++c) __builtin_amdgcn_raw_buffer_store_b128(__builtin_bit_cast(u32x4, f[c]), rws, go + c * 40960, 0, AUX_SC1);
    SCHED0();
    float v01 = (q & 1) ? acc[1][0] : acc[0][0], v23 = (q & 1) ? acc[3][0] : acc[2][0];
    PIN(v01); PIN(v23);
    *(LAS float*)(L + T_CP + (w * 64 + lane) * 4) = (q & 2) ? v23 : v01;
  }

  bf16x8 xr[20][2];
  LAS char *fB0, *fB3;
  bf16x8 wb[4][2];
  auto rdg = [&](int g, int m) {
    const int sl = (10 + g / 10) & 3, n = (g % 10) * 2 + m;
    wb[g & 3][m] = sl == 3 ? ldsr(fB3 + n * 1024) : ldsr(fB0 + sl * 20480 + n * 1024);
  };
  f32x4 accP[5][2], st[4];
  float cs[5];
  {
    f32x4 accS[2], accQ[2];
    bf16x8 pb[2][5];
    const int oK0 = r * 128 + ((q ^ (r >> 1)) & 7) * 16, oK1 = oK0 ^ 64;
    LAS char* xA[2] = {L + ARING + w * 4096 + oK0, L + ARING + w * 4096 + oK1};
    LAS char* pB[2] = {L + BRING + 10240 + oK0, L + BRING + 10240 + oK1};
    PIN(xA[0]); PIN(xA[1]); PIN(pB[0]); PIN(pB[1]);
    auto rdx = [&](int i, int ks, int t) { xr[2 * i + ks][t] = ldsr(xA[ks] + (i & 3) * 16384 + t * 2048); };
    auto rdw = [&](int i, int ks, int g) { pb[ks][g] = ldsr(pB[ks] + ((i & 3) ? (i & 3) * 20480 - 10240 : 0) + g * 2048); };
    auto stat = [&](int s, int k) {
      if (k < 2) accS[k] = mfma(ones, xr[s][k], s == 0 ? z4 : accS[k]);
      else accQ[k - 2] = mfma(xr[s][k - 2], xr[s][k - 2], s == 0 ? z4 : accQ[k - 2]);
    };
#pragma unroll
    for (int i = 0; i < 10; ++i) {
      if (i == DRAIN_G) wait_vm<0>();
      bar();
#pragma unroll
      for (int k = 0; k < 10; ++k) {
        if (i > 0) accP[k >> 1][k & 1] = mfma(xr[2 * i - 1][k & 1], pb[1][k >> 1], accP[k >> 1][k & 1]);
        if (k & 1) rdw(i, 0, k >> 1);
        else if (k < 8) rdx(i, k >> 2, (k >> 1) & 1);
        SCHED0();
      }
      if (i < 9) {
#pragma unroll
        for (int k = 0; k < 18; ++k) {
          if (k < 2) stat(2 * i, k);
          else if (k < 12) {
            accP[(k - 2) >> 1][k & 1] = mfma(xr[2 * i][k & 1], pb[0][(k - 2) >> 1], i == 0 ? z4 : accP[(k - 2) >> 1][k & 1]);
            if (k & 1) rdw(i, 1, (k - 2) >> 1);
          } else if (k < 14) stat(2 * i, k - 10);
          else stat(2 * i + 1, k - 14);
          SCHED0();
        }
      }
    }
    LAS char *tw, *te, *tr, *tc;
    float sm[2], sq[2], rs[2];
    const float sg = id.cb >= 8 ? -1.f : 1.f;
#pragma unroll
    for (int k = 0; k < 28; ++k) {
      if (k == 18) bar();
      if (k < 8) stat(18 + (k >> 2), k & 3);
      else if (k < 18) accP[(k - 8) >> 1][k & 1] = mfma(xr[18][k & 1], pb[0][(k - 8) >> 1], accP[(k - 8) >> 1][k & 1]);
      else accP[(k - 18) >> 1][k & 1] = mfma(xr[19][k & 1], pb[1][(k - 18) >> 1], accP[(k - 18) >> 1][k & 1]);
      if (k < 5) rdw(9, 1, k);
      if (k == 5) { lane = fresh_lane(); r = lane & 15; q = lane >> 4; PIN(r); PIN(q); }
      if (k == 6) { tw = L + T_ST + (w * 32 + r) * 4; te = L + T_EK + (w * 32 + r) * 16; PIN(tw); PIN(te); }
      if (k == 7) { const bool diag = (r >> 2) == q; tw = diag ? tw : L + T_PAD; te = diag ? te : L + T_PAD; PIN(tw); PIN(te); }
      if (k == 8) { tr = L + T_ST + (w * 32 + 4 * q) * 4; tc = L + T_CS + r * 4; PIN(tr); PIN(tc); }
      if (k == 9) { fB0 = L + BRING + lane * 16; fB3 = fB0 + 61440; PIN(fB0); PIN(fB3); }
      if (k == 11) { sm[0] = accS[0][0] * (-1.0f / D); sm[1] = accS[1][0] * (-1.0f / D); PIN(sm[0]); PIN(sm[1]); }
      if (k == 12 || k == 13) { sq[k - 12] = pick4(accQ[k - 12]) * (1.0f / D); PIN(sq[k - 12]); }
      if (k == 14 || k == 15) { const int t = k - 14; sq[t] = fmaxf(sq[t] - sm[t] * sm[t], 0.f) + 1e-5f; rs[t] = __builtin_amdgcn_rsqf(sq[t]); PIN(sq[t]); PIN(rs[t]); }
      if (k == 16 || k == 17) {
        const int t = k - 16;
        const float v = rs[t] * sg;
        *(LAS float*)(tw + t * 64) = v; *(LAS float*)(tw + 512 + t * 64) = v * sm[t];
      }
      if (k >= 18 && k < 24) rdg((k - 18) >> 1, k & 1);
      if (k == 24 || k == 25) {
        st[2 * k - 48] = *(LAS f32x4*)(tr + (k - 24) * 512); st[2 * k - 47] = *(LAS f32x4*)(tr + (k - 24) * 512 + 64);
      }
      if (k == 23) {
#pragma unroll
        for (int g = 0; g < 5; ++g) cs[g] = *(LAS float*)(tc + g * 64);
      }
      if (k == 26 || k == 27) {
        const int t = k - 26;
        *(LAS f32x4*)(te + t * 256) = f32x4{sm[t], -2.302208198f * rs[t], -0.1029432397f * rs[t] * rs[t] * rs[t], sq[t]};
      }
      SCHED0();
    }
  }
  LAS char *xB0, *xB1;
  unsigned abBase, hbase;

  u32x4 abo;
  f32x4 accH[2][4];
  float gv[8], uv[8], wv[8];
  f32x4 cgv, cuv, ek[2];
  auto h_tab = [&](int c) {
    cgv = *(LAS f32x4*)(L + T_CG + (c * 16 + 4 * q) * 4);
    cuv = *(LAS f32x4*)(L + T_CG + 384 + (c * 16 + 4 * q) * 4);
  };
  auto h_op = [&](int c, int n) {
    const int ph = n >> 3, x = n & 7, t = x >> 2, e = x & 3;
    if (ph == 0) { gv[x] = accH[c & 1][t][e] + ek[t][0] * cgv[e]; PIN(gv[x]); uv[x] = accH[c & 1][2 + t][e] + ek[t][0] * cuv[e]; PIN(uv[x]); }
    if (ph == 1) { wv[x] = gv[x] * (ek[t][1] + ek[t][2] * (gv[x] * gv[x])); PIN(wv[x]); }
    if (ph == 2) { wv[x] = __builtin_amdgcn_exp2f(wv[x]); PIN(wv[x]); uv[x] *= gv[x]; PIN(uv[x]); }
    if (ph == 3) { wv[x] = __builtin_amdgcn_rcpf(wv[x] * ek[t][3] + ek[t][3]); PIN(wv[x]); }
    if (ph == 4) {
      uv[x] *= wv[x]; PIN(uv[x]);
      if (x == 7) {
        u32x4 o = {pack2(uv[0], uv[1]), pack2(uv[2], uv[3]), pack2(uv[4], uv[5]), pack2(uv[6], uv[7])};
        __builtin_amdgcn_raw_buffer_store_b128(o, rws, hbase + c * 2048, 0, AUX_SC1);
      }
    }
  };
  const int wm = w >> 1, wn = w & 1;
  LAS char *aBase0, *aBase1, *yBase;
  bf16x8 xa0[4], xa1[4], yb0[5], yb1[5];
  sfor<0, 120>([&](auto gi) __attribute__((always_inline)) {
    constexpr int g = decltype(gi)::v, c = g / 20, s = g % 20;
    if (g == 110) {
      const int l2 = fresh_lane(), r2 = l2 & 15, oK0 = r2 * 128 + (((l2 >> 4) ^ (r2 >> 1)) & 7) * 16;
      aBase0 = L + ARING + wm * 8192 + oK0; aBase1 = L + ARING + wm * 8192 + (oK0 ^ 64); yBase = L + BRING + wn * 5120 + l2 * 16;
    }
    if (g % 10 == 8) {
      if (g == 38) wait_vm<0>();
      bar();
    }
#pragma unroll
    for (int k = 0; k < 4; ++k) {
      accH[c & 1][k] = mfma(wb[g & 3][k >> 1], xr[s][k & 1], s == 0 ? z4 : accH[c & 1][k]);
      bool rd = true;
      if (g >= 118) {
        const int n = (g - 118) * 4 + k;
        if (n < 4) xa0[n] = ldsr(aBase0 + 2 * 16384 + n * 2048); else yb0[n - 4] = ldsr(yBase + 2 * 20480 + (n - 4) * 1024);
      } else if (g % 10 == 8) rdg(g + 2 + (k >> 1), k & 1);
      else if (g % 10 != 7 && k < 2) rdg(g + 3, k);
      else rd = false;
      if (!rd) {
        const int n = s % 10 == 7 ? 2 * s + k : 2 * s + k - 2;
        if (c == 0) {
          if (n == 0) { abBase = sAb + lane * 16; hbase = sH + lane * 16; PIN(abBase); PIN(hbase); }
          if (n == 1) { xB0 = L + ARING + w * 4096 + (r * 128 + ((q ^ (r >> 1)) & 7) * 16); PIN(xB0); }
          if (n == 6) { xB1 = (LAS char*)((size_t)xB0 ^ 64); PIN(xB1); }
          if (n >= 2 && n < 27) {
            const int gg = (n - 2) / 5, ph = (n - 2) % 5;
            if (ph < 4) {
              const int t = ph >> 1, e = (ph & 1) * 2;
              unsigned pk = pack2(accP[gg][t][e] * st[t][e] + st[2 + t][e] * cs[gg], accP[gg][t][e + 1] * st[t][e + 1] + st[2 + t][e + 1] * cs[gg]);
              PIN(pk);
              abo[ph] = pk;
            } else __builtin_amdgcn_raw_buffer_store_b128(abo, rws, abBase + gg * 1024, 0, AUX_SC1);
          }
          if (n >= 8 && n < 32 && !(n & 1)) {
            const int m = (n - 8) >> 1;
            xr[12 + (m >> 1)][m & 1] = ldsr(((m & 2) ? xB1 : xB0) + ((6 + (m >> 2)) & 3) * 16384 + (m & 1) * 2048);
          }
          if (n == 34) ek[0] = *(LAS f32x4*)(L + T_EK + (w * 32 + r) * 16);
          if (n == 35) ek[1] = *(LAS f32x4*)(L + T_EK + (w * 32 + 16 + r) * 16);
        } else if (c < 5) h_op(c - 1, n);
        else if (n < 36) { h_op(4, n); if (n >= 32) h_op(4, n + 4); }
        if (n == (c == 0 ? 38 : 36)) h_tab(c);
      }
      SCHED0();
    }
  });

  f32x4 accY[5][4];
  auto y_stage = [&](int i, int h0) {
    LAS char* a0 = aBase0 + ((i + 2) & 3) * 16384; LAS char* a1 = aBase1 + ((i + 2) & 3) * 16384;
    LAS char* b = yBase + ((i + 2) & 3) * 20480;
    PIN(a0); PIN(a1); PIN(b);
    bar();
    int n = h0;
#pragma unroll
    for (int k = 0; k < 20; ++k) {
      accY[k >> 2][k & 3] = mfma(yb1[k >> 2], xa1[k & 3], accY[k >> 2][k & 3]);
      if (k < 4) xa0[k] = ldsr(a0 + k * 2048);
      else if (k < 9) yb0[k - 4] = ldsr(b + (k - 4) * 1024);
      else if (k < 11) yb1[k - 9] = ldsr(b + 10240 + (k - 9) * 1024);
      else if (k == 12) yb1[2] = ldsr(b + 10240 + 2048);
      else if (k == 16) yb1[3] = ldsr(b + 10240 + 3072);
      else if (n < 40) h_op(5, n++);
      SCHED0();
    }
#pragma unroll
    for (int k = 0; k < 20; ++k) {
      accY[k >> 2][k & 3] = mfma(yb0[k >> 2], xa0[k & 3], accY[k >> 2][k & 3]);
      if (k == 0) yb1[4] = ldsr(b + 10240 + 4096);
      else if (k < 5) xa1[k - 1] = ldsr(a1 + (k - 1) * 2048);
      else if (n < 40) h_op(5, n++);
      SCHED0();
    }
  };
#pragma unroll
  for (int k = 0; k < 20; ++k) {
    if (k == 0) yb0[4] = ldsr(yBase + 2 * 20480 + 4096);
    accY[k >> 2][k & 3] = mfma(yb0[k >> 2], xa0[k & 3], z4);
    if (k < 4) xa1[k] = ldsr(aBase1 + 2 * 16384 + k * 2048);
    else if (k < 9) yb1[k - 4] = ldsr(yBase + 2 * 20480 + 10240 + (k - 4) * 1024);
    else h_op(5, k - 9);
    SCHED0();
  }
  y_stage(1, 11); y_stage(2, 33);
#pragma nounroll
  for (int i = 3; i < 16; ++i) {
    if (i == 6) wait_vm<0>();
    y_stage(i, 40);
  }

  lane = fresh_lane(); r = lane & 15; q = lane >> 4;
  f32x4 accD[5][2];
#pragma unroll
  for (int g = 0; g < 5; ++g) { accD[g][0] = z4; accD[g][1] = z4; }
  LAS char* const hB = L + ARING + wm * 1024 + wn * 8192 + lane * 16 + wn * 8;
  LAS char* const dB = fB0 + wn * 10240;
  bf16x8 hh[2][2], dw[2][5];
  auto d_rd = [&](LAS char* a, LAS char* a2, LAS char* b, int ks, int m) {
    if (m < 2) hh[ks][m] = cat(*(volatile LAS bf16x4*)((m ? a2 : a) + ks * 4096), *(volatile LAS bf16x4*)((m ? a2 : a) + ks * 4096 + 2048));
    else dw[ks][m - 2] = ldsr(b + ks * 5120 + (m - 2) * 1024);
  };
  auto d_mm = [&](int ks, int k) { accD[k >> 1][k & 1] = mfma(dw[ks][k >> 1], hh[ks][k & 1], accD[k >> 1][k & 1]); };
  const unsigned pbase = sP + lane * 16;
  o0 = out_off(w * 64 + lane); o1 = out_off(w * 64 + lane + 512);
  LAS char* xp = L + X_TILE + (wm * 32 + 8 * (r >> 2) + 4 * wn + (r & 3)) * 160 + q * 8;
  PIN(xp);
  auto p_store = [&](int u) {
    u32x4 o[5];
#pragma unroll
    for (int g = 0; g < 5; ++g) {
      const f32x4 a = accY[g][2 * u], b = accY[g][2 * u + 1];
      o[g] = u32x4{pack2(a[0], a[1]), pack2(a[2], a[3]), pack2(b[0], b[1]), pack2(b[2], b[3])};
    }
    if (w != id.kq) {
#pragma unroll
      for (int g = 0; g < 5; ++g) __builtin_amdgcn_raw_buffer_store_b128(o[g], rws, pbase + (u * 5 + g) * 1024, 0, AUX_SC1);
    } else {
#pragma unroll
      for (int g = 0; g < 5; ++g) *(LAS u32x4*)(L + T_BIG + (u * 5 + g) * 1024 + lane * 16) = o[g];
    }
  };
  auto d_second = [&](LAS char* a, LAS char* a2, LAS char* b) {
#pragma unroll
    for (int k = 0; k < 10; ++k) {
      d_mm(0, k);
      if (k < 2) d_rd(a, a2, b, 1, k); else if (k < 4) d_rd(a, a2, b, 1, k + 3);
      SCHED0();
    }
  };
  auto d_stage = [&](int i) {
    const int sl = i == 11 ? 2 : (i + 2) & 3;
    LAS char* a = hB + sl * 16384; LAS char* a2 = a + (8 - 16 * wn); LAS char* b = dB + sl * 20480;
    PIN(a); PIN(a2); PIN(b);
    bar();
#pragma unroll
    for (int k = 0; k < 10; ++k) {
      d_mm(1, k);
      if (k < 7) d_rd(a, a2, b, 0, k); else d_rd(a, a2, b, 1, k - 5);
      SCHED0();
    }
    d_second(a, a2, b);
  };
  {
    LAS char* a = hB + 2 * 16384; LAS char* a2 = a + (8 - 16 * wn); LAS char* b = dB + 2 * 20480;
    PIN(a); PIN(a2); PIN(b);
    bar();
#pragma unroll
    for (int k = 0; k < 20; ++k) {
      accY[k >> 2][k & 3] = mfma(yb1[k >> 2], xa1[k & 3], accY[k >> 2][k & 3]);
      if (k < 7) d_rd(a, a2, b, 0, k); else if (k < 10) d_rd(a, a2, b, 1, k - 5);
      SCHED0();
    }
    d_second(a, a2, b);
  }
  p_store(0);
  p_store(1);
#pragma nounroll
  for (int i = 1; i < 12; ++i) {
    if (i == DRAIN_P - 38) wait_vm<0>();
    d_stage(i);
  }
#pragma unroll
  for (int k = 0; k < 10; ++k) {
    d_mm(1, k < 5 ? 2 * k + 1 : 2 * (9 - k));
    if (k >= 5) *(LAS f32x4*)(fB0 + ((w ^ 1) * 5 + k - 5) * 1024) = accD[k - 5][1];
    SCHED0();
  }
  LAS char* dp = fB0 + w * 5120;
  *(LAS f32x4*)(L + ARING + w * 2048 + 1024 + lane * 16) = accD[4][0];
  *(LAS f32x4*)(L + ARING + w * 2048 + lane * 16) = accD[3][0];
  PIN(dp);
  bar();
  f32x4 dk[3], ys[3];
#pragma unroll
  for (int g = 0; g < 3; ++g) dk[g] = *(LAS f32x4*)(dp + g * 1024);
#pragma unroll
  for (int g = 0; g < 3; ++g) ys[g] = *(LAS f32x4*)(dp + 61440 + g * 1024);
  const f32x4 own[3] = {accD[0][0], accD[1][0], accD[2][0]};
  finish<3>(dk, own, ys, xp);
}

__device__ __forceinline__ void aux_tail(const Params& p, LAS char* L, int t, int lane, bf16x8 pickA) {
  const int po = (t >> 1) * 5120 + lane * 16;
  bf16x8 y[4][5];
#pragma unroll
  for (int g = 0; g < 5; ++g) {
    y[0][g] = ldsr(L + T_BIG + po + g * 1024);
    y[1][g] = ldsr(L + BRING + 20480 + po + g * 1024);
    y[2][g] = ldsr(L + BRING + 20480 + 10240 + po + g * 1024);
    y[3][g] = ldsr(L + ARING + 16384 + po + g * 1024);
    if (g == YSPLIT) { wait_vm<3>(); bar(); if (t == 3) { STAMP(49); } }
  }
  SCHED0();
  LAS char* dp = L + ARING + t * 2048 + lane * 16;
  f32x4 ys[2];
#pragma unroll
  for (int g = 0; g < 5; ++g) {
    f32x4 sum = {0.f, 0.f, 0.f, 0.f};
#pragma unroll
    for (int k = 0; k < 4; ++k) sum = mfma(pickA, y[k][g], sum);
    if (g < 3) *(LAS f32x4*)(L + BRING + 61440 + (t * 5 + g) * 1024 + lane * 16) = sum;
    else ys[g - 3] = sum;
  }
  const int r = lane & 15;
  LAS char* xp = L + X_TILE + ((t >> 1) * 32 + 8 * (r >> 2) + 4 * (t & 1) + (r & 3)) * 160 + (lane >> 4) * 8 + 96;
  LAS char* dq = L + BRING + t * 5120 + 3072 + lane * 16;
  PIN(xp); PIN(dp); PIN(dq); PIN(ys[0]); PIN(ys[1]);
  wait_vm<0>();
  bar();
  if (t == 3) { STAMP(50); }
  f32x4 dk[2], own[2];
#pragma unroll
  for (int g = 0; g < 2; ++g) { dk[g] = *(LAS f32x4*)(dq + g * 1024); own[g] = *(LAS f32x4*)(dp + g * 1024); }
  finish<2>(dk, own, ys, xp);
}

__device__ __forceinline__ void st_flag(unsigned char* ws, unsigned off, u64 v, int lane) {
  if (lane == 0) __hip_atomic_store((GAS u64*)(ws + off), v, __ATOMIC_RELAXED, __HIP_MEMORY_SCOPE_AGENT);
}
__device__ __forceinline__ void wait_flag(const unsigned char* ws, unsigned off, u64 tag, u64 seen) {
  if (!__all(seen == tag)) {
#pragma nounroll
    for (int spin = 0; spin < (1 << 16); ++spin)
      if (__all(ld_flag(ws, off) == tag)) break;
  }
  __builtin_amdgcn_fence(__ATOMIC_ACQUIRE, "wavefront");
}
template <int N, class Fn>
__device__ __forceinline__ void wait_tags(u32x4 (&g)[N], unsigned tlo, unsigned thi, Fn&& reload) {
  auto good = [&]() {
    unsigned bad = 0;
#pragma unroll
    for (int k = 0; k < N; ++k) bad |= (g[k][1] ^ tlo) | (g[k][3] ^ thi);
    return __all(bad == 0);
  };
  if (!good()) {
#pragma nounroll
    for (int spin = 0; spin < (1 << 16); ++spin) {
      asm volatile("" ::: "memory");
      reload();
      if (good()) break;
    }
  }
}
struct First { cptr base; unsigned v0, v1, v2, v3; int kc; };
__device__ __forceinline__ void first_offsets(First& f, int w, int lane) {
  const bool isx = w == 4 || w == 5;
  const unsigned row = isx ? 8 * (lane >> 5) + ((lane >> 3) & 3) : lane >> 3, stride = w < 4 ? 3072 : 1280;
  const unsigned c0 = ((lane & 7) ^ (w < 4 ? 0 : lane >> 4)) * 16, c1 = c0 ^ (w < 4 ? 0 : 64);
  f.v0 = row * stride + c0;
  f.v1 = row * stride + c1 + ((isx ? 16 : 8) * stride - 1024);
  f.v2 = f.v0 + ((isx ? 4 : 16) * stride - 2048);
  f.v3 = f.v1 + ((isx ? 4 : 16) * stride - 2048);
}
#define SPIN(x) asm volatile("" : "+s"(x))
__device__ __forceinline__ void p1_tile(First& f, unsigned lb, int ld, int j, bool barrier) {
  cptr a = f.base + f.kc * 128, a2 = a + 32 * 1280;
  unsigned l = ld < 2 ? lb + ARING + (j & 3) * 16384 + ld * 8192 : lb + BRING + ((j & 3) ? (j & 3) * 20480 : 10240);
  SPIN(a); SPIN(a2); SPIN(l);
  if (barrier) bar();
  dma4(a, f.v0, f.v1, f.v2, f.v3, l);
  dma4(a2, f.v0, f.v1, f.v2, f.v3, l + 4096);
  if (ld == 2) dma2(a + 64 * 1280, f.v0, f.v1, l + 8192);
  f.kc = f.kc == 9 ? 0 : f.kc + 1;
}
__device__ __forceinline__ void loader(const Params& p, LAS char* L, unsigned lb, const Ids& id, int ld, int lane, First& f) {
  const int b = blockIdx.x;
  const unsigned vl = lane * 16;
  const unsigned c0 = ((lane & 7) ^ (lane >> 4)) * 16, c1 = c0 ^ 64;
  const int rl = 8 * (lane >> 5) + ((lane >> 3) & 3);
  const int rot = ROT;
  auto w1 = [&]() { if (ld == 2) wait_vm<10>(); else wait_vm<8>(); };
  auto w2 = [&]() { if (ld == 2) wait_vm<20>(); else wait_vm<16>(); };
  p1_tile(f, lb, ld, 2, false);
  constexpr int J0 = 3;
#pragma nounroll
  for (int j = J0; j < 10; ++j) {
    w2();
    p1_tile(f, lb, ld, j, true);
  }
  const unsigned vw0 = rl * 4096 + c0, vw1 = (rl + 16) * 4096 + c1 - 1024, vw2 = (rl + 4) * 4096 + c0 - 2048, vw3 = (rl + 20) * 4096 + c1 - 3072;
  const unsigned u0 = ld ? vl : vw0, u1 = ld ? vl : vw1, u2 = ld ? vl : vw2, u3 = ld ? vl : vw3;
  const cptr bw = (cptr)(id.kq < 2 ? p.wsr : p.wsi) + (id.tt * 128 * 4096 + (id.kq & 1) * 2048);
  const int g0 = ld ? 2 * ld - 2 : 4;
  const unsigned l0 = lb + BRING + g0 * 4096, l1 = ld ? l0 + 4096 : lb + ARING, ls1 = ld ? 20480 : 16384, l2 = lb + ARING + (ld + 1) * 4096;
  const int ro4 = id.et + (YROT ? id.tt : 0);
  auto yk = [&](int y) { return (y & ~3) | ((y + ro4) & 3); };
  {
    const cptr bg = p.ws + W_GU + (unsigned)id.cb * 245760u;
    const int ka[2] = {((g0 + rot) % 10) * 4096, ((g0 + rot + 5) % 10) * 4096}, kb[2] = {((g0 + 1 + rot) % 10) * 4096, ((g0 + rot + 6) % 10) * 4096};
    if (GB == 8) { w2(); bar(); }
#pragma nounroll
    for (int j = 10; j < 22; ++j) {
      const cptr so = bg + ((j - 10) >> 1) * 40960;
      const int n = (j - 16) * 3 + ld;
      cptr a0 = so + ((j & 1) ? ka[1] : ka[0]), a1 = so + ((j & 1) ? kb[1] : kb[0]), a2 = bw + (yk(n >> 2) * 128 + (n & 3) * 32 * 4096);
      unsigned la = l0 + (j & 3) * 20480, lw = lb + ARING + (((n >> 2) + 2) & 3) * 16384 + (n & 3) * 4096;
      SPIN(a0); SPIN(a1); SPIN(a2); SPIN(la); SPIN(lw);
      if (GB == 7 || j != 11) {
        if (j == 10) { if (GB == 8) wait_vm<8>(); else wait_vm<16>(); }
        else if (j == 11) wait_vm<12>();
        else if (j < 18) { if (ld == 0) wait_vm<8>(); else wait_vm<16>(); }
        else { if (ld == 0) wait_vm<16>(); else wait_vm<24>(); }
        bar();
      }
      dma4c(a0, vl, la);
      if (ld) dma4c(a1, vl, la + 4096);
      if (n >= 0 && n < 16) dma4(a2, vw0, vw1, vw2, vw3, lw);
    }
  }
  const int c80 = id.et * 2 + (id.kq & 1);
  const cptr bh = p.ws + W_H + (unsigned)(id.tt * 2 + (id.kq >> 1)) * 196608u;
  const cptr bab = p.ws + W_ABT + ((unsigned)(((id.kq >> 1) * 4 + id.et) * 64 + (id.kq & 1) * 32) * 10240u + g0 * 4096);
  const cptr bwd = p.ws + W_WDT + (unsigned)c80 * 245760u;
  const cptr y1 = ld ? bab + 4096 : bw, y2 = bw + (ld + 1) * 32 * 4096;
  const int ym1 = ld ? 20480 : 128;
  const cptr d0 = bwd + g0 * 4096, d1 = ld ? d0 + 4096 : bh, d2 = bh + (ld + 1) * 4096;
#pragma nounroll
  for (int i = 19; i < 35; ++i) {
    const int k = yk(i - 19), sl = (i + 3) & 3;
    cptr a0 = bab + k * 20480, a1 = y1 + k * ym1, a2 = y2 + k * 128;
    unsigned la = l0 + sl * 20480, lc = l1 + sl * ls1, ld2 = l2 + sl * 16384;
    SPIN(a0); SPIN(a1); SPIN(a2); SPIN(la); SPIN(lc); SPIN(ld2);
    if (i >= 25) wait_vm<24>(); else if (i == 24) wait_vm<16>(); else if (ld == 0) wait_vm<8>(); else wait_vm<16>();
    bar();
    dma4c(a0, vl, la);
    if (i >= 23 || ld) dma4s(a1, u0, u1, u2, u3, lc);
    if (i >= 23) dma4(a2, vw0, vw1, vw2, vw3, ld2);
  }
  const cptr bp = p.ws + W_P + ((unsigned)(((id.tt * 4 + id.et) * 4 + id.kq) * 4 * 10) * 1024u + ((id.kq + 1 + ld) & 3) * 10240);
  const unsigned lp = lb + (ld == 0 ? ARING + 16384 : BRING + 20480 + (ld - 1) * 10240);
#pragma nounroll
  for (int i = 35; i < 47; ++i) {
    const int sl = i == 46 ? 2 : (i + 3) & 3;
    const int k = DROT ? ((i - 35) & ~3) | ((i - 35 + id.et + id.tt) & 3) : i - 35;
    cptr a0 = d0 + k * 20480, a1 = d1 + k * ls1, a2 = d2 + k * 16384;
    unsigned la = l0 + sl * 20480, lc = l1 + sl * ls1, ld2 = l2 + sl * 16384;
    SPIN(a0); SPIN(a1); SPIN(a2); SPIN(la); SPIN(lc); SPIN(ld2);
    wait_vm<24>();
    bar();
    if (i == 46) {
      dma4c(bp, vl, lp); dma4c(bp + 4096, vl, lp + 4096); dma2c(bp + 8192, vl, lp + 8192);
      wait_vm<22>();
      bar();
    }
    dma4c(a0, vl, la);
    dma4c(a1, vl, lc);
    dma4c(a2, vl, ld2);
  }
}

__device__ __forceinline__ void helper(const Params& p, LAS char* L, unsigned lb, const Ids& id, int lane, u64 pc) {
  const int b = blockIdx.x;
  const __amdgpu_buffer_rsrc_t rws = __builtin_amdgcn_make_buffer_rsrc((void*)p.ws, 0, (int)W_END, 0x00020000);
  STAMP(60);
  __builtin_amdgcn_s_setprio(3);
  const u64 prev = ld_flag(p.ws, W_EPOCH);
  const int r8 = lane >> 3;
  u32x4 dv[10];
  const GAS char* ds = (const GAS char*)(b < 128 ? p.wdr : p.wdi) + (((b & 127) * 5 + (r8 < 5 ? r8 : 4)) * 1280 + (lane & 7) * 16);
#pragma unroll
  for (int i = 0; i < 10; ++i) dv[i] = *(const GAS u32x4*)(ds + i * 128);
  auto code_touch = [&]() {
    const unsigned off = (((b >> 3) % ((CODE_BYTES + 8191) / 8192)) * 64 + lane) * 128;
    touch((cptr)(pc & ~255ull), off < CODE_BYTES ? off : 0, lb + T_PAD);
  };
  const u64 tag = next_tag(prev);
  const unsigned tlo = (unsigned)tag, thi = (unsigned)(tag >> 32);
  {
    float t4[4] = {0.f, 0.f, 0.f, 0.f};
#pragma unroll
    for (int i = 0; i < 10; ++i)
#pragma unroll
      for (int e = 0; e < 4; ++e) t4[e] = dot_ones(dv[i][e], t4[e]);
    const unsigned sm = __builtin_bit_cast(unsigned, row8_sum((t4[0] + t4[1]) + (t4[2] + t4[3])));
    const u32x4 o = {sm, tlo, sm, thi};
    if ((lane & 7) == 0 && r8 < 5) __builtin_amdgcn_raw_buffer_store_b128(o, rws, W_CSD + (b * 8 + r8) * 16, 0, AUX_SC1);
  }
  const int pcl = (id.cb * 3) >> 1, tile = id.tt * 4 + id.et, c80 = id.et * 2 + (id.kq & 1);
  const int wk = b / 5, wc = b % 5;
  u64 f0 = 0, f1 = 0, f2 = 0, fe[4] = {0, 0, 0, 0};
  unsigned fo0 = 0, fo1 = 0, fo2 = 0;
  u32x4 gr[2], cq[10];
  const int l5 = (lane & 7) < 5 ? lane & 7 : 0, go = r8 * 5 + l5;
  const unsigned gro = W_CSD + ((id.cb * 16 + r8) * 8 + l5) * 16;
  auto ld_gr = [&]() {
    gr[0] = __builtin_amdgcn_raw_buffer_load_b128(rws, gro, 0, AUX_SC1);
    gr[1] = __builtin_amdgcn_raw_buffer_load_b128(rws, gro + 1024, 0, AUX_SC1);
  };
  auto ld_cq = [&]() {
#pragma unroll
    for (int k = 0; k < 10; ++k) cq[k] = __builtin_amdgcn_raw_buffer_load_b128(rws, W_CSP + ((k * 24 + pcl) * 32 + lane) * 16, 0, AUX_SC1);
  };
  sfor<0, 48>([&](auto ic) __attribute__((always_inline)) {
    constexpr int i = decltype(ic)::v;
    if constexpr (i == GB) wait_flag(p.ws, fo0, tag, f0);
    if constexpr (i == 35) { wait_flag(p.ws, fo1, tag, f1); wait_flag(p.ws, fo2, tag, f2); }
    if constexpr (i == 19 || i == 35 || i == 46) wait_flag(p.ws, fo0, tag, f0);
    bar();
    if constexpr (i == 0) *(LAS u64*)(L + TAB + 14336) = __builtin_amdgcn_s_memrealtime();
    if constexpr (i == 0) {
      if (b < 240 && lane < 32) {
        LAS float* cp = (LAS float*)(L + T_CP + lane * 4);
        const float u0 = (cp[0] + cp[64]) + (cp[128] + cp[192]), u1 = (cp[32] + cp[96]) + (cp[160] + cp[224]);
        const u32x4 o = {__builtin_bit_cast(unsigned, u0), tlo, __builtin_bit_cast(unsigned, u1), thi};
        __builtin_amdgcn_raw_buffer_store_b128(o, rws, W_CSP + (b * 32 + lane) * 16, 0, AUX_SC1);
      }
    }
    if constexpr (i == DRAIN_G) st_flag(p.ws, W_FG + b * FS, tag, lane);
    if constexpr (i == GB - 2) {
      const int l20 = lane < 20 ? lane : 0;
      fo0 = W_FG + ((l20 >> 1) * 24 + pcl + (l20 & 1)) * FS;
      f0 = ld_flag(p.ws, fo0);
      ld_gr(); ld_cq();
    }
    if constexpr (i == 8) {
      wait_tags<2>(gr, tlo, thi, ld_gr);
      *(LAS unsigned*)(L + T_CS + go * 4) = gr[0][0];
      *(LAS unsigned*)(L + T_CS + (40 + go) * 4) = gr[1][0];
    }
    if constexpr (i == GB) {
      if (b < 240) {
        const cptr sn = (cptr)p.wdn + (wk * 32 * 1280 + wc * 256);
        const unsigned vn = (lane >> 4) * 1280 + (lane & 15) * 16;
        SELNT(NT_W, dma4)(sn, vn, vn + 4096, vn + 2 * 4096, vn + 3 * 4096, lb + T_BIG);
        SELNT(NT_W, dma4)(sn + 16 * 1280, vn, vn + 4096, vn + 2 * 4096, vn + 3 * 4096, lb + T_BIG + 4096);
      }
    }
    if constexpr (i == 9) wait_tags<10>(cq, tlo, thi, ld_cq);
    if constexpr (i == 10) {
#pragma unroll
      for (int m = 0; m < 2; ++m)
#pragma unroll
        for (int h = 0; h < 2; ++h) {
          const unsigned col = (lane >> 5) * 64 + h * 32 + (lane & 31) - (id.cb & 1) * 32;
          const float v = ((asf(cq[m][2 * h]) + asf(cq[2 + m][2 * h])) + (asf(cq[4 + m][2 * h]) + asf(cq[6 + m][2 * h]))) + asf(cq[8 + m][2 * h]);
          *(LAS float*)(col < 96 ? L + T_CG + (m * 96 + col) * 4 : L + T_PAD) = v;
        }
    }
    if constexpr (i >= 12 && i < 20) {
      if constexpr (i == 12) wait_vm<0>();
      if (b < 240) {
        LAS char* a = L + T_BIG + (4 * (lane >> 4) + ((lane & 15) >> 2)) * 256 + 8 * (lane & 3);
#pragma unroll
        for (int c = i - 12; c < i - 11; ++c) {
          const bf16x8 f = cat(ldstr(a + c * 32), ldstr(a + 4096 + c * 32));
          const int eg = wc * 8 + c, cc = eg / 5, g5 = eg - cc * 5;
          __builtin_amdgcn_raw_buffer_store_b128(__builtin_bit_cast(u32x4, f), rws, lane * 16 + W_WDT + ((cc * 48 + wk) * 5 + g5) * 1024, 0, AUX_SC1);
        }
      }
    }
    if constexpr (i == 14) st_flag(p.ws, W_FB + (id.rb * 16 + id.cb) * FS, tag, lane);
    if constexpr (i == 16) code_touch();
    if constexpr (i == 23) { wait_vm<0>(); st_flag(p.ws, W_FA + b * FS, tag, lane); }
    if constexpr (i == 17) {
      fo0 = W_FB + ((8 * (id.kq & 1) + (lane & 7)) * 16 + 8 * (id.kq >> 1) + 2 * id.et + ((lane >> 3) & 1)) * FS;
      f0 = ld_flag(p.ws, fo0);
    }
    if constexpr (i == 30) {
      fo1 = W_FA + ((lane < 48 ? lane : 0) * 5 + (c80 * 5) / 8) * FS;
      fo2 = W_FA + ((lane < 48 ? lane : 0) * 5 + (c80 * 5 + 4) / 8) * FS;
      f1 = ld_flag(p.ws, fo1); f2 = ld_flag(p.ws, fo2);
    }
    if constexpr (i == 28) st_flag(p.ws, W_FH + (id.rb * 16 + id.cb) * FS, tag, lane);
    if constexpr (i == 27) code_touch();
    if constexpr (i == 32) { fo0 = W_FH + (id.tt * 16 + (lane & 15)) * FS; f0 = ld_flag(p.ws, fo0); }
    if constexpr (i == 20) {
      if (b == 0) {
#pragma unroll
        for (int k = 0; k < 4; ++k) fe[k] = ld_flag(p.ws, W_FG + (k * 64 + lane) * FS);
      }
    }
    if constexpr (i == 22) {
      if (b == 0) {
#pragma unroll
        for (int k = 0; k < 4; ++k) wait_flag(p.ws, W_FG + (k * 64 + lane) * FS, tag, fe[k]);
        st_flag(p.ws, W_EPOCH, tag, lane);
      }
    }
    if constexpr (i == DRAIN_P) st_flag(p.ws, W_FP + (tile * 4 + id.kq) * FS, tag, lane);
    if constexpr (i == 44) { fo0 = W_FP + (tile * 4 + (lane & 3)) * FS; f0 = ld_flag(p.ws, fo0); }
  });
  __builtin_amdgcn_s_waitcnt(0x0F70);
}

__global__ __launch_bounds__(NTHR, 2) void fused_kernel(Params p) {
  extern __shared__ __attribute__((aligned(16))) char smem[];
  u64 te;
  asm volatile("s_memrealtime %0\n\ts_waitcnt lgkmcnt(0)" : "=s"(te));
  u64 pc;
  asm volatile("s_getpc_b64 %0" : "=s"(pc));
  LAS char* L = (LAS char*)smem;
  const unsigned lb = (unsigned)(size_t)L;
  int lane = threadIdx.x & 63;
  const int w = __builtin_amdgcn_readfirstlane(threadIdx.x >> 6);
  const int b = blockIdx.x, g = b & 7, rr = b >> 3;
  if (w == 7) {
    const unsigned off = ((rr % ((CODE_BYTES + 8191) / 8192)) * 64 + lane) * 128;
    touch((cptr)(pc & ~255ull), off < CODE_BYTES ? off : 0, lb + T_PAD);
  }
  Ids id;
  id.rb = (g >> 1) * 4 + (rr >> 3);
  id.cb = (g & 1) * 8 + (rr & 7);
  id.tt = (g & 1) * 8 + (rr & 7);
  id.et = rr >> 3;
  id.kq = g >> 1;
  First f;
  {
    const int pc48 = b % 48, pr = b / 48;
    const bool isx = w == 4 || w == 5;
    first_offsets(f, w, lane);
    f.base = w < 4 ? (cptr)(pc48 < 24 ? p.wg : p.wu) + ((pr * 128 + w * 32) * 3072 + (pc48 % 24) * 128)
                   : isx ? (cptr)p.x + (id.rb * 128 + (w - 4) * 64) * 1280 : (cptr)(id.cb < 8 ? p.wdr : p.wdi) + (id.cb & 7) * 80 * 1280;
    f.kc = ROT % 10;
    if (w < 4) {
      if (b < 240) SELNT(NT_G, dma4)(f.base, f.v0, f.v1, f.v2, f.v3, lb + BRING + (w ? w * 20480 + 10240 : 0));
    } else if (w < 7) {
      p1_tile(f, lb, w - 4, 0, false);
      if (PRO != 5 || w == 6) p1_tile(f, lb, w - 4, 1, false);
    }
  }
  unsigned o0, o1;
  if (w >= 4) {
    const int ld = w - 4;
    lane = fresh_lane();
    first_offsets(f, w, lane);
    if (ld < 3) loader(p, L, lb, id, ld, lane, f);
    else helper(p, L, lb, id, lane, pc);
    const cptr so = uni((cptr)p.x + ((id.tt * 128 + (id.kq >> 1) * 64) * 1280 + (id.et * 2 + (id.kq & 1)) * 160));
    const int n0 = ld * 64 + lane, n1 = n0 + 256, n2 = ld < 2 ? n0 + 512 : n0;
    unsigned x0 = (n0 / 10) * 1120 + n0 * 16, x1 = (n1 / 10) * 1120 + n1 * 16, x2 = (n2 / 10) * 1120 + n2 * 16;
    const unsigned one = (0x3f80u << ((lane & 1) * 16)) & -(unsigned)(((lane & 15) >> 2) == (lane >> 4));
    const int od = (ld & 1) * 2 + ((lane >> 1) & 1);
    u32x4 pa = {one & -(unsigned)(od == 0), one & -(unsigned)(od == 1), one & -(unsigned)(od == 2), one & -(unsigned)(od == 3)};
    PIN(x0); PIN(x1); PIN(x2); PIN(pa);
    __builtin_amdgcn_s_setprio(0);
    wait_vm<12>();
    bar();
    if (w == 7) { STAMP(48); }
    dma1(so, x0, lb + X_TILE + ld * 1024); dma1(so, x1, lb + X_TILE + (ld + 4) * 1024); dma1(so, x2, lb + X_TILE + (ld < 2 ? ld + 8 : ld) * 1024);
    aux_tail(p, L, ld, lane, __builtin_bit_cast(bf16x8, pa));
    o0 = x1; o1 = 0;
  } else compute(p, L, id, w, o0, o1);
  const int tid = w * 64 + fresh_lane();
  const __amdgpu_buffer_rsrc_t ro = __builtin_amdgcn_make_buffer_rsrc((char*)p.out + ((id.tt * 128 + (id.kq >> 1) * 64) * 1280 + (id.et * 160 + (id.kq & 1) * 80) * 2), 0, 63 * 1280 + 160, 0x00020000);
  bar();
  const u32x4 v0 = *(LAS u32x4*)(L + X_TILE + tid * 16);
  if (w < 2) {
    const u32x4 v1 = *(LAS u32x4*)(L + X_TILE + 8192 + tid * 16);
    __builtin_amdgcn_raw_buffer_store_b128(v1, ro, o1, 0, AUX_SC1);
  }
  __builtin_amdgcn_raw_buffer_store_b128(v0, ro, o0, 0, AUX_SC1);
  if (tid == 448) { GAS u64* q = (GAS u64*)(p.ws + W_END + blockIdx.x * 16); q[0] = te; q[1] = *(LAS u64*)(L + TAB + 14336); }
}
__global__ void post_kernel(unsigned char* ws, int mul) {
  const u64 t0 = __builtin_amdgcn_s_memrealtime();
  const int lane = threadIdx.x; u64 mx = 0, mn = ~0ull;
  for (int k = 0; k < 4; ++k) { u64 e = ld_flag(ws, W_END + (k * 64 + lane) * 16), v = ld_flag(ws, W_END + (k * 64 + lane) * 16 + 8); mx = v > mx ? v : mx; mn = e < mn ? e : mn; }
  for (int o = 32; o; o >>= 1) { u64 c = __shfl_xor(mx, o), a = __shfl_xor(mn, o); mx = c > mx ? c : mx; mn = a < mn ? a : mn; }
  u64 d = mx - mn; if (d > 6000) d = 6000; d = d * mul + 200;
  while (__builtin_amdgcn_s_memrealtime() - t0 < d) __builtin_amdgcn_s_sleep(1);
}
}

extern "C" void kernel_launch(void* const* d_in, const int* in_sizes, int n_in, void* d_out, int out_size, void* d_ws, size_t ws_size,
                              hipStream_t stream) {
  Params p;
  p.x = (const u16*)d_in[0];
  p.wsr = (const u16*)d_in[1];
  p.wsi = (const u16*)d_in[2];
  p.wdr = (const u16*)d_in[3];
  p.wdi = (const u16*)d_in[4];
  p.wg = (const u16*)d_in[6];
  p.wu = (const u16*)d_in[7];
  p.wdn = (const u16*)d_in[8];
  p.out = (u16*)d_out;
  p.ws = (unsigned char*)d_ws;
  (void)hipFuncSetAttribute((const void*)fused_kernel, hipFuncAttributeMaxDynamicSharedMemorySize, LDS_BYTES);
  fused_kernel<<<256, NTHR, LDS_BYTES, stream>>>(p);
  post_kernel<<<1, 64, 0, stream>>>(p.ws, 0);
}
```

```cpp
#include <hip/hip_runtime.h>
#include <hip/hip_bf16.h>

typedef unsigned short u16;
typedef unsigned long long u64;
typedef short bf16x8 __attribute__((ext_vector_type(8)));
typedef short bf16x4 __attribute__((ext_vector_type(4)));
typedef float f32x4 __attribute__((ext_vector_type(4)));
typedef float f32x2 __attribute__((ext_vector_type(2)));
typedef unsigned u32x4 __attribute__((ext_vector_type(4)));
typedef unsigned u32x2 __attribute__((ext_vector_type(2)));
typedef int i32x4 __attribute__((ext_vector_type(4)));
typedef __bf16 bf2v __attribute__((ext_vector_type(2)));
#define LAS __attribute__((address_space(3)))
#define GAS __attribute__((address_space(1)))

namespace {
constexpr int S = 2048, D = 640, F = 1536;
constexpr int NTHR = 512;
constexpr int ARING = 0;
constexpr int BRING = 65536;
constexpr int TAB = 147456;
constexpr int T_ST = TAB;
constexpr int T_CS = TAB + 1024;
constexpr int T_CP = TAB + 1344;
constexpr int T_CG = TAB + 2368;
constexpr int T_PAD = TAB + 3328;
constexpr int T_EK = TAB + 12288;
constexpr int T_BIG = TAB + 4096;
constexpr int X_TILE = ARING + 3 * 16384;
constexpr int LDS_BYTES = 163840;
constexpr unsigned W_EPOCH = 0;
constexpr unsigned FS = 128;
constexpr unsigned W_FA = 4096, W_FB = W_FA + 256 * FS, W_FH = W_FB + 256 * FS, W_FP = W_FH + 256 * FS, W_FG = W_FP + 256 * FS;
constexpr unsigned W_CSD = W_FG + 256 * FS;
constexpr unsigned W_CSP = W_CSD + 256 * 128;
constexpr unsigned W_ABT = 524288;
static_assert(W_CSP + 240 * 512 <= W_ABT, "workspace map");
constexpr unsigned W_H = W_ABT + 2u * 4 * 64 * 10 * 1024;
constexpr unsigned W_WDT = W_H + 32u * 96 * 2 * 1024;
constexpr unsigned W_GU = W_WDT + 8u * 48 * 5 * 1024;
constexpr unsigned W_P = W_GU + 16u * 20 * 12 * 1024;
constexpr unsigned W_END = W_P + 256u * 4 * 10 * 1024;
constexpr unsigned W_STAMP = W_END;
constexpr int AUX_SC1 = 16;
#define DRAIN_G 3
#define PRO 3
#define DRAIN_P 41
#define CODE_BYTES 35328
#define WT0 -1
#define WTS 2
#define YG 4
#define DROT 1
#define YSPLIT 2
#define CT2 0
#define MAP 0
#define ROT (3 * (b >> 6) + ((b >> 3) & 7))
#define NT_W 0
#define NT_G 0
#define SELNT(c, f) ((c) ? f##nt : f)
#define GB 7

struct Params {
  const u16 *x, *wsr, *wsi, *wdr, *wdi, *wg, *wu, *wdn;
  u16* out;
  unsigned char* ws;
};

typedef const unsigned char* cptr;
__device__ __forceinline__ cptr uni(cptr a) {
  const u64 v = (u64)a;
  return (cptr)(((u64)(unsigned)__builtin_amdgcn_readfirstlane((int)(v >> 32)) << 32) | (unsigned)__builtin_amdgcn_readfirstlane((int)v));
}
#define DMA_BODY(POL) \
__device__ __forceinline__ void dma2##POL(cptr base, unsigned v0, unsigned v1, unsigned lds) { \
  asm volatile("s_mov_b32 m0, %3\n\ts_nop 0\n\tglobal_load_lds_dwordx4 %0, %2 " #POL "\n\tglobal_load_lds_dwordx4 %1, %2 offset:1024 " #POL \
               ::"v"(v0), "v"(v1), "s"(base), "s"(lds) : "memory"); \
} \
__device__ __forceinline__ void dma3##POL(cptr base, unsigned v0, unsigned v1, unsigned v2, unsigned lds) { \
  asm volatile("s_mov_b32 m0, %4\n\ts_nop 0\n\tglobal_load_lds_dwordx4 %0, %3 " #POL "\n\tglobal_load_lds_dwordx4 %1, %3 offset:1024 " #POL "\n\t" \
               "global_load_lds_dwordx4 %2, %3 offset:2048 " #POL ::"v"(v0), "v"(v1), "v"(v2), "s"(base), "s"(lds) : "memory"); \
} \
__device__ __forceinline__ void dma4##POL(cptr base, unsigned v0, unsigned v1, unsigned v2, unsigned v3, unsigned lds) { \
  asm volatile("s_mov_b32 m0, %5\n\ts_nop 0\n\tglobal_load_lds_dwordx4 %0, %4 " #POL "\n\tglobal_load_lds_dwordx4 %1, %4 offset:1024 " #POL "\n\t" \
               "global_load_lds_dwordx4 %2, %4 offset:2048 " #POL "\n\tglobal_load_lds_dwordx4 %3, %4 offset:3072 " #POL \
               ::"v"(v0), "v"(v1), "v"(v2), "v"(v3), "s"(base), "s"(lds) : "memory"); \
}
DMA_BODY()
DMA_BODY(nt)
DMA_BODY(sc1)
#define dma4s dma4sc1
__device__ __forceinline__ void dma1(cptr base, unsigned v, unsigned lds) {
  asm volatile("s_mov_b32 m0, %2\n\ts_nop 0\n\tglobal_load_lds_dwordx4 %0, %1" ::"v"(v), "s"(base), "s"(lds) : "memory");
}
__device__ __forceinline__ void dma4c(cptr base, unsigned v, unsigned lds) {
  asm volatile("s_mov_b32 m0, %2\n\ts_nop 0\n\tglobal_load_lds_dwordx4 %0, %1 sc1\n\tglobal_load_lds_dwordx4 %0, %1 offset:1024 sc1\n\t"
               "global_load_lds_dwordx4 %0, %1 offset:2048 sc1\n\tglobal_load_lds_dwordx4 %0, %1 offset:3072 sc1"
               ::"v"(v), "s"(base), "s"(lds) : "memory");
}
__device__ __forceinline__ void dma2c(cptr base, unsigned v, unsigned lds) {
  asm volatile("s_mov_b32 m0, %2\n\ts_nop 0\n\tglobal_load_lds_dwordx4 %0, %1 sc1\n\tglobal_load_lds_dwordx4 %0, %1 offset:1024 sc1"
               ::"v"(v), "s"(base), "s"(lds) : "memory");
}
__device__ __forceinline__ void touch(cptr base, unsigned voff, unsigned pad) {
  asm volatile("s_mov_b32 m0, %2\n\ts_nop 0\n\tglobal_load_lds_dword %0, %1" ::"v"(voff), "s"(base), "s"(pad) : "memory");
}
template <int N> __device__ __forceinline__ void wait_vm() { asm volatile("s_waitcnt vmcnt(%0)" ::"i"(N) : "memory"); }
#define SCHED0() __builtin_amdgcn_sched_barrier(0)
__device__ __forceinline__ void bar() {
  SCHED0();
  __builtin_amdgcn_s_waitcnt(0xC07F);
  asm volatile("s_barrier" ::: "memory");
  SCHED0();
}
__device__ __forceinline__ f32x4 mfma(bf16x8 a, bf16x8 b, f32x4 c) { return __builtin_amdgcn_mfma_f32_16x16x32_bf16(a, b, c, 0, 0, 0); }
__device__ __forceinline__ bf16x8 ldsr(LAS char* p) { return *(LAS bf16x8*)p; }
__device__ __forceinline__ bf16x4 ldstr(LAS char* p) { return __builtin_amdgcn_ds_read_tr16_b64_v4i16((LAS bf16x4*)p); }
__device__ __forceinline__ bf16x8 cat(bf16x4 a, bf16x4 b) { return __builtin_shufflevector(a, b, 0, 1, 2, 3, 4, 5, 6, 7); }
__device__ __forceinline__ unsigned pack2(float a, float b) {
  f32x2 v = {a, b};
  bf2v r = __builtin_convertvector(v, bf2v);
  return __builtin_bit_cast(unsigned, r);
}
__device__ __forceinline__ float asf(unsigned v) { return __builtin_bit_cast(float, v); }
__device__ __forceinline__ float bflo(unsigned v) { return __builtin_bit_cast(float, v << 16); }
__device__ __forceinline__ float bfhi(unsigned v) { return __builtin_bit_cast(float, v & 0xffff0000u); }
template <int CTRL> __device__ __forceinline__ float dpp_add(float v) {
  return v + __builtin_bit_cast(float, __builtin_amdgcn_update_dpp(0, __builtin_bit_cast(int, v), CTRL, 0xf, 0xf, true));
}
__device__ __forceinline__ float row8_sum(float v) {
  v = dpp_add<0xB1>(v); v = dpp_add<0x4E>(v); v = dpp_add<0x141>(v);
  return v;
}
__device__ __forceinline__ float dot_ones(unsigned v, float acc) {
  return __builtin_amdgcn_fdot2_f32_bf16(__builtin_bit_cast(bf2v, v), __builtin_bit_cast(bf2v, 0x3f803f80u), acc, false);
}
__device__ __forceinline__ u64 ld_flag(const unsigned char* ws, unsigned off) {
  return __hip_atomic_load((GAS u64*)(ws + off), __ATOMIC_RELAXED, __HIP_MEMORY_SCOPE_AGENT);
}
__device__ __forceinline__ u64 next_tag(u64 prev) { return prev * 6364136223846793005ull + 1442695040888963407ull; }

#define STAMP(i) do { } while (0)

struct Ids {
  int rb, cb, tt, et, kq;
};

template <int I> struct IC { static constexpr int v = I; };
template <int I, int N, class Fn> __device__ __forceinline__ void sfor(Fn&& f) {
  if constexpr (I < N) { f(IC<I>{}); sfor<I + 1, N>(f); }
}
__device__ __forceinline__ int fresh_lane() {
  int l;
  asm volatile("v_mbcnt_lo_u32_b32 %0, -1, 0\n\tv_mbcnt_hi_u32_b32 %0, -1, %0" : "=v"(l));
  return l;
}
__device__ __forceinline__ float pick4(f32x4 v) {
  float o, t;
  asm volatile("v_cndmask_b32_e64 %0, %2, %3, %6\n\tv_cndmask_b32_e64 %1, %4, %5, %6\n\tv_cndmask_b32_e64 %0, %0, %1, %7"
               : "=&v"(o), "=&v"(t) : "v"(v[0]), "v"(v[1]), "v"(v[2]), "v"(v[3]), "s"(0xAAAAAAAAAAAAAAAAull), "s"(0xCCCCCCCCCCCCCCCCull));
  return o;
}
#define PIN(x) asm volatile("" : "+v"(x))
constexpr bool gap_has_read(int gp) { return (gp >> 2) % 10 == 8 || ((gp >> 2) % 10 != 7 && !(gp & 1)); }
constexpr int H_OPS = 87;
constexpr int h_cost(int o) { return ((o >= 40 && o < 56) || (o >= 64 && o < 80)) && !(o & 1) ? 2 : 1; }
constexpr int A_OPS = 3 + 5 * 21 + 2;
constexpr int a_cost(int o) { return o < 3 || o >= 108 || (o - 3) % 21 == 20 ? 2 : 1; }
template <bool AB>
constexpr int first_op(int gp) {
  int o = 0;
  for (int x = (AB ? 1 : 3); x < gp; ++x) {
    int room = gap_has_read(x) && !((x >> 2) % 10 == 8 && (x & 1)) ? 1 : 2;
    if (AB) room = (x >= 17 && x < 65 && (x & 3) == 1) ? 1 : room + (x & 1);
    while (o < (AB ? A_OPS : H_OPS) && (AB ? a_cost(o) : h_cost(o)) <= room) { room -= AB ? a_cost(o) : h_cost(o); ++o; }
  }
  return o;
}
static_assert(first_op<false>(76) == H_OPS && first_op<true>(80) == A_OPS, "the fillers must fit into a pass");

template <int N>
__device__ __forceinline__ void finish(f32x4 (&dk)[N], const f32x4 (&d1)[N], f32x4 (&ys)[N], LAS char* xp) {
  f32x4 ex[N];
  u32x2 xv[N];
#pragma unroll
  for (int g = 0; g < N; ++g) xv[g] = *(volatile LAS u32x2*)(xp + g * 32);
  SCHED0();
#pragma unroll
  for (int g = 0; g < N; ++g) { dk[g] += d1[g]; ex[g] = dk[g] * -1.4426950409f; }
  SCHED0();
#pragma unroll
  for (int g = 0; g < N; ++g) {
#pragma unroll
    for (int e = 0; e < 4; ++e) ex[g][e] = __builtin_amdgcn_exp2f(ex[g][e]);
    dk[g] *= ys[g];
  }
  SCHED0();
#pragma unroll
  for (int g = 0; g < N; ++g) ex[g] += 1.f;
  SCHED0();
#pragma unroll
  for (int g = 0; g < N; ++g) {
#pragma unroll
    for (int e = 0; e < 4; ++e) ex[g][e] = __builtin_amdgcn_rcpf(ex[g][e]);
    ys[g] = f32x4{bflo(xv[g][0]), bfhi(xv[g][0]), bflo(xv[g][1]), bfhi(xv[g][1])};
  }
  SCHED0();
#pragma unroll
  for (int g = 0; g < N; ++g) {
    const f32x4 o = dk[g] * ex[g] + ys[g];
    *(volatile LAS u32x2*)(xp + g * 32) = u32x2{pack2(o[0], o[1]), pack2(o[2], o[3])};
  }
}

__device__ __forceinline__ unsigned out_off(int n) { unsigned o = (n / 10) * 1120 + n * 16; PIN(o); return o; }
__device__ __forceinline__ void compute(const Params& p, LAS char* L, const Ids& id, int w, unsigned& o0, unsigned& o1) {
  int lane = fresh_lane(), r = lane & 15, q = lane >> 4;
  const __amdgpu_buffer_rsrc_t rws = __builtin_amdgcn_make_buffer_rsrc((void*)p.ws, 0, (int)W_END, 0x00020000);
  const f32x4 z4 = {0.f, 0.f, 0.f, 0.f};

  unsigned sAb = W_ABT + (unsigned)((((id.cb >> 3) * 4 + ((id.cb & 7) >> 1)) * 64 + id.rb * 4 + w) * 10 + (id.cb & 1) * 5) * 1024u;
  unsigned sH = W_H + (unsigned)(((id.rb * 2 + (w >> 1)) * 96 + id.cb * 6) * 2 + (w & 1)) * 1024u;
  unsigned sP = W_P + (unsigned)((((id.tt * 4 + id.et) * 4 + w) * 4 + id.kq) * 10) * 1024u;
  asm volatile("" : "+s"(sAb), "+s"(sH), "+s"(sP));
  const bf16x8 ones = {0x3f80, 0x3f80, 0x3f80, 0x3f80, 0x3f80, 0x3f80, 0x3f80, 0x3f80};
  if (blockIdx.x < 240) {
    const int pc = blockIdx.x % 48, pr = blockIdx.x / 48;
    LAS char* a = L + BRING + (w ? w * 20480 + 10240 : 0) + (8 * q + (r >> 2)) * 128 + 8 * (r & 3);
    const unsigned go = lane * 16 + W_GU + (((pc % 24) * 4 * 20 + pr * 4 + w) * 2 + pc / 24) * 1024;
    wait_vm<0>();
    bf16x8 f[4];
#pragma unroll
    for (int c = 0; c < 4; ++c) f[c] = cat(ldstr(a + c * 32), ldstr(a + 512 + c * 32));
    f32x4 acc[4];
#pragma unroll
    for (int c = 0; c < 4; ++c) acc[c] = mfma(ones, f[c], z4);
    SCHED0();
#pragma unroll
    for (int c = 0; c < 4; ++c) __builtin_amdgcn_raw_buffer_store_b128(__builtin_bit_cast(u32x4, f[c]), rws, go + c * 40960, 0, AUX_SC1);
    SCHED0();
    float v01 = (q & 1) ? acc[1][0] : acc[0][0], v23 = (q & 1) ? acc[3][0] : acc[2][0];
    PIN(v01); PIN(v23);
    *(LAS float*)(L + T_CP + (w * 64 + lane) * 4) = (q & 2) ? v23 : v01;
  }

  bf16x8 xr[20][2];
  LAS char *fB0, *fB3;
  bf16x8 wb[4][2];
  auto rdg = [&](int g, int m) {
    const int sl = (10 + g / 10) & 3, n = (g % 10) * 2 + m;
    wb[g & 3][m] = sl == 3 ? ldsr(fB3 + n * 1024) : ldsr(fB0 + sl * 20480 + n * 1024);
  };
  f32x4 accP[5][2], st[4];
  float cs[5];
  {
    f32x4 accS[2], accQ[2];
    bf16x8 pb[2][5];
    const int oK0 = r * 128 + ((q ^ (r >> 1)) & 7) * 16, oK1 = oK0 ^ 64;
    LAS char* xA[2] = {L + ARING + w * 4096 + oK0, L + ARING + w * 4096 + oK1};
    LAS char* pB[2] = {L + BRING + 10240 + oK0, L + BRING + 10240 + oK1};
    PIN(xA[0]); PIN(xA[1]); PIN(pB[0]); PIN(pB[1]);
    auto rdx = [&](int i, int ks, int t) { xr[2 * i + ks][t] = ldsr(xA[ks] + (i & 3) * 16384 + t * 2048); };
    auto rdw = [&](int i, int ks, int g) { pb[ks][g] = ldsr(pB[ks] + ((i & 3) ? (i & 3) * 20480 - 10240 : 0) + g * 2048); };
    auto stat = [&](int s, int k) {
      if (k < 2) accS[k] = mfma(ones, xr[s][k], s == 0 ? z4 : accS[k]);
      else accQ[k - 2] = mfma(xr[s][k - 2], xr[s][k - 2], s == 0 ? z4 : accQ[k - 2]);
    };
#pragma unroll
    for (int i = 0; i < 10; ++i) {
      if (i == DRAIN_G) wait_vm<0>();
      bar();
#pragma unroll
      for (int k = 0; k < 10; ++k) {
        if (i > 0) accP[k >> 1][k & 1] = mfma(xr[2 * i - 1][k & 1], pb[1][k >> 1], accP[k >> 1][k & 1]);
        if (k & 1) rdw(i, 0, k >> 1);
        else if (k < 8) rdx(i, k >> 2, (k >> 1) & 1);
        SCHED0();
      }
      if (i < 9) {
#pragma unroll
        for (int k = 0; k < 18; ++k) {
          if (k < 2) stat(2 * i, k);
          else if (k < 12) {
            accP[(k - 2) >> 1][k & 1] = mfma(xr[2 * i][k & 1], pb[0][(k - 2) >> 1], i == 0 ? z4 : accP[(k - 2) >> 1][k & 1]);
            if (k & 1) rdw(i, 1, (k - 2) >> 1);
          } else if (k < 14) stat(2 * i, k - 10);
          else stat(2 * i + 1, k - 14);
          SCHED0();
        }
      }
    }
    LAS char *tw, *te, *tr, *tc;
    float sm[2], sq[2], rs[2];
    const float sg = id.cb >= 8 ? -1.f : 1.f;
#pragma unroll
    for (int k = 0; k < 28; ++k) {
      if (k == 18) bar();
      if (k < 8) stat(18 + (k >> 2), k & 3);
      else if (k < 18) accP[(k - 8) >> 1][k & 1] = mfma(xr[18][k & 1], pb[0][(k - 8) >> 1], accP[(k - 8) >> 1][k & 1]);
      else accP[(k - 18) >> 1][k & 1] = mfma(xr[19][k & 1], pb[1][(k - 18) >> 1], accP[(k - 18) >> 1][k & 1]);
      if (k < 5) rdw(9, 1, k);
      if (k == 5) { lane = fresh_lane(); r = lane & 15; q = lane >> 4; PIN(r); PIN(q); }
      if (k == 6) { tw = L + T_ST + (w * 32 + r) * 4; te = L + T_EK + (w * 32 + r) * 16; PIN(tw); PIN(te); }
      if (k == 7) { const bool diag = (r >> 2) == q; tw = diag ? tw : L + T_PAD; te = diag ? te : L + T_PAD; PIN(tw); PIN(te); }
      if (k == 8) { tr = L + T_ST + (w * 32 + 4 * q) * 4; tc = L + T_CS + r * 4; PIN(tr); PIN(tc); }
      if (k == 9) { fB0 = L + BRING + lane * 16; fB3 = fB0 + 61440; PIN(fB0); PIN(fB3); }
      if (k == 11) { sm[0] = accS[0][0] * (-1.0f / D); sm[1] = accS[1][0] * (-1.0f / D); PIN(sm[0]); PIN(sm[1]); }
      if (k == 12 || k == 13) { sq[k - 12] = pick4(accQ[k - 12]) * (1.0f / D); PIN(sq[k - 12]); }
      if (k == 14 || k == 15) { const int t = k - 14; sq[t] = fmaxf(sq[t] - sm[t] * sm[t], 0.f) + 1e-5f; rs[t] = __builtin_amdgcn_rsqf(sq[t]); PIN(sq[t]); PIN(rs[t]); }
      if (k == 16 || k == 17) {
        const int t = k - 16;
        const float v = rs[t] * sg;
        *(LAS float*)(tw + t * 64) = v; *(LAS float*)(tw + 512 + t * 64) = v * sm[t];
      }
      if (k >= 18 && k < 24) rdg((k - 18) >> 1, k & 1);
      if (k == 24 || k == 25) {
        st[2 * k - 48] = *(LAS f32x4*)(tr + (k - 24) * 512); st[2 * k - 47] = *(LAS f32x4*)(tr + (k - 24) * 512 + 64);
      }
      if (k == 23) {
#pragma unroll
        for (int g = 0; g < 5; ++g) cs[g] = *(LAS float*)(tc + g * 64);
      }
      if (k == 26 || k == 27) {
        const int t = k - 26;
        *(LAS f32x4*)(te + t * 256) = f32x4{sm[t], -2.302208198f * rs[t], -0.1029432397f * rs[t] * rs[t] * rs[t], sq[t]};
      }
      SCHED0();
    }
  }
  LAS char *xB0, *xB1, *tG;
  unsigned abBase, hbase;

  f32x4 accH[2][4];
  float gv[8], uv[8], wv[8];
  f32x4 cgv, cuv, ek[2];
  u32x4 ho;
  auto h_ins = [&](int c, int o) __attribute__((always_inline)) {
    if (o < 16) {
      const int x = o >> 1, t = x >> 2, e = x & 3;
      if (o & 1) { uv[x] = accH[c & 1][2 + t][e] + ek[t][0] * cuv[e]; PIN(uv[x]); } else { gv[x] = accH[c & 1][t][e] + ek[t][0] * cgv[e]; PIN(gv[x]); }
    } else if (o < 40) {
      const int x = o & 7, t = x >> 2;
      wv[x] = o < 24 ? gv[x] * gv[x] : o < 32 ? ek[t][1] + ek[t][2] * wv[x] : gv[x] * wv[x];
      PIN(wv[x]);
    } else if (o < 56) {
      const int x = (o - 40) >> 1;
      if (o & 1) { uv[x] *= gv[x]; PIN(uv[x]); } else { wv[x] = __builtin_amdgcn_exp2f(wv[x]); PIN(wv[x]); }
    } else if (o < 64) {
      const int x = o & 7, t = x >> 2;
      wv[x] = wv[x] * ek[t][3] + ek[t][3]; PIN(wv[x]);
    } else if (o < 82) {
      const int x = o < 80 ? (o - 64) >> 1 : o - 72;
      if (o < 80 && !(o & 1)) { wv[x] = __builtin_amdgcn_rcpf(wv[x]); PIN(wv[x]); }
      else if (x >= 2) { uv[x - 2] *= wv[x - 2]; PIN(uv[x - 2]); }
      else if (c < 5) { if (x == 0) cgv = *(LAS f32x4*)(tG + (c + 1) * 64); else cuv = *(LAS f32x4*)(tG + 384 + (c + 1) * 64); }
    } else if (o < 86) {
      unsigned pk = pack2(uv[2 * (o - 82)], uv[2 * (o - 82) + 1]); PIN(pk); ho[o - 82] = pk;
    } else __builtin_amdgcn_raw_buffer_store_b128(ho, rws, hbase + c * 2048, 0, AUX_SC1);
  };
  float av[8];
  auto a_ins = [&](int o) __attribute__((always_inline)) {
    if (o == 0) { abBase = sAb + lane * 16; hbase = sH + lane * 16; PIN(abBase); PIN(hbase); }
    else if (o == 1) { xB0 = L + ARING + w * 4096 + (r * 128 + ((q ^ (r >> 1)) & 7) * 16); PIN(xB0); }
    else if (o == 2) { xB1 = (LAS char*)((size_t)xB0 ^ 64); tG = L + T_CG + q * 16; PIN(xB1); PIN(tG); }
    else if (o < 108) {
      const int gg = (o - 3) / 21, m = (o - 3) % 21, x = m & 7, t = x >> 2, e = x & 3;
      if (m < 8) { av[x] = st[2 + t][e] * cs[gg]; PIN(av[x]); }
      else if (m < 16) { av[x] = accP[gg][t][e] * st[t][e] + av[x]; PIN(av[x]); }
      else if (m < 20) { unsigned pk = pack2(av[2 * (m - 16)], av[2 * (m - 16) + 1]); PIN(pk); ho[m - 16] = pk; }
      else __builtin_amdgcn_raw_buffer_store_b128(ho, rws, abBase + gg * 1024, 0, AUX_SC1);
    } else if (o < 110) ek[o - 108] = *(LAS f32x4*)(L + T_EK + (w * 32 + (o - 108) * 16 + r) * 16);
  };
  const int wm = w >> 1, wn = w & 1;
  LAS char *aBase0, *aBase1, *yBase;
  bf16x8 xa0[4], xa1[4], yb0[5], yb1[5];
  sfor<0, 120>([&](auto gi) __attribute__((always_inline)) {
    constexpr int g = decltype(gi)::v, c = g / 20, s = g % 20;
    if (g == 110) {
      const int l2 = fresh_lane(), r2 = l2 & 15, oK0 = r2 * 128 + (((l2 >> 4) ^ (r2 >> 1)) & 7) * 16;
      aBase0 = L + ARING + wm * 8192 + oK0; aBase1 = L + ARING + wm * 8192 + (oK0 ^ 64); yBase = L + BRING + wn * 5120 + l2 * 16;
    }
    if (g % 10 == 8) {
      if (g == 38) wait_vm<first_op<false>(72) == H_OPS ? 1 : 0>();
      bar();
    }
    sfor<0, 4>([&](auto ki) __attribute__((always_inline)) {
      constexpr int k = decltype(ki)::v;
      accH[c & 1][k] = mfma(wb[g & 3][k >> 1], xr[s][k & 1], s == 0 ? z4 : accH[c & 1][k]);
      SCHED0();
      if (g >= 118) {
        const int n = (g - 118) * 4 + k;
        if (n < 4) xa0[n] = ldsr(aBase0 + 2 * 16384 + n * 2048); else yb0[n - 4] = ldsr(yBase + 2 * 20480 + (n - 4) * 1024);
      } else if (g % 10 == 8) rdg(g + 2 + (k >> 1), k & 1);
      else if (g % 10 != 7 && !(k & 1)) rdg(g + 3, k >> 1);
      if (c == 0) {
        if (s >= 4 && s < 16 && k == 1) {
          const int m = s - 4;
          xr[12 + (m >> 1)][m & 1] = ldsr(((m & 2) ? xB1 : xB0) + ((6 + (m >> 2)) & 3) * 16384 + (m & 1) * 2048);
        }
        if (s == 19 && k == 1) cgv = *(LAS f32x4*)(tG);
        if (s == 19 && k == 3) cuv = *(LAS f32x4*)(tG + 384);
      }
      sfor<first_op<c == 0>(4 * s + k), first_op<c == 0>(4 * s + k + 1)>([&](auto oi) __attribute__((always_inline)) {
        if constexpr (c == 0) a_ins(decltype(oi)::v); else h_ins(c - 1, decltype(oi)::v);
      });
      SCHED0();
    });
  });

  f32x4 accY[5][4];
  LAS char* const yBase3 = yBase + 61440;
  auto y_stage = [&](auto sli, auto h0i) __attribute__((always_inline)) {
    constexpr int sl = decltype(sli)::v, h0 = decltype(h0i)::v;
    LAS char* a0 = aBase0 + sl * 16384; LAS char* a1 = aBase1 + sl * 16384;
    LAS char* b = sl == 3 ? yBase3 : yBase + sl * 20480;
    bar();
    sfor<0, 40>([&](auto ki) __attribute__((always_inline)) {
      constexpr int k = decltype(ki)::v;
      if constexpr (k < 20) accY[k >> 2][k & 3] = mfma(yb1[k >> 2], xa1[k & 3], accY[k >> 2][k & 3]);
      else accY[(k - 20) >> 2][k & 3] = mfma(yb0[(k - 20) >> 2], xa0[k & 3], accY[(k - 20) >> 2][k & 3]);
      SCHED0();
      if constexpr (k < 8 && !(k & 1)) xa0[k >> 1] = ldsr(a0 + (k >> 1) * 2048);
      else if constexpr (k < 18 && !(k & 1)) yb0[(k - 8) >> 1] = ldsr(b + ((k - 8) >> 1) * 1024);
      else if constexpr (k == 18) yb1[0] = ldsr(b + 10240);
      else if constexpr (k >= 19 && k < 31 && k % 3 == 1) yb1[(k - 16) / 3] = ldsr(b + 10240 + ((k - 16) / 3) * 1024);
      else if constexpr (k >= 21 && k < 31 && k % 3 == 0) xa1[(k - 21) / 3] = ldsr(a1 + ((k - 21) / 3) * 2048);
      if constexpr (h0 + k < H_OPS) h_ins(5, h0 + k);
      SCHED0();
    });
  };
  sfor<0, 20>([&](auto ki) __attribute__((always_inline)) {
    constexpr int k = decltype(ki)::v;
    if constexpr (k == 0) yb0[4] = ldsr(yBase + 2 * 20480 + 4096);
    accY[k >> 2][k & 3] = mfma(yb0[k >> 2], xa0[k & 3], z4);
    if constexpr (k < 4) xa1[k] = ldsr(aBase1 + 2 * 16384 + k * 2048);
    else if constexpr (k < 9) yb1[k - 4] = ldsr(yBase + 2 * 20480 + 10240 + (k - 4) * 1024);
    else { h_ins(5, 2 * (k - 9)); h_ins(5, 2 * (k - 9) + 1); }
    SCHED0();
  });
  y_stage(IC<3>{}, IC<22>{}); y_stage(IC<0>{}, IC<62>{}); y_stage(IC<1>{}, IC<99>{});
#pragma nounroll
  for (int i = 0; i < 3; ++i) {
    y_stage(IC<2>{}, IC<99>{}); y_stage(IC<3>{}, IC<99>{});
    wait_vm<0>();
    y_stage(IC<0>{}, IC<99>{}); y_stage(IC<1>{}, IC<99>{});
  }

  lane = fresh_lane(); r = lane & 15; q = lane >> 4;
  f32x4 accD[5][2];
#pragma unroll
  for (int g = 0; g < 5; ++g) { accD[g][0] = z4; accD[g][1] = z4; }
  LAS char* const hB = L + ARING + wm * 1024 + wn * 8192 + lane * 16 + wn * 8;
  LAS char* const dB = fB0 + wn * 10240;
  bf16x8 hh[2][2], dw[2][5];
  auto d_rd = [&](LAS char* a, LAS char* a2, LAS char* b, int ks, int m) {
    if (m < 2) hh[ks][m] = cat(*(volatile LAS bf16x4*)((m ? a2 : a) + ks * 4096), *(volatile LAS bf16x4*)((m ? a2 : a) + ks * 4096 + 2048));
    else dw[ks][m - 2] = ldsr(b + ks * 5120 + (m - 2) * 1024);
  };
  auto d_mm = [&](int ks, int k) { accD[k >> 1][k & 1] = mfma(dw[ks][k >> 1], hh[ks][k & 1], accD[k >> 1][k & 1]); };
  const unsigned pbase = sP + lane * 16;
  o0 = out_off(w * 64 + lane); o1 = out_off(w * 64 + lane + 512);
  LAS char* xp = L + X_TILE + (wm * 32 + 8 * (r >> 2) + 4 * wn + (r & 3)) * 160 + q * 8;
  PIN(xp);
  auto p_store = [&](int u) {
    u32x4 o[5];
#pragma unroll
    for (int g = 0; g < 5; ++g) {
      const f32x4 a = accY[g][2 * u], b = accY[g][2 * u + 1];
      o[g] = u32x4{pack2(a[0], a[1]), pack2(a[2], a[3]), pack2(b[0], b[1]), pack2(b[2], b[3])};
    }
    if (w != id.kq) {
#pragma unroll
      for (int g = 0; g < 5; ++g) __builtin_amdgcn_raw_buffer_store_b128(o[g], rws, pbase + (u * 5 + g) * 1024, 0, AUX_SC1);
    } else {
#pragma unroll
      for (int g = 0; g < 5; ++g) *(LAS u32x4*)(L + T_BIG + (u * 5 + g) * 1024 + lane * 16) = o[g];
    }
  };
  auto d_second = [&](LAS char* a, LAS char* a2, LAS char* b) {
#pragma unroll
    for (int k = 0; k < 10; ++k) {
      d_mm(0, k);
      if (k < 2) d_rd(a, a2, b, 1, k); else if (k < 4) d_rd(a, a2, b, 1, k + 3);
      SCHED0();
    }
  };
  auto d_stage = [&](int i) {
    const int sl = i == 11 ? 2 : (i + 2) & 3;
    LAS char* a = hB + sl * 16384; LAS char* a2 = a + (8 - 16 * wn); LAS char* b = dB + sl * 20480;
    PIN(a); PIN(a2); PIN(b);
    bar();
#pragma unroll
    for (int k = 0; k < 10; ++k) {
      d_mm(1, k);
      if (k < 7) d_rd(a, a2, b, 0, k); else d_rd(a, a2, b, 1, k - 5);
      SCHED0();
    }
    d_second(a, a2, b);
  };
  {
    LAS char* a = hB + 2 * 16384; LAS char* a2 = a + (8 - 16 * wn); LAS char* b = dB + 2 * 20480;
    PIN(a); PIN(a2); PIN(b);
    bar();
#pragma unroll
    for (int k = 0; k < 20; ++k) {
      accY[k >> 2][k & 3] = mfma(yb1[k >> 2], xa1[k & 3], accY[k >> 2][k & 3]);
      if (k < 7) d_rd(a, a2, b, 0, k); else if (k < 10) d_rd(a, a2, b, 1, k - 5);
      SCHED0();
    }
    d_second(a, a2, b);
  }
  p_store(0);
  p_store(1);
#pragma nounroll
  for (int i = 1; i < 12; ++i) {
    if (i == DRAIN_P - 38) wait_vm<0>();
    d_stage(i);
  }
#pragma unroll
  for (int k = 0; k < 10; ++k) {
    d_mm(1, k < 5 ? 2 * k + 1 : 2 * (9 - k));
    if (k >= 5) *(LAS f32x4*)(fB0 + ((w ^ 1) * 5 + k - 5) * 1024) = accD[k - 5][1];
    SCHED0();
  }
  LAS char* dp = fB0 + w * 5120;
  *(LAS f32x4*)(L + ARING + w * 2048 + 1024 + lane * 16) = accD[4][0];
  *(LAS f32x4*)(L + ARING + w * 2048 + lane * 16) = accD[3][0];
  PIN(dp);
  bar();
  f32x4 dk[3], ys[3];
#pragma unroll
  for (int g = 0; g < 3; ++g) dk[g] = *(LAS f32x4*)(dp + g * 1024);
#pragma unroll
  for (int g = 0; g < 3; ++g) ys[g] = *(LAS f32x4*)(dp + 61440 + g * 1024);
  const f32x4 own[3] = {accD[0][0], accD[1][0], accD[2][0]};
  finish<3>(dk, own, ys, xp);
}

__device__ __forceinline__ void aux_tail(const Params& p, LAS char* L, int t, int lane, bf16x8 pickA) {
  const int po = (t >> 1) * 5120 + lane * 16;
  bf16x8 y[4][5];
#pragma unroll
  for (int g = 0; g < 5; ++g) {
    y[0][g] = ldsr(L + T_BIG + po + g * 1024);
    y[1][g] = ldsr(L + BRING + 20480 + po + g * 1024);
    y[2][g] = ldsr(L + BRING + 20480 + 10240 + po + g * 1024);
    y[3][g] = ldsr(L + ARING + 16384 + po + g * 1024);
    if (g == YSPLIT) { wait_vm<3>(); bar(); if (t == 3) { STAMP(49); } }
  }
  SCHED0();
  LAS char* dp = L + ARING + t * 2048 + lane * 16;
  f32x4 ys[2];
#pragma unroll
  for (int g = 0; g < 5; ++g) {
    f32x4 sum = {0.f, 0.f, 0.f, 0.f};
#pragma unroll
    for (int k = 0; k < 4; ++k) sum = mfma(pickA, y[k][g], sum);
    if (g < 3) *(LAS f32x4*)(L + BRING + 61440 + (t * 5 + g) * 1024 + lane * 16) = sum;
    else ys[g - 3] = sum;
  }
  const int r = lane & 15;
  LAS char* xp = L + X_TILE + ((t >> 1) * 32 + 8 * (r >> 2) + 4 * (t & 1) + (r & 3)) * 160 + (lane >> 4) * 8 + 96;
  LAS char* dq = L + BRING + t * 5120 + 3072 + lane * 16;
  PIN(xp); PIN(dp); PIN(dq); PIN(ys[0]); PIN(ys[1]);
  wait_vm<0>();
  bar();
  if (t == 3) { STAMP(50); }
  f32x4 dk[2], own[2];
#pragma unroll
  for (int g = 0; g < 2; ++g) { dk[g] = *(LAS f32x4*)(dq + g * 1024); own[g] = *(LAS f32x4*)(dp + g * 1024); }
  finish<2>(dk, own, ys, xp);
}

__device__ __forceinline__ void st_flag(unsigned char* ws, unsigned off, u64 v, int lane) {
  if (lane == 0) __hip_atomic_store((GAS u64*)(ws + off), v, __ATOMIC_RELAXED, __HIP_MEMORY_SCOPE_AGENT);
}
__device__ __forceinline__ void wait_flag(const unsigned char* ws, unsigned off, u64 tag, u64 seen) {
  if (!__all(seen == tag)) {
#pragma nounroll
    for (int spin = 0; spin < (1 << 16); ++spin)
      if (__all(ld_flag(ws, off) == tag)) break;
  }
  __builtin_amdgcn_fence(__ATOMIC_ACQUIRE, "wavefront");
}
template <int N, class Fn>
__device__ __forceinline__ void wait_tags(u32x4 (&g)[N], unsigned tlo, unsigned thi, Fn&& reload) {
  auto good = [&]() {
    unsigned bad = 0;
#pragma unroll
    for (int k = 0; k < N; ++k) bad |= (g[k][1] ^ tlo) | (g[k][3] ^ thi);
    return __all(bad == 0);
  };
  if (!good()) {
#pragma nounroll
    for (int spin = 0; spin < (1 << 16); ++spin) {
      asm volatile("" ::: "memory");
      reload();
      if (good()) break;
    }
  }
}
struct First { cptr base; unsigned v0, v1, v2, v3; int kc; };
__device__ __forceinline__ void first_offsets(First& f, int w, int lane) {
  const bool isx = w == 4 || w == 5;
  const unsigned row = isx ? 8 * (lane >> 5) + ((lane >> 3) & 3) : lane >> 3, stride = w < 4 ? 3072 : 1280;
  const unsigned c0 = ((lane & 7) ^ (w < 4 ? 0 : lane >> 4)) * 16, c1 = c0 ^ (w < 4 ? 0 : 64);
  f.v0 = row * stride + c0;
  f.v1 = row * stride + c1 + ((isx ? 16 : 8) * stride - 1024);
  f.v2 = f.v0 + ((isx ? 4 : 16) * stride - 2048);
  f.v3 = f.v1 + ((isx ? 4 : 16) * stride - 2048);
}
#define SPIN(x) asm volatile("" : "+s"(x))
__device__ __forceinline__ void p1_tile(First& f, unsigned lb, int ld, int j, bool barrier) {
  cptr a = f.base + f.kc * 128, a2 = a + 32 * 1280;
  unsigned l = ld < 2 ? lb + ARING + (j & 3) * 16384 + ld * 8192 : lb + BRING + ((j & 3) ? (j & 3) * 20480 : 10240);
  SPIN(a); SPIN(a2); SPIN(l);
  if (barrier) bar();
  dma4(a, f.v0, f.v1, f.v2, f.v3, l);
  dma4(a2, f.v0, f.v1, f.v2, f.v3, l + 4096);
  if (ld == 2) dma2(a + 64 * 1280, f.v0, f.v1, l + 8192);
  f.kc = f.kc == 9 ? 0 : f.kc + 1;
}
__device__ __forceinline__ void loader(const Params& p, LAS char* L, unsigned lb, const Ids& id, int ld, int lane, First& f) {
  const int b = blockIdx.x;
  const unsigned vl = lane * 16;
  const unsigned c0 = ((lane & 7) ^ (lane >> 4)) * 16, c1 = c0 ^ 64;
  const int rl = 8 * (lane >> 5) + ((lane >> 3) & 3);
  const int rot = ROT;
  auto w1 = [&]() { if (ld == 2) wait_vm<10>(); else wait_vm<8>(); };
  auto w2 = [&]() { if (ld == 2) wait_vm<20>(); else wait_vm<16>(); };
  p1_tile(f, lb, ld, 2, false);
  constexpr int J0 = 3;
#pragma nounroll
  for (int j = J0; j < 10; ++j) {
    w2();
    p1_tile(f, lb, ld, j, true);
  }
  const unsigned vw0 = rl * 4096 + c0, vw1 = (rl + 16) * 4096 + c1 - 1024, vw2 = (rl + 4) * 4096 + c0 - 2048, vw3 = (rl + 20) * 4096 + c1 - 3072;
  const unsigned u0 = ld ? vl : vw0, u1 = ld ? vl : vw1, u2 = ld ? vl : vw2, u3 = ld ? vl : vw3;
  const cptr bw = (cptr)(id.kq < 2 ? p.wsr : p.wsi) + (id.tt * 128 * 4096 + (id.kq & 1) * 2048);
  const int g0 = ld ? 2 * ld - 2 : 4;
  const unsigned l0 = lb + BRING + g0 * 4096, l1 = ld ? l0 + 4096 : lb + ARING, ls1 = ld ? 20480 : 16384, l2 = lb + ARING + (ld + 1) * 4096;
  const int ro = YG == 8 ? 2 * id.et + id.tt : id.et + id.tt;
  auto yk = [&](int y) { return (y & ~(YG - 1)) | ((y + ro) & (YG - 1)); };
  {
    const cptr bg = p.ws + W_GU + (unsigned)id.cb * 245760u;
    const int ka[2] = {((g0 + rot) % 10) * 4096, ((g0 + rot + 5) % 10) * 4096}, kb[2] = {((g0 + 1 + rot) % 10) * 4096, ((g0 + rot + 6) % 10) * 4096};
    if (GB == 8) { w2(); bar(); }
#pragma nounroll
    for (int j = 10; j < 22; ++j) {
      const cptr so = bg + ((j - 10) >> 1) * 40960;
      const int n = j - 16;
      cptr a0 = so + ((j & 1) ? ka[1] : ka[0]), a1 = so + ((j & 1) ? kb[1] : kb[0]), a2 = bw + (yk(n >> 2) * 128 + (n & 3) * 32 * 4096);
      unsigned la = l0 + (j & 3) * 20480, lw = lb + ARING + (((n >> 2) + 2) & 3) * 16384 + (n & 3) * 4096;
      SPIN(a0); SPIN(a1); SPIN(a2); SPIN(la); SPIN(lw);
      if (GB == 7 || j != 11) {
        if (j == 10) { if (GB == 8) wait_vm<8>(); else wait_vm<16>(); }
        else if (j == 11) wait_vm<12>();
        else if (j < 18) { if (ld == 0) wait_vm<8>(); else wait_vm<16>(); }
        else wait_vm<16>();
        bar();
      }
      dma4c(a0, vl, la);
      if (ld) dma4c(a1, vl, la + 4096);
      else if (n >= 0) dma4(a2, vw0, vw1, vw2, vw3, lw);
    }
  }
  const int c80 = id.et * 2 + (id.kq & 1);
  const cptr bh = p.ws + W_H + (unsigned)(id.tt * 2 + (id.kq >> 1)) * 196608u;
  const cptr bab = p.ws + W_ABT + ((unsigned)(((id.kq >> 1) * 4 + id.et) * 64 + (id.kq & 1) * 32) * 10240u + g0 * 4096);
  const cptr bwd = p.ws + W_WDT + (unsigned)c80 * 245760u;
  const cptr y1 = ld ? bab + 4096 : bw, y2 = bw + (ld + 1) * 32 * 4096;
  const int ym1 = ld ? 20480 : 128;
  const cptr d0 = bwd + g0 * 4096, d1 = ld ? d0 + 4096 : bh, d2 = bh + (ld + 1) * 4096;
#pragma nounroll
  for (int i = 19; i < 35; ++i) {
    const int k = yk(i - 19), sl = (i + 3) & 3;
    cptr a0 = bab + k * 20480, a1 = y1 + k * ym1, a2 = y2 + k * 128;
    unsigned la = l0 + sl * 20480, lc = l1 + sl * ls1, ld2 = l2 + sl * 16384;
    if (i < 22 && ld == 0) {
      const int n = 2 * i - 32;
      a1 = bw + (yk(n >> 2) * 128 + (n & 3) * 32 * 4096); a2 = a1 + 32 * 4096;
      lc = lb + ARING + (((n >> 2) + 2) & 3) * 16384 + (n & 3) * 4096; ld2 = lc + 4096;
    }
    SPIN(a0); SPIN(a1); SPIN(a2); SPIN(la); SPIN(lc); SPIN(ld2);
    if (i >= 24) wait_vm<24>(); else wait_vm<16>();
    bar();
    dma4c(a0, vl, la);
    dma4s(a1, u0, u1, u2, u3, lc);
    if (i >= 22 || ld == 0) dma4(a2, vw0, vw1, vw2, vw3, ld2);
  }
  const cptr bp = p.ws + W_P + ((unsigned)(((id.tt * 4 + id.et) * 4 + id.kq) * 4 * 10) * 1024u + ((id.kq + 1 + ld) & 3) * 10240);
  const unsigned lp = lb + (ld == 0 ? ARING + 16384 : BRING + 20480 + (ld - 1) * 10240);
#pragma nounroll
  for (int i = 35; i < 47; ++i) {
    const int sl = i == 46 ? 2 : (i + 3) & 3;
    const int k = DROT == 1 ? ((i - 35) & ~3) | ((i - 35 + id.et + id.tt) & 3) : DROT == 2 ? (i - 35 + 3 * id.et + (id.tt & 7)) % 12 : (i < 41 ? 0 : 6) + (i - 35 + id.et + (id.et >> 1) + (id.tt & 7)) % 6;
    cptr a0 = d0 + k * 20480, a1 = d1 + k * ls1, a2 = d2 + k * 16384;
    unsigned la = l0 + sl * 20480, lc = l1 + sl * ls1, ld2 = l2 + sl * 16384;
    SPIN(a0); SPIN(a1); SPIN(a2); SPIN(la); SPIN(lc); SPIN(ld2);
    wait_vm<24>();
    bar();
    if (i == 46) {
      dma4c(bp, vl, lp); dma4c(bp + 4096, vl, lp + 4096); dma2c(bp + 8192, vl, lp + 8192);
      wait_vm<22>();
      bar();
    }
    dma4c(a0, vl, la);
    dma4c(a1, vl, lc);
    dma4c(a2, vl, ld2);
  }
}

__device__ __forceinline__ void helper(const Params& p, LAS char* L, unsigned lb, const Ids& id, int lane, u64 pc) {
  const int b = blockIdx.x;
  const __amdgpu_buffer_rsrc_t rws = __builtin_amdgcn_make_buffer_rsrc((void*)p.ws, 0, (int)W_END, 0x00020000);
  STAMP(60);
  __builtin_amdgcn_s_setprio(3);
  const u64 prev = ld_flag(p.ws, W_EPOCH);
  const int r8 = lane >> 3;
  u32x4 dv[10];
  const GAS char* ds = (const GAS char*)(b < 128 ? p.wdr : p.wdi) + (((b & 127) * 5 + (r8 < 5 ? r8 : 4)) * 1280 + (lane & 7) * 16);
#pragma unroll
  for (int i = 0; i < 10; ++i) dv[i] = *(const GAS u32x4*)(ds + i * 128);
  auto code_touch = [&]() {
    const unsigned off = (((b >> 3) % ((CODE_BYTES + 8191) / 8192)) * 64 + lane) * 128;
    touch((cptr)(pc & ~255ull), off < CODE_BYTES ? off : 0, lb + T_PAD);
  };
  const u64 tag = next_tag(prev);
  const unsigned tlo = (unsigned)tag, thi = (unsigned)(tag >> 32);
  {
    float t4[4] = {0.f, 0.f, 0.f, 0.f};
#pragma unroll
    for (int i = 0; i < 10; ++i)
#pragma unroll
      for (int e = 0; e < 4; ++e) t4[e] = dot_ones(dv[i][e], t4[e]);
    const unsigned sm = __builtin_bit_cast(unsigned, row8_sum((t4[0] + t4[1]) + (t4[2] + t4[3])));
    const u32x4 o = {sm, tlo, sm, thi};
    if ((lane & 7) == 0 && r8 < 5) __builtin_amdgcn_raw_buffer_store_b128(o, rws, W_CSD + (b * 8 + r8) * 16, 0, AUX_SC1);
  }
  const int pcl = (id.cb * 3) >> 1, tile = id.tt * 4 + id.et, c80 = id.et * 2 + (id.kq & 1);
  const int wk = b / 5, wc = b % 5;
  u64 f0 = 0, f1 = 0;
  unsigned fo0 = 0, fo1 = 0;
  u32x4 gr[2], cq[10];
  const int l5 = (lane & 7) < 5 ? lane & 7 : 0, go = r8 * 5 + l5;
  const unsigned gro = W_CSD + ((id.cb * 16 + r8) * 8 + l5) * 16;
  auto ld_gr = [&]() {
    gr[0] = __builtin_amdgcn_raw_buffer_load_b128(rws, gro, 0, AUX_SC1);
    gr[1] = __builtin_amdgcn_raw_buffer_load_b128(rws, gro + 1024, 0, AUX_SC1);
  };
  auto ld_cq = [&]() {
#pragma unroll
    for (int k = 0; k < 10; ++k) cq[k] = __builtin_amdgcn_raw_buffer_load_b128(rws, W_CSP + ((k * 24 + pcl) * 32 + lane) * 16, 0, AUX_SC1);
  };
  sfor<0, 48>([&](auto ic) __attribute__((always_inline)) {
    constexpr int i = decltype(ic)::v;
    if constexpr (i == GB) wait_flag(p.ws, fo0, tag, f0);
    if constexpr (i == 19 || i == 35 || i == 46) wait_flag(p.ws, fo0, tag, f0);
    if constexpr (i == 35) wait_flag(p.ws, fo1, tag, f1);
    bar();
    STAMP(i);
    if constexpr (i == 0) {
      if (b < 240 && lane < 32) {
        LAS float* cp = (LAS float*)(L + T_CP + lane * 4);
        const float u0 = (cp[0] + cp[64]) + (cp[128] + cp[192]), u1 = (cp[32] + cp[96]) + (cp[160] + cp[224]);
        const u32x4 o = {__builtin_bit_cast(unsigned, u0), tlo, __builtin_bit_cast(unsigned, u1), thi};
        __builtin_amdgcn_raw_buffer_store_b128(o, rws, W_CSP + (b * 32 + lane) * 16, 0, AUX_SC1);
      }
    }
    if constexpr (i == GB) { ld_gr(); ld_cq(); }
    if constexpr (i == DRAIN_G) st_flag(p.ws, W_FG + b * FS, tag, lane);
    if constexpr (i == GB - 2) {
      const int l20 = lane < 20 ? lane : 0;
      fo0 = W_FG + ((l20 >> 1) * 24 + pcl + (l20 & 1)) * FS;
      f0 = ld_flag(p.ws, fo0);
    }
    if constexpr (i == 9) {
      wait_tags<2>(gr, tlo, thi, ld_gr);
      *(LAS unsigned*)(L + T_CS + go * 4) = gr[0][0];
      *(LAS unsigned*)(L + T_CS + (40 + go) * 4) = gr[1][0];
    }
    if constexpr (i == 19) {
      if (b < 240) {
        const cptr sn = (cptr)p.wdn + (wk * 32 * 1280 + wc * 256);
        const unsigned vn = (lane >> 4) * 1280 + (lane & 15) * 16;
        SELNT(NT_W, dma4)(sn, vn, vn + 4096, vn + 2 * 4096, vn + 3 * 4096, lb + T_BIG);
        SELNT(NT_W, dma4)(sn + 16 * 1280, vn, vn + 4096, vn + 2 * 4096, vn + 3 * 4096, lb + T_BIG + 4096);
      }
    }
    if constexpr (i == 10) wait_tags<10>(cq, tlo, thi, ld_cq);
    if constexpr (i == 11) {
#pragma unroll
      for (int m = 0; m < 2; ++m)
#pragma unroll
        for (int h = 0; h < 2; ++h) {
          const unsigned col = (lane >> 5) * 64 + h * 32 + (lane & 31) - (id.cb & 1) * 32;
          const float v = ((asf(cq[m][2 * h]) + asf(cq[2 + m][2 * h])) + (asf(cq[4 + m][2 * h]) + asf(cq[6 + m][2 * h]))) + asf(cq[8 + m][2 * h]);
          *(LAS float*)(col < 96 ? L + T_CG + (m * 96 + col) * 4 : L + T_PAD) = v;
        }
    }
    if constexpr (i >= 22 && i < 26) {
      if constexpr (i == 22) wait_vm<0>();
      if (b < 240) {
        LAS char* a = L + T_BIG + (4 * (lane >> 4) + ((lane & 15) >> 2)) * 256 + 8 * (lane & 3);
#pragma unroll
        for (int c = (i - 22) * 2; c < (i - 22) * 2 + 2; ++c) {
          const bf16x8 f = cat(ldstr(a + c * 32), ldstr(a + 4096 + c * 32));
          const int eg = wc * 8 + c, cc = eg / 5, g5 = eg - cc * 5;
          __builtin_amdgcn_raw_buffer_store_b128(__builtin_bit_cast(u32x4, f), rws, lane * 16 + W_WDT + ((cc * 48 + wk) * 5 + g5) * 1024, 0, AUX_SC1);
        }
      }
    }
    if constexpr (i == 14) st_flag(p.ws, W_FB + (id.rb * 16 + id.cb) * FS, tag, lane);
    if constexpr (CT2 && i == 16) code_touch();
    if constexpr (i == 17) {
      fo0 = W_FB + ((8 * (id.kq & 1) + (lane & 7)) * 16 + 8 * (id.kq >> 1) + 2 * id.et + ((lane >> 3) & 1)) * FS;
      f0 = ld_flag(p.ws, fo0);
    }
    if constexpr (i == 28) { wait_vm<0>(); st_flag(p.ws, W_FH + b * FS, tag, lane); }
    if constexpr (i == 32) {
      const int hb = (b & 0x39) | ((lane & 3) << 1) | ((lane & 12) << 4);
      fo0 = W_FH + (lane < 48 ? lane * 5 + (c80 * 5) / 8 : hb) * FS;
      fo1 = W_FH + (lane < 48 ? lane * 5 + (c80 * 5 + 4) / 8 : hb) * FS;
      f0 = ld_flag(p.ws, fo0); f1 = ld_flag(p.ws, fo1);
    }
    if constexpr (i == 35) { if (b == 0) st_flag(p.ws, W_EPOCH, tag, lane); }
    if constexpr (i == DRAIN_P) st_flag(p.ws, W_FP + (tile * 4 + id.kq) * FS, tag, lane);
    if constexpr (i == 44) { fo0 = W_FP + (tile * 4 + (lane & 3)) * FS; f0 = ld_flag(p.ws, fo0); }
  });
  __builtin_amdgcn_s_waitcnt(0x0F70);
}

__global__ __launch_bounds__(NTHR, 2) void fused_kernel(Params p) {
  extern __shared__ __attribute__((aligned(16))) char smem[];
  u64 pc;
  asm volatile("s_getpc_b64 %0" : "=s"(pc));
  LAS char* L = (LAS char*)smem;
  const unsigned lb = (unsigned)(size_t)L;
  int lane = threadIdx.x & 63;
  const int w = __builtin_amdgcn_readfirstlane(threadIdx.x >> 6);
  const int b = blockIdx.x, g = b & 7, rr = b >> 3;
  if (w == 7) {
    const unsigned off = ((rr % ((CODE_BYTES + 8191) / 8192)) * 64 + lane) * 128;
    touch((cptr)(pc & ~255ull), off < CODE_BYTES ? off : 0, lb + T_PAD);
  }
  Ids id;
  id.rb = (g & 1) * 8 + (rr & 7);
  id.cb = (g >> 1) * 4 + (rr >> 3);
  id.tt = (g & 1) * 8 + (rr & 7);
  id.et = rr >> 3;
  id.kq = g >> 1;
  First f;
  {
    const int pc48 = b % 48, pr = b / 48;
    const bool isx = w == 4 || w == 5;
    first_offsets(f, w, lane);
    f.base = w < 4 ? (cptr)(pc48 < 24 ? p.wg : p.wu) + ((pr * 128 + w * 32) * 3072 + (pc48 % 24) * 128)
                   : isx ? (cptr)p.x + (id.rb * 128 + (w - 4) * 64) * 1280 : (cptr)(id.cb < 8 ? p.wdr : p.wdi) + (id.cb & 7) * 80 * 1280;
    f.kc = ROT % 10;
    if (w < 4) {
      if (b < 240) SELNT(NT_G, dma4)(f.base, f.v0, f.v1, f.v2, f.v3, lb + BRING + (w ? w * 20480 + 10240 : 0));
    } else if (w < 7) {
      p1_tile(f, lb, w - 4, 0, false);
      if (PRO != 5 || w == 6) p1_tile(f, lb, w - 4, 1, false);
    }
  }
  unsigned o0, o1;
  if (w >= 4) {
    const int ld = w - 4;
    lane = fresh_lane();
    first_offsets(f, w, lane);
    if (ld < 3) loader(p, L, lb, id, ld, lane, f);
    else helper(p, L, lb, id, lane, pc);
    const cptr so = uni((cptr)p.x + ((id.tt * 128 + (id.kq >> 1) * 64) * 1280 + (id.et * 2 + (id.kq & 1)) * 160));
    const int n0 = ld * 64 + lane, n1 = n0 + 256, n2 = ld < 2 ? n0 + 512 : n0;
    unsigned x0 = (n0 / 10) * 1120 + n0 * 16, x1 = (n1 / 10) * 1120 + n1 * 16, x2 = (n2 / 10) * 1120 + n2 * 16;
    const unsigned one = (0x3f80u << ((lane & 1) * 16)) & -(unsigned)(((lane & 15) >> 2) == (lane >> 4));
    const int od = (ld & 1) * 2 + ((lane >> 1) & 1);
    u32x4 pa = {one & -(unsigned)(od == 0), one & -(unsigned)(od == 1), one & -(unsigned)(od == 2), one & -(unsigned)(od == 3)};
    PIN(x0); PIN(x1); PIN(x2); PIN(pa);
    __builtin_amdgcn_s_setprio(0);
    wait_vm<12>();
    bar();
    if (w == 7) { STAMP(48); }
    dma1(so, x0, lb + X_TILE + ld * 1024); dma1(so, x1, lb + X_TILE + (ld + 4) * 1024); dma1(so, x2, lb + X_TILE + (ld < 2 ? ld + 8 : ld) * 1024);
    aux_tail(p, L, ld, lane, __builtin_bit_cast(bf16x8, pa));
    o0 = x1; o1 = 0;
  } else compute(p, L, id, w, o0, o1);
  const int tid = w * 64 + fresh_lane();
  const __amdgpu_buffer_rsrc_t ro = __builtin_amdgcn_make_buffer_rsrc((char*)p.out + ((id.tt * 128 + (id.kq >> 1) * 64) * 1280 + (id.et * 160 + (id.kq & 1) * 80) * 2), 0, 63 * 1280 + 160, 0x00020000);
  bar();
  const u32x4 v0 = *(LAS u32x4*)(L + X_TILE + tid * 16);
  if (w < 2) {
    const u32x4 v1 = *(LAS u32x4*)(L + X_TILE + 8192 + tid * 16);
    __builtin_amdgcn_raw_buffer_store_b128(v1, ro, o1, 0, AUX_SC1);
  }
  __builtin_amdgcn_raw_buffer_store_b128(v0, ro, o0, 0, AUX_SC1);
}
}

extern "C" void kernel_launch(void* const* d_in, const int* in_sizes, int n_in, void* d_out, int out_size, void* d_ws, size_t ws_size,
                              hipStream_t stream) {
  Params p;
  p.x = (const u16*)d_in[0];
  p.wsr = (const u16*)d_in[1];
  p.wsi = (const u16*)d_in[2];
  p.wdr = (const u16*)d_in[3];
  p.wdi = (const u16*)d_in[4];
  p.wg = (const u16*)d_in[6];
  p.wu = (const u16*)d_in[7];
  p.wdn = (const u16*)d_in[8];
  p.out = (u16*)d_out;
  p.ws = (unsigned char*)d_ws;
  (void)hipFuncSetAttribute((const void*)fused_kernel, hipFuncAttributeMaxDynamicSharedMemorySize, LDS_BYTES);
  fused_kernel<<<256, NTHR, LDS_BYTES, stream>>>(p);
}
```

```cpp
#include <hip/hip_runtime.h>
#include <hip/hip_bf16.h>

typedef unsigned short u16;
typedef unsigned long long u64;
typedef short bf16x8 __attribute__((ext_vector_type(8)));
typedef short bf16x4 __attribute__((ext_vector_type(4)));
typedef float f32x4 __attribute__((ext_vector_type(4)));
typedef float f32x2 __attribute__((ext_vector_type(2)));
typedef unsigned u32x4 __attribute__((ext_vector_type(4)));
typedef unsigned u32x2 __attribute__((ext_vector_type(2)));
typedef __bf16 bf2v __attribute__((ext_vector_type(2)));
#define LAS __attribute__((address_space(3)))
#define GAS __attribute__((address_space(1)))

namespace {
constexpr int D = 640;
constexpr int NTHR = 512;
constexpr int ARING = 0;
constexpr int BRING = 65536;
constexpr int TAB = 147456;
constexpr int T_ST = TAB;
constexpr int T_CS = TAB + 1024;
constexpr int T_CP = TAB + 1344;
constexpr int T_CG = TAB + 2368;
constexpr int T_PAD = TAB + 3328;
constexpr int T_EK = TAB + 12288;
constexpr int T_BIG = TAB + 4096;
constexpr int X_TILE = ARING + 3 * 16384;
constexpr int LDS_BYTES = 163840;
constexpr unsigned W_EPOCH = 0;
constexpr unsigned FS = 128;
constexpr unsigned W_FB = 4096, W_FH = W_FB + 256 * FS, W_FP = W_FH + 256 * FS, W_FG = W_FP + 256 * FS;
constexpr unsigned W_CSD = W_FG + 256 * FS;
constexpr unsigned W_ST = W_CSD + 256 * 128;
constexpr unsigned W_CSP = W_ST + 2048 * 16;
constexpr unsigned W_ABT = 524288;
static_assert(W_CSP + 240 * 512 <= W_ABT, "workspace map");
constexpr unsigned W_H = W_ABT + 2u * 4 * 64 * 10 * 1024;
constexpr unsigned W_WDT = W_H + 32u * 96 * 2 * 1024;
constexpr unsigned W_GU = W_WDT + 8u * 48 * 5 * 1024;
constexpr unsigned W_P = W_GU + 16u * 20 * 12 * 1024;
constexpr unsigned W_END = W_P + 256u * 4 * 10 * 1024;
constexpr int AUX_SC1 = 16;
constexpr int DRAIN_G = 2;
constexpr int GB = 7;
constexpr int DRAIN_P = 40;

struct Params {
  const u16 *x, *wsr, *wsi, *wdr, *wdi, *wg, *wu, *wdn;
  u16* out;
  unsigned char* ws;
};

typedef const unsigned char* cptr;
__device__ __forceinline__ cptr uni(cptr a) {
  const u64 v = (u64)a;
  return (cptr)(((u64)(unsigned)__builtin_amdgcn_readfirstlane((int)(v >> 32)) << 32) | (unsigned)__builtin_amdgcn_readfirstlane((int)v));
}
__device__ __forceinline__ void dma1(cptr base, unsigned v, unsigned lds) {
  asm volatile("s_mov_b32 m0, %2\n\ts_nop 0\n\tglobal_load_lds_dwordx4 %0, %1" ::"v"(v), "s"(base), "s"(lds) : "memory");
}
__device__ __forceinline__ void dma2(cptr base, unsigned v0, unsigned v1, unsigned lds) {
  asm volatile("s_mov_b32 m0, %3\n\ts_nop 0\n\tglobal_load_lds_dwordx4 %0, %2\n\tglobal_load_lds_dwordx4 %1, %2 offset:1024"
               ::"v"(v0), "v"(v1), "s"(base), "s"(lds) : "memory");
}
#define DMA4(POL) \
__device__ __forceinline__ void dma4##POL(cptr base, unsigned v0, unsigned v1, unsigned v2, unsigned v3, unsigned lds) { \
  asm volatile("s_mov_b32 m0, %5\n\ts_nop 0\n\tglobal_load_lds_dwordx4 %0, %4 " #POL "\n\tglobal_load_lds_dwordx4 %1, %4 offset:1024 " #POL "\n\t" \
               "global_load_lds_dwordx4 %2, %4 offset:2048 " #POL "\n\tglobal_load_lds_dwordx4 %3, %4 offset:3072 " #POL \
               ::"v"(v0), "v"(v1), "v"(v2), "v"(v3), "s"(base), "s"(lds) : "memory"); \
}
DMA4()
DMA4(nt)
__device__ __forceinline__ void touch(cptr base, unsigned voff, unsigned pad) {
  asm volatile("s_mov_b32 m0, %2\n\ts_nop 0\n\tglobal_load_lds_dword %0, %1" ::"v"(voff), "s"(base), "s"(pad) : "memory");
}
template <int N> __device__ __forceinline__ void wait_vm() { asm volatile("s_waitcnt vmcnt(%0)" ::"i"(N) : "memory"); }
#define SCHED0() __builtin_amdgcn_sched_barrier(0)
__device__ __forceinline__ void bar() {
  SCHED0();
  __builtin_amdgcn_s_waitcnt(0xC07F);
  asm volatile("s_barrier" ::: "memory");
  SCHED0();
}
__device__ __forceinline__ f32x4 mfma(bf16x8 a, bf16x8 b, f32x4 c) { return __builtin_amdgcn_mfma_f32_16x16x32_bf16(a, b, c, 0, 0, 0); }
__device__ __forceinline__ bf16x8 ldsr(LAS char* p) { return *(LAS bf16x8*)p; }
__device__ __forceinline__ bf16x4 ldstr(LAS char* p) { return __builtin_amdgcn_ds_read_tr16_b64_v4i16((LAS bf16x4*)p); }
__device__ __forceinline__ bf16x8 cat(bf16x4 a, bf16x4 b) { return __builtin_shufflevector(a, b, 0, 1, 2, 3, 4, 5, 6, 7); }
__device__ __forceinline__ unsigned pack2(float a, float b) {
  f32x2 v = {a, b};
  bf2v r = __builtin_convertvector(v, bf2v);
  return __builtin_bit_cast(unsigned, r);
}
__device__ __forceinline__ float asf(unsigned v) { return __builtin_bit_cast(float, v); }
__device__ __forceinline__ float bflo(unsigned v) { return __builtin_bit_cast(float, v << 16); }
__device__ __forceinline__ float bfhi(unsigned v) { return __builtin_bit_cast(float, v & 0xffff0000u); }
template <int CTRL> __device__ __forceinline__ float dpp_add(float v) {
  return v + __builtin_bit_cast(float, __builtin_amdgcn_update_dpp(0, __builtin_bit_cast(int, v), CTRL, 0xf, 0xf, true));
}
__device__ __forceinline__ float row16_sum(float v) {
  v = dpp_add<0x128>(v); v = dpp_add<0x124>(v); v = dpp_add<0x122>(v); v = dpp_add<0x121>(v);
  return v;
}
__device__ __forceinline__ float dot_ones(unsigned v, float acc) {
  return __builtin_amdgcn_fdot2_f32_bf16(__builtin_bit_cast(bf2v, v), __builtin_bit_cast(bf2v, 0x3f803f80u), acc, false);
}
__device__ __forceinline__ u64 ld_flag(const unsigned char* ws, unsigned off) {
  return __hip_atomic_load((GAS u64*)(ws + off), __ATOMIC_RELAXED, __HIP_MEMORY_SCOPE_AGENT);
}
__device__ __forceinline__ u64 next_tag(u64 prev) { return prev * 6364136223846793005ull + 1442695040888963407ull; }

struct Ids {
  int rb, cb, tt, et, kq, rot;
};

struct Code { u64 pc; unsigned len; };
__device__ __forceinline__ void code_touch(const Code& c, unsigned lb, int lane) {
  const unsigned off = (((blockIdx.x >> 3) & 7) * 64 + lane) * 128;
  touch((cptr)(c.pc & ~127ull), off < c.len ? off : 0, lb + T_PAD);
}

template <int I> struct IC { static constexpr int v = I; };
template <int I, int N, class Fn> __device__ __forceinline__ void sfor(Fn&& f) {
  if constexpr (I < N) { f(IC<I>{}); sfor<I + 1, N>(f); }
}
__device__ __forceinline__ int fresh_lane() {
  int l;
  asm volatile("v_mbcnt_lo_u32_b32 %0, -1, 0\n\tv_mbcnt_hi_u32_b32 %0, -1, %0" : "=v"(l));
  return l;
}
#define PIN(x) asm volatile("" : "+v"(x))
constexpr bool gap_has_read(int gp) { return (gp >> 2) % 10 == 8 || ((gp >> 2) % 10 != 7 && !(gp & 1)); }
constexpr int H_OPS = 87;
constexpr int h_cost(int o) { return ((o >= 40 && o < 56) || (o >= 64 && o < 80)) && !(o & 1) ? 2 : 1; }
constexpr int A_OPS = 3 + 5 * 21 + 2;
constexpr int a_cost(int o) { return o < 3 || o >= 108 || (o - 3) % 21 == 20 ? 2 : 1; }
template <bool AB>
constexpr int first_op(int gp) {
  int o = 0;
  for (int x = (AB ? 1 : 3); x < gp; ++x) {
    int room = gap_has_read(x) && !((x >> 2) % 10 == 8 && (x & 1)) ? 1 : 2;
    if (AB) room = (x >= 17 && x < 65 && (x & 3) == 1) ? 1 : room + (x & 1);
    while (o < (AB ? A_OPS : H_OPS) && (AB ? a_cost(o) : h_cost(o)) <= room) { room -= AB ? a_cost(o) : h_cost(o); ++o; }
  }
  return o;
}
static_assert(first_op<false>(68) < H_OPS && first_op<false>(76) == H_OPS && first_op<true>(80) == A_OPS, "the fillers must fit into a pass");

template <int N>
__device__ __forceinline__ void finish(f32x4 (&dk)[N], const f32x4 (&d1)[N], f32x4 (&ys)[N], LAS char* xp) {
  f32x4 ex[N];
  u32x2 xv[N];
#pragma unroll
  for (int g = 0; g < N; ++g) xv[g] = *(volatile LAS u32x2*)(xp + g * 32);
  SCHED0();
#pragma unroll
  for (int g = 0; g < N; ++g) { dk[g] += d1[g]; ex[g] = dk[g] * -1.4426950409f; }
  SCHED0();
#pragma unroll
  for (int g = 0; g < N; ++g) {
#pragma unroll
    for (int e = 0; e < 4; ++e) ex[g][e] = __builtin_amdgcn_exp2f(ex[g][e]);
    dk[g] *= ys[g];
  }
  SCHED0();
#pragma unroll
  for (int g = 0; g < N; ++g) ex[g] += 1.f;
  SCHED0();
#pragma unroll
  for (int g = 0; g < N; ++g) {
#pragma unroll
    for (int e = 0; e < 4; ++e) ex[g][e] = __builtin_amdgcn_rcpf(ex[g][e]);
    ys[g] = f32x4{bflo(xv[g][0]), bfhi(xv[g][0]), bflo(xv[g][1]), bfhi(xv[g][1])};
  }
  SCHED0();
#pragma unroll
  for (int g = 0; g < N; ++g) {
    const f32x4 o = dk[g] * ex[g] + ys[g];
    *(volatile LAS u32x2*)(xp + g * 32) = u32x2{pack2(o[0], o[1]), pack2(o[2], o[3])};
  }
}

__device__ __forceinline__ unsigned out_off(int n) { unsigned o = (n / 10) * 1120 + n * 16; PIN(o); return o; }
__device__ __forceinline__ void compute(const Params& p, LAS char* L, const Ids& id, int w, unsigned& o0, unsigned& o1) {
  int lane = fresh_lane(), r = lane & 15, q = lane >> 4;
  const __amdgpu_buffer_rsrc_t rws = __builtin_amdgcn_make_buffer_rsrc((void*)p.ws, 0, (int)W_END, 0x00020000);
  const f32x4 z4 = {0.f, 0.f, 0.f, 0.f};

  unsigned sAb = W_ABT + (unsigned)((((id.cb >> 3) * 4 + ((id.cb & 7) >> 1)) * 64 + id.rb * 4 + w) * 10 + (id.cb & 1) * 5) * 1024u;
  unsigned sH = W_H + (unsigned)(((id.rb * 2 + (w >> 1)) * 96 + id.cb * 6) * 2 + (w & 1)) * 1024u;
  unsigned sP = W_P + (unsigned)((((id.tt * 4 + id.et) * 4 + w) * 4 + ((id.kq - w - 1) & 3)) * 10) * 1024u;
  asm volatile("" : "+s"(sAb), "+s"(sH), "+s"(sP));
  const int k8 = (w & 1) * 4 + q;
  u32x4 rv[5];
  {
    const GAS char* src = w < 2 ? (const GAS char*)p.x + (id.rb * 128 + id.cb * 8 + k8) * 1280
                                : (const GAS char*)(id.cb < 8 ? p.wdr : p.wdi) + ((id.cb & 7) * 80 + id.rb * 5 + (k8 < 5 ? k8 : 4)) * 1280;
#pragma unroll
    for (int i = 0; i < 5; ++i) rv[i] = *(const GAS u32x4*)(src + i * 256 + r * 16);
  }
  const u64 prev = ld_flag(p.ws, W_EPOCH + (lane >> 6));
  SCHED0();
  asm volatile("" ::: "memory");
  __builtin_amdgcn_s_waitcnt(0x0F70);
  asm volatile("" ::: "memory");
  const bf16x8 ones = {0x3f80, 0x3f80, 0x3f80, 0x3f80, 0x3f80, 0x3f80, 0x3f80, 0x3f80};
  if (blockIdx.x < 240) {
    const int pc = blockIdx.x % 48, pr = blockIdx.x / 48;
    LAS char* a = L + BRING + (w ? w * 20480 + 10240 : 0) + (8 * q + (r >> 2)) * 128 + 8 * (r & 3);
    const unsigned go = lane * 16 + W_GU + (((pc % 24) * 4 * 20 + pr * 4 + w) * 2 + pc / 24) * 1024;
    bf16x8 f[4];
#pragma unroll
    for (int c = 0; c < 4; ++c) f[c] = cat(ldstr(a + c * 32), ldstr(a + 512 + c * 32));
    f32x4 acc[4];
#pragma unroll
    for (int c = 0; c < 4; ++c) acc[c] = mfma(ones, f[c], z4);
    SCHED0();
#pragma unroll
    for (int c = 0; c < 4; ++c) __builtin_amdgcn_raw_buffer_store_b128(__builtin_bit_cast(u32x4, f[c]), rws, go + c * 40960, 0, AUX_SC1);
    SCHED0();
    float v01 = (q & 1) ? acc[1][0] : acc[0][0], v23 = (q & 1) ? acc[3][0] : acc[2][0];
    PIN(v01); PIN(v23);
    *(LAS float*)(L + T_CP + (w * 64 + lane) * 4) = (q & 2) ? v23 : v01;
  }

  {
    float t4[4] = {0.f, 0.f, 0.f, 0.f};
#pragma unroll
    for (int i = 0; i < 5; ++i)
#pragma unroll
      for (int e = 0; e < 4; ++e) t4[e] = dot_ones(rv[i][e], t4[e]);
    float o0 = row16_sum((t4[0] + t4[1]) + (t4[2] + t4[3])), o1 = o0;
    if (w < 2) {
      o0 *= 1.0f / D;
      f32x2 q2[4] = {{0.f, 0.f}, {0.f, 0.f}, {0.f, 0.f}, {0.f, 0.f}};
#pragma unroll
      for (int i = 0; i < 5; ++i)
#pragma unroll
        for (int e = 0; e < 4; ++e) {
          const f32x2 dlt = f32x2{bflo(rv[i][e]), bfhi(rv[i][e])} - o0;
          q2[e] += dlt * dlt;
        }
      const f32x2 qs = (q2[0] + q2[1]) + (q2[2] + q2[3]);
      o1 = row16_sum(qs[0] + qs[1]) * (1.0f / D) + 1e-5f;
    }
    const u64 tag = next_tag(prev);
    const u32x4 o = {__builtin_bit_cast(unsigned, o0), (unsigned)tag, __builtin_bit_cast(unsigned, o1), (unsigned)(tag >> 32)};
    if (r == 0) __builtin_amdgcn_raw_buffer_store_b128(o, rws, (w < 2 ? W_ST + (id.rb * 16 + id.cb) * 128 : W_CSD + (id.cb * 16 + id.rb) * 128) + k8 * 16, 0, AUX_SC1);
  }

  bf16x8 xr[20][2];
  LAS char *fB0, *fB3;
  bf16x8 wb[4][2];
  auto rdg = [&](int g, int m) {
    const int sl = (10 + g / 10) & 3, n = (g % 10) * 2 + m;
    wb[g & 3][m] = sl == 3 ? ldsr(fB3 + n * 1024) : ldsr(fB0 + sl * 20480 + n * 1024);
  };
  f32x4 accP[5][2], st[4];
  float cs[5];
  {
    bf16x8 pb[2][5];
    LAS char *tr, *tc;
    const int oK0 = r * 128 + ((q ^ (r >> 1)) & 7) * 16, oK1 = oK0 ^ 64;
    LAS char* xA[2] = {L + ARING + w * 4096 + oK0, L + ARING + w * 4096 + oK1};
    LAS char* pB[2] = {L + BRING + 10240 + oK0, L + BRING + 10240 + oK1};
    PIN(xA[0]); PIN(xA[1]); PIN(pB[0]); PIN(pB[1]);
    auto rdx = [&](int i, int ks, int t) { xr[2 * i + ks][t] = ldsr(xA[ks] + (i & 3) * 16384 + t * 2048); };
    auto rdw = [&](int i, int ks, int g) { pb[ks][g] = ldsr(pB[ks] + ((i & 3) ? (i & 3) * 20480 - 10240 : 0) + g * 2048); };
#pragma unroll
    for (int i = 0; i < 10; ++i) {
      if (i == DRAIN_G) wait_vm<0>();
      bar();
#pragma unroll
      for (int k = 0; k < 10; ++k) {
        if (i > 0) accP[k >> 1][k & 1] = mfma(xr[2 * i - 1][k & 1], pb[1][k >> 1], accP[k >> 1][k & 1]);
        if (k < 2) rdx(i, 0, k);
        else if (k < 7) rdw(i, 0, k - 2);
        else if (k < 9) rdx(i, 1, k - 7);
        SCHED0();
      }
#pragma unroll
      for (int k = 0; k < 10; ++k) {
        accP[k >> 1][k & 1] = mfma(xr[2 * i][k & 1], pb[0][k >> 1], i == 0 ? z4 : accP[k >> 1][k & 1]);
        if (k < 5) rdw(i, 1, k);
        if (i == 9) {
          if (k == 5) { lane = fresh_lane(); r = lane & 15; q = lane >> 4; PIN(r); PIN(q); }
          if (k == 6) { tr = L + T_ST + (w * 32 + 4 * q) * 4; tc = L + T_CS + r * 4; PIN(tr); PIN(tc); }
          if (k == 7) { fB0 = L + BRING + lane * 16; fB3 = fB0 + 61440; PIN(fB0); PIN(fB3); }
        }
        SCHED0();
      }
    }
    bar();
#pragma unroll
    for (int k = 0; k < 10; ++k) {
      accP[k >> 1][k & 1] = mfma(xr[19][k & 1], pb[1][k >> 1], accP[k >> 1][k & 1]);
      if (k < 6) rdg(k >> 1, k & 1);
      else st[k - 6] = *(LAS f32x4*)(tr + ((k - 6) >> 1) * 512 + (k & 1) * 64);
      if (k == 5) {
#pragma unroll
        for (int g = 0; g < 5; ++g) cs[g] = *(LAS float*)(tc + g * 64);
      }
      SCHED0();
    }
  }
  LAS char *xB0, *xB1, *tG;
  unsigned abBase, hbase;

  f32x4 accH[2][4];
  float gv[8], uv[8], wv[8];
  f32x4 cgv, cuv, ek[2];
  u32x4 ho;
  auto h_ins = [&](int c, int o) __attribute__((always_inline)) {
    if (o < 16) {
      const int x = o >> 1, t = x >> 2, e = x & 3;
      if (o & 1) { uv[x] = accH[c & 1][2 + t][e] + ek[t][0] * cuv[e]; PIN(uv[x]); } else { gv[x] = accH[c & 1][t][e] + ek[t][0] * cgv[e]; PIN(gv[x]); }
    } else if (o < 40) {
      const int x = o & 7, t = x >> 2;
      wv[x] = o < 24 ? gv[x] * gv[x] : o < 32 ? ek[t][1] + ek[t][2] * wv[x] : gv[x] * wv[x];
      PIN(wv[x]);
    } else if (o < 56) {
      const int x = (o - 40) >> 1;
      if (o & 1) { uv[x] *= gv[x]; PIN(uv[x]); } else { wv[x] = __builtin_amdgcn_exp2f(wv[x]); PIN(wv[x]); }
    } else if (o < 64) {
      const int x = o & 7, t = x >> 2;
      wv[x] = wv[x] * ek[t][3] + ek[t][3]; PIN(wv[x]);
    } else if (o < 82) {
      const int x = o < 80 ? (o - 64) >> 1 : o - 72;
      if (o < 80 && !(o & 1)) { wv[x] = __builtin_amdgcn_rcpf(wv[x]); PIN(wv[x]); }
      else if (x >= 2) { uv[x - 2] *= wv[x - 2]; PIN(uv[x - 2]); }
      else if (c < 5) { if (x == 0) cgv = *(LAS f32x4*)(tG + (c + 1) * 64); else cuv = *(LAS f32x4*)(tG + 384 + (c + 1) * 64); }
    } else if (o < 86) {
      unsigned pk = pack2(uv[2 * (o - 82)], uv[2 * (o - 82) + 1]); PIN(pk); ho[o - 82] = pk;
    } else __builtin_amdgcn_raw_buffer_store_b128(ho, rws, hbase + c * 2048, 0, AUX_SC1);
  };
  float av[8];
  auto a_ins = [&](int o) __attribute__((always_inline)) {
    if (o == 0) { abBase = sAb + lane * 16; hbase = sH + lane * 16; PIN(abBase); PIN(hbase); }
    else if (o == 1) { xB0 = L + ARING + w * 4096 + (r * 128 + ((q ^ (r >> 1)) & 7) * 16); PIN(xB0); }
    else if (o == 2) { xB1 = (LAS char*)((size_t)xB0 ^ 64); tG = L + T_CG + q * 16; PIN(xB1); PIN(tG); }
    else if (o < 108) {
      const int gg = (o - 3) / 21, m = (o - 3) % 21, x = m & 7, t = x >> 2, e = x & 3;
      if (m < 8) { av[x] = st[2 + t][e] * cs[gg]; PIN(av[x]); }
      else if (m < 16) { av[x] = accP[gg][t][e] * st[t][e] + av[x]; PIN(av[x]); }
      else if (m < 20) { unsigned pk = pack2(av[2 * (m - 16)], av[2 * (m - 16) + 1]); PIN(pk); ho[m - 16] = pk; }
      else __builtin_amdgcn_raw_buffer_store_b128(ho, rws, abBase + gg * 1024, 0, AUX_SC1);
    } else if (o < 110) ek[o - 108] = *(LAS f32x4*)(L + T_EK + (w * 32 + (o - 108) * 16 + r) * 16);
  };
  const int wm = w >> 1, wn = w & 1;
  LAS char *aBase0, *aBase1, *yBase;
  bf16x8 xa0[4], xa1[4], yb0[5], yb1[5];
  sfor<0, 120>([&](auto gi) __attribute__((always_inline)) {
    constexpr int g = decltype(gi)::v, c = g / 20, s = g % 20;
    if (g == 110) {
      const int l2 = fresh_lane(), r2 = l2 & 15, oK0 = r2 * 128 + (((l2 >> 4) ^ (r2 >> 1)) & 7) * 16;
      aBase0 = L + ARING + wm * 8192 + oK0; aBase1 = L + ARING + wm * 8192 + (oK0 ^ 64); yBase = L + BRING + wn * 5120 + l2 * 16;
    }
    if (g == 37) wait_vm<0>();
    if (g % 10 == 8) bar();
    sfor<0, 4>([&](auto ki) __attribute__((always_inline)) {
      constexpr int k = decltype(ki)::v;
      accH[c & 1][k] = mfma(wb[g & 3][k >> 1], xr[s][k & 1], s == 0 ? z4 : accH[c & 1][k]);
      SCHED0();
      if constexpr (g >= 118) {
        constexpr int n = (g - 118) * 4 + k;
        if constexpr (n < 4) xa0[n] = ldsr(aBase0 + 2 * 16384 + n * 2048); else yb0[n - 4] = ldsr(yBase + 2 * 20480 + (n - 4) * 1024);
      } else if (g % 10 == 8) rdg(g + 2 + (k >> 1), k & 1);
      else if (g % 10 != 7 && !(k & 1)) rdg(g + 3, k >> 1);
      if (c == 0) {
        if (s >= 4 && s < 16 && k == 1) {
          const int m = s - 4;
          xr[12 + (m >> 1)][m & 1] = ldsr(((m & 2) ? xB1 : xB0) + ((6 + (m >> 2)) & 3) * 16384 + (m & 1) * 2048);
        }
        if (s == 19 && k == 1) cgv = *(LAS f32x4*)(tG);
        if (s == 19 && k == 3) cuv = *(LAS f32x4*)(tG + 384);
      }
      sfor<first_op<c == 0>(4 * s + k), first_op<c == 0>(4 * s + k + 1)>([&](auto oi) __attribute__((always_inline)) {
        if constexpr (c == 0) a_ins(decltype(oi)::v); else h_ins(c - 1, decltype(oi)::v);
      });
      SCHED0();
    });
  });

  f32x4 accY[5][4];
  LAS char* const yBase3 = yBase + 61440;
  f32x4 accD[5][2];
  LAS char *hB, *hB2, *dB, *xp, *e1, *e2, *dp;
  unsigned pbase;
  auto y_stage = [&](auto sli, auto h0i) __attribute__((always_inline)) {
    constexpr int sl = decltype(sli)::v, h0 = decltype(h0i)::v;
    LAS char* a0 = aBase0 + sl * 16384; LAS char* a1 = aBase1 + sl * 16384;
    LAS char* b = sl == 3 ? yBase3 : yBase + sl * 20480;
    bar();
    sfor<0, 40>([&](auto ki) __attribute__((always_inline)) {
      constexpr int k = decltype(ki)::v;
      if constexpr (k < 20) accY[k >> 2][k & 3] = mfma(yb1[k >> 2], xa1[k & 3], accY[k >> 2][k & 3]);
      else accY[(k - 20) >> 2][k & 3] = mfma(yb0[(k - 20) >> 2], xa0[k & 3], accY[(k - 20) >> 2][k & 3]);
      SCHED0();
      if constexpr (k < 8 && !(k & 1)) xa0[k >> 1] = ldsr(a0 + (k >> 1) * 2048);
      else if constexpr (k < 18 && !(k & 1)) yb0[(k - 8) >> 1] = ldsr(b + ((k - 8) >> 1) * 1024);
      else if constexpr (k == 18) yb1[0] = ldsr(b + 10240);
      else if constexpr (k >= 19 && k < 31 && k % 3 == 1) yb1[(k - 16) / 3] = ldsr(b + 10240 + ((k - 16) / 3) * 1024);
      else if constexpr (k >= 21 && k < 31 && k % 3 == 0) xa1[(k - 21) / 3] = ldsr(a1 + ((k - 21) / 3) * 2048);
      if constexpr (h0 + k < H_OPS) h_ins(5, h0 + k);
      if constexpr (h0 == 101) {
        if constexpr (k == 31) { lane = fresh_lane(); r = lane & 15; q = lane >> 4; PIN(r); PIN(q); }
        if constexpr (k == 32) { hB = L + ARING + wm * 1024 + wn * 8192 + lane * 16 + wn * 8; hB2 = hB + (8 - 16 * wn); PIN(hB); PIN(hB2); }
        if constexpr (k == 33) { dB = fB0 + wn * 10240; pbase = sP + lane * 16; PIN(dB); PIN(pbase); }
        if constexpr (k == 35) { e1 = fB0 + (w ^ 1) * 5120; e2 = L + ARING + w * 2048 + lane * 16; dp = fB0 + w * 5120; PIN(e1); PIN(e2); PIN(dp); }
        if constexpr (k == 34) o0 = out_off(w * 64 + lane);
        if constexpr (k == 36) o1 = out_off(w * 64 + lane + 512);
        if constexpr (k == 38) { xp = L + X_TILE + (wm * 32 + 8 * (r >> 2) + 4 * wn + (r & 3)) * 160 + q * 8; PIN(xp); }
      }
      SCHED0();
    });
  };
  sfor<0, 20>([&](auto ki) __attribute__((always_inline)) {
    constexpr int k = decltype(ki)::v;
    if constexpr (k == 0) yb0[4] = ldsr(yBase + 2 * 20480 + 4096);
    accY[k >> 2][k & 3] = mfma(yb0[k >> 2], xa0[k & 3], z4);
    if constexpr (k < 4) xa1[k] = ldsr(aBase1 + 2 * 16384 + k * 2048);
    else if constexpr (k < 9) yb1[k - 4] = ldsr(yBase + 2 * 20480 + 10240 + (k - 4) * 1024);
    else { h_ins(5, 2 * (k - 9)); h_ins(5, 2 * (k - 9) + 1); }
    SCHED0();
  });
  y_stage(IC<3>{}, IC<22>{}); y_stage(IC<0>{}, IC<62>{}); y_stage(IC<1>{}, IC<101>{});
#pragma nounroll
  for (int i = 0; i < 3; ++i) {
    y_stage(IC<2>{}, IC<99>{}); y_stage(IC<3>{}, IC<99>{});
    wait_vm<0>();
    y_stage(IC<0>{}, IC<99>{}); y_stage(IC<1>{}, IC<99>{});
  }

  bf16x8 hh[2][2], dw[2][5];
  auto d_rd = [&](LAS char* a, LAS char* a2, LAS char* b, int ks, int m) {
    if (m < 2) hh[ks][m] = cat(*(volatile LAS bf16x4*)((m ? a2 : a) + ks * 4096), *(volatile LAS bf16x4*)((m ? a2 : a) + ks * 4096 + 2048));
    else dw[ks][m - 2] = ldsr(b + ks * 5120 + (m - 2) * 1024);
  };
  auto d_mm = [&](int ks, int k) { accD[k >> 1][k & 1] = mfma(dw[ks][k >> 1], hh[ks][k & 1], accD[k >> 1][k & 1]); };
#pragma unroll
  for (int g = 0; g < 5; ++g) { accD[g][0] = z4; accD[g][1] = z4; }
  auto p_store = [&](int u) {
    u32x4 o[5];
#pragma unroll
    for (int g = 0; g < 5; ++g) {
      const f32x4 a = accY[g][2 * u], b = accY[g][2 * u + 1];
      o[g] = u32x4{pack2(a[0], a[1]), pack2(a[2], a[3]), pack2(b[0], b[1]), pack2(b[2], b[3])};
    }
    if (w != id.kq) {
#pragma unroll
      for (int g = 0; g < 5; ++g) __builtin_amdgcn_raw_buffer_store_b128(o[g], rws, pbase + (u * 5 + g) * 1024, 0, AUX_SC1);
    } else {
#pragma unroll
      for (int g = 0; g < 5; ++g) *(LAS u32x4*)(L + T_BIG + (u * 5 + g) * 1024 + lane * 16) = o[g];
    }
  };
  auto d_second = [&](LAS char* a, LAS char* a2, LAS char* b) {
#pragma unroll
    for (int k = 0; k < 10; ++k) {
      d_mm(0, k);
      if (k < 2) d_rd(a, a2, b, 1, k); else if (k < 4) d_rd(a, a2, b, 1, k + 3);
      SCHED0();
    }
  };
  auto d_stage = [&](int i) {
    const int sl = (i + 2) & 3;
    LAS char* a = hB + sl * 16384; LAS char* a2 = hB2 + sl * 16384; LAS char* b = dB + sl * 20480;
    PIN(a); PIN(a2); PIN(b);
    bar();
#pragma unroll
    for (int k = 0; k < 10; ++k) {
      d_mm(1, k);
      if (k < 7) d_rd(a, a2, b, 0, k); else d_rd(a, a2, b, 1, k - 5);
      SCHED0();
    }
    d_second(a, a2, b);
  };
  {
    LAS char* a = hB + 2 * 16384; LAS char* a2 = hB2 + 2 * 16384; LAS char* b = dB + 2 * 20480;
    bar();
#pragma unroll
    for (int k = 0; k < 20; ++k) {
      accY[k >> 2][k & 3] = mfma(yb1[k >> 2], xa1[k & 3], accY[k >> 2][k & 3]);
      if (k < 7) d_rd(a, a2, b, 0, k); else if (k < 10) d_rd(a, a2, b, 1, k - 5);
      SCHED0();
    }
    d_second(a, a2, b);
  }
  p_store(0);
  p_store(1);
#pragma nounroll
  for (int i = 1; i < 11; ++i) {
    if (i == DRAIN_P - 38) wait_vm<0>();
    d_stage(i);
  }
#pragma unroll
  for (int k = 0; k < 10; ++k) d_mm(1, k);
  bar();
  {
    LAS char* a = hB + 2 * 16384; LAS char* a2 = hB2 + 2 * 16384; LAS char* b = dB + 2 * 20480;
    d_rd(a, a2, b, 0, 1); d_rd(a, a2, b, 1, 1);
#pragma unroll
    for (int g = 4; g >= 0; --g) {
      d_rd(a, a2, b, 0, 2 + g); d_rd(a, a2, b, 1, 2 + g);
      if (g == 3) { d_rd(a, a2, b, 0, 0); d_rd(a, a2, b, 1, 0); }
    }
    SCHED0();
#pragma unroll
    for (int g = 4; g >= 0; --g) {
      d_mm(0, 2 * g + 1); d_mm(1, 2 * g + 1);
      if (g < 4) *(LAS f32x4*)(e1 + (g + 1) * 1024) = accD[g + 1][1];
      SCHED0();
      if (g < 4) { d_mm(0, 2 * g + 2); d_mm(1, 2 * g + 2); }
      if (g == 2 || g == 1) *(LAS f32x4*)(e2 + (g - 1) * 1024) = accD[g + 2][0];
      SCHED0();
    }
    d_mm(0, 0); d_mm(1, 0);
    *(LAS f32x4*)(e1) = accD[0][1];
  }
  bar();
  f32x4 dk[3], ys[3];
#pragma unroll
  for (int g = 0; g < 3; ++g) dk[g] = *(LAS f32x4*)(dp + g * 1024);
#pragma unroll
  for (int g = 0; g < 3; ++g) ys[g] = *(LAS f32x4*)(dp + 61440 + g * 1024);
  const f32x4 own[3] = {accD[0][0], accD[1][0], accD[2][0]};
  finish<3>(dk, own, ys, xp);
}

struct TailAddr { LAS char *y0, *y1, *y3, *yo, *dp, *dq, *xp; };
__device__ __forceinline__ void tail_addr(TailAddr& a, LAS char* L, int t, int lane) {
  const int po = (t >> 1) * 5120 + lane * 16, r = lane & 15;
  a.y0 = L + T_BIG + po; a.y1 = L + BRING + 20480 + po; a.y3 = L + ARING + 16384 + po;
  a.yo = L + BRING + 61440 + t * 5120 + lane * 16;
  a.dp = L + ARING + t * 2048 + lane * 16; a.dq = L + BRING + t * 5120 + 3072 + lane * 16;
  a.xp = L + X_TILE + ((t >> 1) * 32 + 8 * (r >> 2) + 4 * (t & 1) + (r & 3)) * 160 + (lane >> 4) * 8 + 96;
  PIN(a.y0); PIN(a.y1); PIN(a.y3); PIN(a.yo); PIN(a.dp); PIN(a.dq); PIN(a.xp);
}
__device__ __forceinline__ void aux_tail(bf16x8 pickA, const TailAddr& a, const bf16x8 (&own)[5]) {
  bf16x8 y[3][5];
  f32x4 sum[5];
#pragma unroll
  for (int g = 0; g < 5; ++g) {
    y[0][g] = ldsr(a.y1 + g * 1024); y[1][g] = ldsr(a.y1 + 10240 + g * 1024); y[2][g] = ldsr(a.y3 + g * 1024);
    sum[g] = mfma(pickA, own[g], f32x4{0.f, 0.f, 0.f, 0.f});
  }
  wait_vm<3>();
  bar();
#pragma unroll
  for (int k = 0; k < 3; ++k)
#pragma unroll
    for (int g = 0; g < 5; ++g) sum[g] = mfma(pickA, y[k][g], sum[g]);
  SCHED0();
#pragma unroll
  for (int g = 0; g < 3; ++g) *(LAS f32x4*)(a.yo + g * 1024) = sum[g];
  f32x4 ys[2] = {sum[3], sum[4]};
  wait_vm<0>();
  bar();
  f32x4 dk[2], d1[2];
#pragma unroll
  for (int g = 0; g < 2; ++g) { dk[g] = *(LAS f32x4*)(a.dq + g * 1024); d1[g] = *(LAS f32x4*)(a.dp + g * 1024); }
  finish<2>(dk, d1, ys, a.xp);
}

__device__ __forceinline__ void st_flag(unsigned char* ws, unsigned off, u64 v, int lane) {
  if (lane == 0) __hip_atomic_store((GAS u64*)(ws + off), v, __ATOMIC_RELAXED, __HIP_MEMORY_SCOPE_AGENT);
}
__device__ __forceinline__ void wait_flag(const unsigned char* ws, unsigned off, u64 tag, u64 seen) {
  if (!__all(seen == tag)) {
#pragma nounroll
    for (int spin = 0; spin < (1 << 16); ++spin)
      if (__all(ld_flag(ws, off) == tag)) break;
  }
  __builtin_amdgcn_fence(__ATOMIC_ACQUIRE, "wavefront");
}
template <int N, class Fn>
__device__ __forceinline__ void wait_tags(u32x4 (&g)[N], unsigned tlo, unsigned thi, Fn&& reload) {
  auto good = [&]() {
    unsigned bad = 0;
#pragma unroll
    for (int k = 0; k < N; ++k) bad |= (g[k][1] ^ tlo) | (g[k][3] ^ thi);
    return __all(bad == 0);
  };
  if (!good()) {
#pragma nounroll
    for (int spin = 0; spin < (1 << 16); ++spin) {
      asm volatile("" ::: "memory");
      reload();
      if (good()) break;
    }
  }
}
struct First { cptr base; unsigned v0, v1, v2, v3, kc; };
__device__ __forceinline__ void first_offsets(First& f, unsigned w, unsigned lane) {
  const unsigned isx = w < 6, l3 = lane >> 3;
  const unsigned base = (l3 + (isx ? l3 & 4 : 0)) * 1280, c0 = ((lane & 7) ^ (lane >> 4)) * 16;
  f.v0 = base + c0;
  f.v1 = base + (c0 ^ 64) + (isx ? 16 * 1280 - 1024 : 8 * 1280 - 1024);
  f.v2 = f.v0 + (isx ? 4 * 1280 - 2048 : 16 * 1280 - 2048);
  f.v3 = f.v1 + (isx ? 4 * 1280 - 2048 : 16 * 1280 - 2048);
}
#define SPIN(x) asm volatile("" : "+s"(x))
__device__ __forceinline__ void p1_tile(First& f, unsigned lb, int ld, int j) {
  cptr a = f.base + f.kc * 128, a2 = a + 32 * 1280;
  unsigned l = ld < 2 ? lb + ARING + (j & 3) * 16384 + ld * 8192 : lb + BRING + ((j & 3) ? (j & 3) * 20480 : 10240);
  dma4(a, f.v0, f.v1, f.v2, f.v3, l);
  dma4(a2, f.v0, f.v1, f.v2, f.v3, l + 4096);
  if (ld == 2) dma2(a + 64 * 1280, f.v0, f.v1, l + 8192);
  f.kc = f.kc == 9 ? 0 : f.kc + 1;
}
__device__ __forceinline__ cptr lane_ptr(u64 tab, int i) {
  return (cptr)(((u64)(unsigned)__builtin_amdgcn_readlane((int)(tab >> 32), i) << 32) | (unsigned)__builtin_amdgcn_readlane((int)tab, i));
}
__device__ __forceinline__ void loader(const Params& p, unsigned lb, const Ids& id, int ld, int lane, First& f) {
  const int rot = id.rot, ro = id.et + id.tt;
  const int i = lane, j = i + 3;
  const unsigned sl = j & 3, g0 = ld ? 2 * ld - 2 : 4;
  const unsigned lA = lb + ARING + sl * 16384 + 1, lB = lb + BRING + sl * 20480 + 1;
  const u64 ws = (u64)p.ws, bw = (u64)(id.kq < 2 ? p.wsr : p.wsi) + (id.tt * 128 * 4096 + (id.kq & 1) * 2048);
  u64 t0, t1, t2;
  unsigned m0, m1, m2, wc;
  {
    auto r4 = [&](int y) { return (y & ~3) | ((y + ro) & 3); };
    const int d = i == 47 ? 11 : i - 35;
    const unsigned oP = W_P + (unsigned)((id.tt * 4 + id.et) * 4 + id.kq) * 40960u;
    const unsigned oB = i < 35 ? W_ABT + (unsigned)(((id.kq >> 1) * 4 + id.et) * 64 + (id.kq & 1) * 32) * 10240u + r4(i - 19) * 20480
                      : i == 46 ? oP : W_WDT + (unsigned)(id.et * 2 + (id.kq & 1)) * 245760u + r4(d) * 20480;
    const unsigned oH = i == 46 ? oP + 20480 : W_H + (unsigned)(id.tt * 2 + (id.kq >> 1)) * 196608u + r4(d) * 16384;
    const unsigned oG = W_GU + (unsigned)id.cb * 245760u + ((j - 10) >> 1) * 40960, kg = (j & 1) * 5 + g0 + rot;
    const int n = i < 19 ? i - 13 : i < 22 ? 2 * i - 32 : 4 * (i - 19);
    const u64 aW = bw + (unsigned)(r4(n >> 2) * 128 + (n & 3) * 32 * 4096);
    const unsigned lW = lb + ARING + (((n >> 2) + 2) & 3) * 16384 + (n & 3) * 4096 + 1;
    const u64 a1 = (u64)f.base + ((j + rot) % 10) * 128;
    const unsigned l1 = ld < 2 ? lA + ld * 8192 : sl ? lB : lB + 10240;
    t0 = i < 7 ? a1 : ws + (i < 19 ? oG + (kg % 10) * 4096 : oB + g0 * 4096);
    m0 = i < 7 ? l1 : lB + g0 * 4096;
    t1 = i < 7 ? a1 + 32 * 1280 : ld ? ws + (i < 19 ? oG + ((kg + 1) % 10) * 4096 : oB + g0 * 4096 + 4096) : i < 35 ? aW : ws + oH;
    m1 = i < 7 || ld ? m0 + 4096 : i < 13 ? 0 : i < 35 ? lW : lA;
    t2 = i < 7 ? a1 + 64 * 1280 : i < 35 ? aW + (ld + 1) * 32 * 4096 : ws + (oH + (ld + 1) * 4096);
    m2 = i < 7 ? (ld == 2 ? l1 + 8192 : 0) : i < 19 || (i < 22 && ld) || (i == 46 && ld == 2) ? 0 : i < 35 ? lW + (ld + 1) * 4096 : lA + (ld + 1) * 4096;
    wc = i < 8 ? (ld == 2 ? 20 : 16) : i < 15 ? (ld ? 16 : 8) : i < 24 || i == 47 ? 16 : 24;
  }
  unsigned va[4] = {f.v0, f.v1, f.v2, f.v3}, vb[4] = {f.v0, f.v1, f.v2, f.v3}, vc[4] = {f.v0, f.v1, f.v2, f.v3};
  const unsigned c0 = ((lane & 7) ^ (lane >> 4)) * 16, rl = 8 * (lane >> 5) + ((lane >> 3) & 3), vl = lane * 16;
  unsigned vw[4] = {rl * 4096 + c0, (rl + 16) * 4096 + (c0 ^ 64) - 1024, (rl + 4) * 4096 + c0 - 2048, (rl + 20) * 4096 + (c0 ^ 64) - 3072};
  PIN(t0); PIN(t1); PIN(t2); PIN(m0); PIN(m1); PIN(m2); PIN(wc);
  PIN(vw[0]); PIN(vw[1]); PIN(vw[2]); PIN(vw[3]);
  p1_tile(f, lb, ld, 2);
  int k = 0;
#pragma nounroll
  for (int end = 7; end <= 63; end += 28) {
    const int ke = end < 48 ? end : 48;
#pragma nounroll
    do {
      const cptr a0 = lane_ptr(t0, k), a1 = lane_ptr(t1, k), a2 = lane_ptr(t2, k);
      const unsigned l0 = __builtin_amdgcn_readlane(m0, k), l1 = __builtin_amdgcn_readlane(m1, k), l2 = __builtin_amdgcn_readlane(m2, k);
      const unsigned c = __builtin_amdgcn_readlane(wc, k);
      asm volatile(
          "s_waitcnt vmcnt(24)\n\ts_cmp_ge_u32 %[c], 24\n\ts_cbranch_scc1 1f\n\t"
          "s_waitcnt vmcnt(20)\n\ts_cmp_ge_u32 %[c], 20\n\ts_cbranch_scc1 1f\n\t"
          "s_waitcnt vmcnt(16)\n\ts_cmp_ge_u32 %[c], 16\n\ts_cbranch_scc1 1f\n\t"
          "s_waitcnt vmcnt(8)\n"
          "1:\n\ts_sub_u32 m0, %[l0], 1\n\ts_barrier\n\t"
          "global_load_lds_dwordx4 %[x0], %[a0] sc1\n\tglobal_load_lds_dwordx4 %[x1], %[a0] offset:1024 sc1\n\t"
          "global_load_lds_dwordx4 %[x2], %[a0] offset:2048 sc1\n\tglobal_load_lds_dwordx4 %[x3], %[a0] offset:3072 sc1\n\t"
          "s_sub_u32 m0, %[l1], 1\n\ts_cbranch_scc1 2f\n\t"
          "global_load_lds_dwordx4 %[y0], %[a1] sc1\n\tglobal_load_lds_dwordx4 %[y1], %[a1] offset:1024 sc1\n\t"
          "global_load_lds_dwordx4 %[y2], %[a1] offset:2048 sc1\n\tglobal_load_lds_dwordx4 %[y3], %[a1] offset:3072 sc1\n"
          "2:\n\ts_sub_u32 m0, %[l2], 1\n\ts_cbranch_scc1 3f\n\t"
          "global_load_lds_dwordx4 %[z0], %[a2] sc1\n\tglobal_load_lds_dwordx4 %[z1], %[a2] offset:1024 sc1\n\t"
          "s_cmp_eq_u32 %[e], 7\n\ts_cbranch_scc1 3f\n\t"
          "global_load_lds_dwordx4 %[z2], %[a2] offset:2048 sc1\n\tglobal_load_lds_dwordx4 %[z3], %[a2] offset:3072 sc1\n"
          "3:"
          :: [c] "s"(c), [l0] "s"(l0), [l1] "s"(l1), [l2] "s"(l2), [a0] "s"(a0), [a1] "s"(a1), [a2] "s"(a2), [e] "s"(end),
             [x0] "v"(va[0]), [x1] "v"(va[1]), [x2] "v"(va[2]), [x3] "v"(va[3]), [y0] "v"(vb[0]), [y1] "v"(vb[1]), [y2] "v"(vb[2]), [y3] "v"(vb[3]),
             [z0] "v"(vc[0]), [z1] "v"(vc[1]), [z2] "v"(vc[2]), [z3] "v"(vc[3])
          : "memory", "scc");
    } while (++k < ke);
#pragma unroll
    for (int e = 0; e < 4; ++e) { va[e] = vl; vb[e] = end == 7 && ld == 0 ? vw[e] : vl; vc[e] = end == 7 ? vw[e] : vl; }
  }
}

__device__ __forceinline__ void helper(const Params& p, LAS char* L, unsigned lb, const Ids& id, int lane, const Code& code) {
  const int b = blockIdx.x;
  const __amdgpu_buffer_rsrc_t rws = __builtin_amdgcn_make_buffer_rsrc((void*)p.ws, 0, (int)W_END, 0x00020000);
  __builtin_amdgcn_s_setprio(3);
  const u64 prev = ld_flag(p.ws, W_EPOCH);
  const int r8 = lane >> 3;
  const u64 tag = next_tag(prev);
  const unsigned tlo = (unsigned)tag, thi = (unsigned)(tag >> 32);
  const int pcl = (id.cb * 3) >> 1, tile = id.tt * 4 + id.et, c80 = id.et * 2 + (id.kq & 1);
  const int wk = b / 5, wc = b % 5;
  float rsd[2];
  u64 f0 = 0, f1 = 0;
  unsigned fo0 = 0, fo1 = 0;
  u32x4 gr[4], cq[10];
  const int l5 = (lane & 7) < 5 ? lane & 7 : 0, go = r8 * 5 + l5;
  const unsigned gro = W_CSD + ((id.cb * 16 + r8) * 8 + l5) * 16;
  auto ld_gr = [&]() {
    gr[0] = __builtin_amdgcn_raw_buffer_load_b128(rws, gro, 0, AUX_SC1);
    gr[1] = __builtin_amdgcn_raw_buffer_load_b128(rws, gro + 1024, 0, AUX_SC1);
    gr[2] = __builtin_amdgcn_raw_buffer_load_b128(rws, W_ST + (id.rb * 128 + lane) * 16, 0, AUX_SC1);
    gr[3] = __builtin_amdgcn_raw_buffer_load_b128(rws, W_ST + (id.rb * 128 + 64 + lane) * 16, 0, AUX_SC1);
  };
  auto ld_cq = [&]() {
#pragma unroll
    for (int k = 0; k < 10; ++k) cq[k] = __builtin_amdgcn_raw_buffer_load_b128(rws, W_CSP + ((k * 24 + pcl) * 32 + lane) * 16, 0, AUX_SC1);
  };
  sfor<0, 48>([&](auto ic) __attribute__((always_inline)) {
    constexpr int i = decltype(ic)::v;
    if constexpr (i == GB) wait_flag(p.ws, fo0, tag, f0);
    if constexpr (i == 19 || i == 35 || i == 46) wait_flag(p.ws, fo0, tag, f0);
    if constexpr (i == 35) wait_flag(p.ws, fo1, tag, f1);
    bar();
    if constexpr (i == 0) {
      if (b < 240 && lane < 32) {
        LAS float* cp = (LAS float*)(L + T_CP + lane * 4);
        const float u0 = (cp[0] + cp[64]) + (cp[128] + cp[192]), u1 = (cp[32] + cp[96]) + (cp[160] + cp[224]);
        const u32x4 o = {__builtin_bit_cast(unsigned, u0), tlo, __builtin_bit_cast(unsigned, u1), thi};
        __builtin_amdgcn_raw_buffer_store_b128(o, rws, W_CSP + (b * 32 + lane) * 16, 0, AUX_SC1);
      }
    }
    if constexpr (i == GB - 2) ld_gr();
    if constexpr (i == GB) ld_cq();
    if constexpr (i == DRAIN_G) st_flag(p.ws, W_FG + b * FS, tag, lane);
    if constexpr (i == GB - 2) {
      const int l20 = lane < 20 ? lane : 0;
      fo0 = W_FG + ((l20 >> 1) * 24 + pcl + (l20 & 1)) * FS;
      f0 = ld_flag(p.ws, fo0);
    }
    const int rho = (lane & 32) + ((lane >> 2) & 1) * 16 + ((lane >> 3) & 3) * 4 + (lane & 3);
    if constexpr (i == 8) {
      wait_tags<4>(gr, tlo, thi, ld_gr);
      *(LAS unsigned*)(L + T_CS + go * 4) = gr[0][0];
      *(LAS unsigned*)(L + T_CS + (40 + go) * 4) = gr[1][0];
    }
    if constexpr (i == 9) {
#pragma unroll
      for (int h = 0; h < 2; ++h) {
        rsd[h] = __builtin_amdgcn_rsqf(asf(gr[2 + h][2]));
        const float v = id.cb >= 8 ? -rsd[h] : rsd[h];
        *(LAS float*)(L + T_ST + (h * 64 + rho) * 4) = v;
        *(LAS float*)(L + T_ST + 512 + (h * 64 + rho) * 4) = -v * asf(gr[2 + h][0]);
      }
    }
    if constexpr (i == 10) {
#pragma unroll
      for (int h = 0; h < 2; ++h)
        *(LAS f32x4*)(L + T_EK + (h * 64 + rho) * 16) = f32x4{-asf(gr[2 + h][0]), -2.302208198f * rsd[h], -0.1029432397f * rsd[h] * rsd[h] * rsd[h], asf(gr[2 + h][2])};
    }
    if constexpr (i == 19) {
      if (b < 240) {
        const cptr sn = (cptr)p.wdn + (wk * 32 * 1280 + wc * 256);
        const unsigned vn = (lane >> 4) * 1280 + (lane & 15) * 16;
        dma4nt(sn, vn, vn + 4096, vn + 2 * 4096, vn + 3 * 4096, lb + T_BIG);
        dma4nt(sn + 16 * 1280, vn, vn + 4096, vn + 2 * 4096, vn + 3 * 4096, lb + T_BIG + 4096);
      }
    }
    if constexpr (i == 10) wait_tags<10>(cq, tlo, thi, ld_cq);
    if constexpr (i == 11) {
#pragma unroll
      for (int m = 0; m < 2; ++m)
#pragma unroll
        for (int h = 0; h < 2; ++h) {
          const unsigned col = (lane >> 5) * 64 + h * 32 + (lane & 31) - (id.cb & 1) * 32;
          const float v = ((asf(cq[m][2 * h]) + asf(cq[2 + m][2 * h])) + (asf(cq[4 + m][2 * h]) + asf(cq[6 + m][2 * h]))) + asf(cq[8 + m][2 * h]);
          *(LAS float*)(col < 96 ? L + T_CG + (m * 96 + col) * 4 : L + T_PAD) = v;
        }
    }
    if constexpr (i >= 22 && i < 26) {
      if constexpr (i == 22) wait_vm<0>();
      if (b < 240) {
        LAS char* a = L + T_BIG + (4 * (lane >> 4) + ((lane & 15) >> 2)) * 256 + 8 * (lane & 3);
#pragma unroll
        for (int c = (i - 22) * 2; c < (i - 22) * 2 + 2; ++c) {
          const bf16x8 f = cat(ldstr(a + c * 32), ldstr(a + 4096 + c * 32));
          const int eg = wc * 8 + c, cc = eg / 5, g5 = eg - cc * 5;
          __builtin_amdgcn_raw_buffer_store_b128(__builtin_bit_cast(u32x4, f), rws, lane * 16 + W_WDT + ((cc * 48 + wk) * 5 + g5) * 1024, 0, AUX_SC1);
        }
      }
    }
    if constexpr (i == 14) st_flag(p.ws, W_FB + (id.rb * 16 + id.cb) * FS, tag, lane);
    if constexpr (i == 16 || i == 30) code_touch(code, lb, lane);
    if constexpr (i == 17) {
      fo0 = W_FB + ((8 * (id.kq & 1) + (lane & 7)) * 16 + 8 * (id.kq >> 1) + 2 * id.et + ((lane >> 3) & 1)) * FS;
      f0 = ld_flag(p.ws, fo0);
    }
    if constexpr (i == 28) { wait_vm<0>(); st_flag(p.ws, W_FH + b * FS, tag, lane); }
    if constexpr (i == 32) {
      const int hb = (b & 0x39) | ((lane & 3) << 1) | ((lane & 12) << 4);
      fo0 = W_FH + (lane < 48 ? lane * 5 + (c80 * 5) / 8 : hb) * FS;
      fo1 = W_FH + (lane < 48 ? lane * 5 + (c80 * 5 + 4) / 8 : hb) * FS;
      f0 = ld_flag(p.ws, fo0); f1 = ld_flag(p.ws, fo1);
    }
    if constexpr (i == 35) { if (b == 0) st_flag(p.ws, W_EPOCH, tag, lane); }
    if constexpr (i == DRAIN_P) st_flag(p.ws, W_FP + (tile * 4 + id.kq) * FS, tag, lane);
    if constexpr (i == 44) { fo0 = W_FP + (tile * 4 + (lane & 3)) * FS; f0 = ld_flag(p.ws, fo0); }
  });
  __builtin_amdgcn_s_waitcnt(0x0F70);
}

__global__ __launch_bounds__(NTHR, 2) void fused_kernel(Params p) {
  extern __shared__ __attribute__((aligned(16))) char smem[];
  typedef const __attribute__((address_space(4))) u64* kptr;
  const kptr ka = (kptr)__builtin_amdgcn_kernarg_segment_ptr();
  Code code;
  asm volatile(".Lfk_begin:\n\ts_getpc_b64 %0\n\ts_mov_b32 %1, .Lfk_end-.Lfk_begin" : "=s"(code.pc), "=s"(code.len));
  LAS char* L = (LAS char*)smem;
  const unsigned lb = (unsigned)(size_t)L;
  int lane = threadIdx.x & 63;
  const int w = __builtin_amdgcn_readfirstlane(threadIdx.x >> 6);
  const int b = blockIdx.x, g = b & 7, rr = b >> 3;
  Ids id;
  id.rb = (g & 1) * 8 + (rr & 7);
  id.cb = (g >> 1) * 4 + (rr >> 3);
  id.tt = (g & 1) * 8 + (rr & 7);
  id.et = rr >> 3;
  id.kq = g >> 1;
  id.rot = 3 * (rr >> 3) + (rr & 7);
  First f;
  {
    if (w == 7) code_touch(code, lb, lane);
    f.kc = id.rot >= 10 ? id.rot - 10 : id.rot;
    first_offsets(f, w, lane);
    const unsigned pr = ((unsigned)b * 171u) >> 13, pc48 = b - pr * 48, pcm = pc48 < 24 ? pc48 : pc48 - 24;
    unsigned va = (lane >> 3) * 3072 + (lane & 7) * 16;
    unsigned o0 = (pr * 128 + w * 32) * 3072 + pcm * 128, o1 = (unsigned)(id.rb * 128 + (w & 1) * 64) * 1280u, o2 = (unsigned)(id.cb & 7) * (80u * 1280u);
    SPIN(o0); SPIN(o1); SPIN(o2);
    const unsigned off = w < 4 ? o0 : w < 6 ? o1 : o2;
    const unsigned sel = w < 4 ? (pc48 < 24 ? 5 : 6) : w < 6 ? 0 : 3 + (id.cb >> 3);
    f.base = (cptr)ka[sel] + off;
    if (w >= 4) {
      if (w < 7) {
        p1_tile(f, lb, w - 4, 0);
        p1_tile(f, lb, w - 4, 1);
      }
    } else if (b < 240) dma4nt(f.base, va, va + 23552, va + 2 * 23552, va + 3 * 23552, lb + BRING + (w ? w * 20480 + 10240 : 0));
  }
  unsigned o0, o1;
  char* ob = (char*)p.out + ((id.tt * 128 + (id.kq >> 1) * 64) * 1280 + (id.et * 160 + (id.kq & 1) * 80) * 2);
  SPIN(ob);
  if (w >= 4) {
    const int ld = w - 4;
    lane = fresh_lane();
    first_offsets(f, w, lane);
    const cptr so = uni((cptr)p.x + ((id.tt * 128 + (id.kq >> 1) * 64) * 1280 + (id.et * 2 + (id.kq & 1)) * 160));
    const int n0 = ld * 64 + lane, n1 = n0 + 256, n2 = ld < 2 ? n0 + 512 : n0;
    unsigned x0 = (n0 / 10) * 1120 + n0 * 16, x1 = (n1 / 10) * 1120 + n1 * 16, x2 = (n2 / 10) * 1120 + n2 * 16;
    const unsigned one = (0x3f80u << ((lane & 1) * 16)) & -(unsigned)(((lane & 15) >> 2) == (lane >> 4));
    const int od = (ld & 1) * 2 + ((lane >> 1) & 1);
    u32x4 pa = {one & -(unsigned)(od == 0), one & -(unsigned)(od == 1), one & -(unsigned)(od == 2), one & -(unsigned)(od == 3)};
    PIN(x0); PIN(x1); PIN(x2); PIN(pa);
    TailAddr ta;
    tail_addr(ta, L, ld, lane);
    if (ld < 3) loader(p, lb, id, ld, lane, f);
    else helper(p, L, lb, id, lane, code);
    __builtin_amdgcn_s_setprio(0);
    bf16x8 own[5];
#pragma unroll
    for (int g5 = 0; g5 < 5; ++g5) own[g5] = ldsr(ta.y0 + g5 * 1024);
    wait_vm<12>();
    bar();
    dma1(so, x0, lb + X_TILE + ld * 1024); dma1(so, x1, lb + X_TILE + (ld + 4) * 1024); dma1(so, x2, lb + X_TILE + (ld < 2 ? ld + 8 : ld) * 1024);
    aux_tail(__builtin_bit_cast(bf16x8, pa), ta, own);
    o0 = x1; o1 = 0;
  } else compute(p, L, id, w, o0, o1);
  const int tid = w * 64 + fresh_lane();
  const __amdgpu_buffer_rsrc_t ro = __builtin_amdgcn_make_buffer_rsrc(ob, 0, 63 * 1280 + 160, 0x00020000);
  bar();
  const u32x4 v0 = *(LAS u32x4*)(L + X_TILE + tid * 16);
  if (w < 2) {
    const u32x4 v1 = *(LAS u32x4*)(L + X_TILE + 8192 + tid * 16);
    __builtin_amdgcn_raw_buffer_store_b128(v1, ro, o1, 0, AUX_SC1);
  }
  __builtin_amdgcn_raw_buffer_store_b128(v0, ro, o0, 0, AUX_SC1);
  asm volatile(".Lfk_end:");
}
}

extern "C" void kernel_launch(void* const* d_in, const int* in_sizes, int n_in, void* d_out, int out_size, void* d_ws, size_t ws_size,
                              hipStream_t stream) {
  Params p;
  p.x = (const u16*)d_in[0];
  p.wsr = (const u16*)d_in[1];
  p.wsi = (const u16*)d_in[2];
  p.wdr = (const u16*)d_in[3];
  p.wdi = (const u16*)d_in[4];
  p.wg = (const u16*)d_in[6];
  p.wu = (const u16*)d_in[7];
  p.wdn = (const u16*)d_in[8];
  p.out = (u16*)d_out;
  p.ws = (unsigned char*)d_ws;
  (void)hipFuncSetAttribute((const void*)fused_kernel, hipFuncAttributeMaxDynamicSharedMemorySize, LDS_BYTES);
  fused_kernel<<<256, NTHR, LDS_BYTES, stream>>>(p);
}
```
